```python
import torch

def _softmax(x, axis=-1):
    return torch.softmax(x, dim=axis)
def _gelu(x):
    return torch.nn.functional.gelu(x, approximate="tanh")
def _silu(x):
    return torch.nn.functional.silu(x)
def _sigmoid(x):
    return torch.sigmoid(x)
def _layernorm(x, axis=-1, eps=1e-5):
    m = x.mean(dim=axis, keepdim=True)
    v = x.var(dim=axis, keepdim=True, unbiased=False)
    return (x - m) / torch.sqrt(v + eps)
def _scatter_add(tgt, idx, src, axis=0):
    return tgt.clone().index_add_(axis, idx, src)
def _segment_sum(src, seg, n):
    out = torch.zeros((n,) + src.shape[1:], dtype=src.dtype)
    return out.index_add_(0, seg, src)
def _pad(x, pw):
    flat = tuple(v for pair in reversed(pw) for v in pair)
    return torch.nn.functional.pad(x, flat)

kernel_name = 'standalone__mha_block_cached__near_ridge'

def setup_inputs():

    _w = (lambda a: (lambda t: t.to(torch.bfloat16) if t.is_floating_point() else t)(torch.as_tensor(a)))
    B, Sq, D = 16, 160, 448
    H, Sk, Dh = 16, 512, 256
    return {
        "x": _w((lambda *s: torch.randn(*s, dtype=torch.bfloat16) * 0.8)(B, Sq, D)),
        "k_cache": _w((lambda *s: torch.randn(*s, dtype=torch.bfloat16) * 0.8)(B, H, Sk, Dh)),
        "v_cache": _w((lambda *s: torch.randn(*s, dtype=torch.bfloat16) * 0.05)(B, H, Sk, Dh)),
        "wq": _w((lambda *s: torch.randn(*s, dtype=torch.bfloat16) * 0.8)(D, H * Dh) * (D ** -0.5)),
        "wo": _w((lambda *s: torch.randn(*s, dtype=torch.bfloat16) * 0.05)(H * Dh, D) * ((H * Dh) ** -0.5)),
    }

def reference(x, k_cache, v_cache, wq, wo):
    B, Sq, D = x.shape
    H, Dh = 16, 256
    q = (lambda x, *d: x.permute(*d))((x @ wq).reshape(B, Sq, H, Dh), 0, 2, 1, 3)
    scores = torch.einsum("bhqd,bhkd->bhqk", q, k_cache) * (Dh ** -0.5)
    p = _softmax(scores, axis=-1)
    o = torch.einsum("bhqk,bhkd->bhqd", p, v_cache)
    return (lambda x, *d: x.permute(*d))(o, 0, 2, 1, 3).reshape(B, Sq, H * Dh) @ wo
```

```cpp
#include <hip/hip_runtime.h>
#include <hip/hip_bf16.h>
typedef unsigned short u16;
__device__ __forceinline__ float bf2f(u16 v){ return __uint_as_float(((unsigned)v)<<16); }
__device__ __forceinline__ u16 f2bf(float f){ unsigned u=__float_as_uint(f); u += 0x7FFF + ((u>>16)&1); return (u16)(u>>16); }
constexpr int B=16,SQ=160,D=448,H=16,SK=512,DH=256;
__global__ void gemm_naive(const u16* A, const u16* Bm, u16* C, int M, int N, int K){
  int n = blockIdx.x*blockDim.x+threadIdx.x; int m = blockIdx.y;
  if(n>=N) return;
  float acc=0.f;
  for(int k=0;k<K;k++) acc += bf2f(A[(size_t)m*K+k])*bf2f(Bm[(size_t)k*N+n]);
  C[(size_t)m*N+n]=f2bf(acc);
}
__global__ void attn_naive(const u16* Q, const u16* Kc, const u16* Vc, u16* O){
  int q = blockIdx.x, h = blockIdx.y, b = blockIdx.z; int t = threadIdx.x;
  __shared__ float qs[DH]; __shared__ float s[SK]; __shared__ float red[256];
  qs[t] = bf2f(Q[((size_t)(b*SQ+q))*(H*DH) + h*DH + t]);
  __syncthreads();
  const u16* Kp = Kc + ((size_t)(b*H+h))*SK*DH; const u16* Vp = Vc + ((size_t)(b*H+h))*SK*DH;
  float mx=-1e30f;
  for(int k=t;k<SK;k+=256){ float a=0; for(int d=0;d<DH;d++) a+=qs[d]*bf2f(Kp[(size_t)k*DH+d]); a*=0.0625f; s[k]=a; mx=fmaxf(mx,a);}
  red[t]=mx; __syncthreads();
  for(int o=128;o>0;o>>=1){ if(t<o) red[t]=fmaxf(red[t],red[t+o]); __syncthreads(); }
  mx=red[0]; __syncthreads();
  float sm=0; for(int k=t;k<SK;k+=256){ float e=__expf(s[k]-mx); s[k]=e; sm+=e; }
  red[t]=sm; __syncthreads();
  for(int o=128;o>0;o>>=1){ if(t<o) red[t]+=red[t+o]; __syncthreads(); }
  sm=red[0];
  float a=0; for(int k=0;k<SK;k++) a+=s[k]*bf2f(Vp[(size_t)k*DH+t]);
  O[((size_t)(b*SQ+q))*(H*DH) + h*DH + t]=f2bf(a/sm);
}
extern "C" void kernel_launch(void* const* d_in, const int* in_sizes, int n_in, void* d_out, int out_size, void* d_ws, size_t ws_size, hipStream_t stream){
  const u16* x=(const u16*)d_in[0]; const u16* kc=(const u16*)d_in[1]; const u16* vc=(const u16*)d_in[2]; const u16* wq=(const u16*)d_in[3]; const u16* wo=(const u16*)d_in[4];
  u16* Qw=(u16*)d_ws; u16* Ow=Qw+(size_t)B*SQ*H*DH;
  gemm_naive<<<dim3(H*DH/256,B*SQ),256,0,stream>>>(x,wq,Qw,B*SQ,H*DH,D);
  attn_naive<<<dim3(SQ,H,B),256,0,stream>>>(Qw,kc,vc,Ow);
  gemm_naive<<<dim3(2,B*SQ),256,0,stream>>>(Ow,wo,(u16*)d_out,B*SQ,D,H*DH);
}
```

```cpp
#include <hip/hip_runtime.h>

typedef unsigned u32x4 __attribute__((ext_vector_type(4)));
typedef float f32x2 __attribute__((ext_vector_type(2)));
typedef __bf16 bf16x2 __attribute__((ext_vector_type(2)));

__global__ __launch_bounds__(256, 1) void mha_main(const void* x, const void* k, const void* v, const void* wq, const void* wo, void* ws, void* out) {
  __shared__ __attribute__((aligned(16))) char lds[163840];
  asm volatile(
R"(.macro Mvv d,a,b
v_mfma_f32_16x16x32_bf16 v[\d:\d+3], a[\a:\a+3], v[\b:\b+3], v[\d:\d+3]
.endm
.macro Mvvz d,a,b
v_mfma_f32_16x16x32_bf16 v[\d:\d+3], a[\a:\a+3], v[\b:\b+3], 0
.endm
.macro Mva d,a,b
v_mfma_f32_16x16x32_bf16 v[\d:\d+3], a[\a:\a+3], a[\b:\b+3], v[\d:\d+3]
.endm
.macro Mvaz d,a,b
v_mfma_f32_16x16x32_bf16 v[\d:\d+3], a[\a:\a+3], a[\b:\b+3], 0
.endm
.macro Mav d,a,b
v_mfma_f32_16x16x32_bf16 a[\d:\d+3], a[\a:\a+3], v[\b:\b+3], a[\d:\d+3]
.endm
.macro Mavz d,a,b
)"
R"(v_mfma_f32_16x16x32_bf16 a[\d:\d+3], a[\a:\a+3], v[\b:\b+3], 0
.endm
.macro Maa d,a,b
v_mfma_f32_16x16x32_bf16 a[\d:\d+3], a[\a:\a+3], a[\b:\b+3], a[\d:\d+3]
.endm
.macro Maaz d,a,b
v_mfma_f32_16x16x32_bf16 a[\d:\d+3], a[\a:\a+3], a[\b:\b+3], 0
.endm
.macro T d,v,o
ds_read_b64_tr_b16 a[\d:\d+1], v\v offset:\o
.endm
.macro N d,v,o
ds_read_b128 a[\d:\d+3], v\v offset:\o
.endm
.macro W n
s_waitcnt lgkmcnt(\n)
)"
R"(.endm
.macro C d,a,b
v_cvt_pk_bf16_f32 v\d, v\a, v\b
.endm
.macro E d
v_exp_f32 v\d, v\d
.endm
.macro F d,c
v_fma_f32 v\d, v\d, s63, v\c
.endm
.macro R d,a
v_accvgpr_read_b32 v\d, a\a
.endm
.macro Z n
s_add_u32 m0, s62, \n
.endm
)"
R"(.Lbegin:
s_load_dwordx2 s[8:9], %0, 0
s_load_dwordx2 s[12:13], %0, 24
s_load_dwordx2 s[16:17], %0, 8
s_load_dwordx2 s[20:21], %0, 16
s_load_dwordx2 s[24:25], %0, 32
s_load_dwordx4 s[4:7], %0, 40
v_lshrrev_b32 v1, 6, v255
v_and_b32 v0, 63, v255
v_readfirstlane_b32 s76, v1
s_bfe_u32 s84, %1, 196613
s_bfe_u32 s85, %1, 131075
s_bfe_u32 s77, %1, 65536
s_bfe_u32 s78, %1, 131073
s_lshl3_add_u32 s77, s77, s84
s_lshl2_add_u32 s78, s78, s85
)"
R"(s_lshl4_add_u32 s86, s77, s78
s_cmp_lt_u32 s76, 2
s_cbranch_scc1 .Lheavy
s_add_u32 s64, s84, s85
s_and_b32 s64, s64, 3
s_lshl_b32 s64, s64, 1
s_lshl_b32 s34, s64, 7
s_lshl_b32 s41, s64, 19
s_and_b32 s80, s76, 1
s_lshl_b32 s62, s80, 10
s_lshl_b32 s87, s80, 3
v_lshrrev_b32 v2, 3, v0
v_add_u32 v2, s87, v2
v_bfe_u32 v3, v2, 1, 1
v_bfe_u32 v4, v2, 3, 1
v_lshl_or_b32 v3, v4, 1, v3
)"
R"(v_bfe_u32 v5, v2, 2, 1
v_xor_b32 v4, v4, v5
v_lshl_or_b32 v3, v4, 2, v3
v_and_b32 v4, 7, v0
v_xor_b32 v3, v3, v4
v_lshlrev_b32 v3, 4, v3
v_mul_u32_u24 v230, 896, v2
v_add_u32 v230, v230, v3
v_lshl_add_u32 v226, v2, 9, v3
W 0
s_mul_i32 s80, s77, 143360
s_add_u32 s8, s8, s80
s_addc_u32 s9, s9, 0
s_and_b32 s9, s9, 0xffff
s_mov_b32 s10, 143360
s_mov_b32 s11, 0x20000
)"
R"(Z 32768
s_nop 0
buffer_load_dwordx4 v230, s[8:11], s34 offen lds
Z 34816
s_add_u32 s64, s34, 14336
buffer_load_dwordx4 v230, s[8:11], s64 offen lds
Z 36864
s_add_u32 s64, s34, 28672
buffer_load_dwordx4 v230, s[8:11], s64 offen lds
Z 38912
s_add_u32 s64, s34, 43008
buffer_load_dwordx4 v230, s[8:11], s64 offen lds
Z 40960
s_add_u32 s64, s34, 57344
buffer_load_dwordx4 v230, s[8:11], s64 offen lds
Z 43008
)"
R"(s_add_u32 s64, s34, 71680
buffer_load_dwordx4 v230, s[8:11], s64 offen lds
Z 45056
s_add_u32 s64, s34, 86016
buffer_load_dwordx4 v230, s[8:11], s64 offen lds
Z 47104
s_add_u32 s64, s34, 100352
buffer_load_dwordx4 v230, s[8:11], s64 offen lds
Z 49152
s_add_u32 s64, s34, 114688
buffer_load_dwordx4 v230, s[8:11], s64 offen lds
Z 51200
s_add_u32 s64, s34, 129024
buffer_load_dwordx4 v230, s[8:11], s64 offen lds
s_mul_i32 s80, s78, 512
s_add_u32 s12, s12, s80
)"
R"(s_addc_u32 s13, s13, 0
s_and_b32 s13, s13, 0xffff
s_mov_b32 s14, 3662336
s_mov_b32 s15, 0x20000
v_bfe_u32 v2, v0, 2, 3
v_bfe_u32 v3, v0, 4, 1
v_lshlrev_b32 v4, 1, v3
v_and_b32 v5, 3, v0
v_xor_b32 v4, v4, v5
v_lshlrev_b32 v4, 4, v4
v_lshrrev_b32 v5, 5, v0
v_lshl_add_u32 v4, v5, 6, v4
v_and_b32 v5, 3, v2
v_lshl_add_u32 v5, v3, 4, v5
v_add_u32 v5, s87, v5
v_lshl_add_u32 v228, v5, 13, v4
)"
R"(v_add_u32 v2, s87, v2
v_lshl_add_u32 v227, v2, 9, v4
v_mul_u32_u24 v229, 896, v2
v_add_u32 v229, v229, v4
Z 0
s_nop 0
buffer_load_dwordx4 v228, s[12:15], s41 offen lds
Z 8064
s_nop 0
buffer_load_dwordx4 v228, s[12:15], s41 offen offset:128 lds
Z 16128
s_nop 0
buffer_load_dwordx4 v228, s[12:15], s41 offen offset:256 lds
Z 24192
s_nop 0
buffer_load_dwordx4 v228, s[12:15], s41 offen offset:384 lds
)"
R"(Z 2048
s_add_u32 s64, s41, 32768
buffer_load_dwordx4 v228, s[12:15], s64 offen lds
Z 10112
s_nop 0
buffer_load_dwordx4 v228, s[12:15], s64 offen offset:128 lds
Z 18176
s_nop 0
buffer_load_dwordx4 v228, s[12:15], s64 offen offset:256 lds
Z 26240
s_nop 0
buffer_load_dwordx4 v228, s[12:15], s64 offen offset:384 lds
Z 4096
s_add_u32 s64, s41, 262144
buffer_load_dwordx4 v228, s[12:15], s64 offen lds
Z 12160
)"
R"(s_nop 0
buffer_load_dwordx4 v228, s[12:15], s64 offen offset:128 lds
Z 20224
s_nop 0
buffer_load_dwordx4 v228, s[12:15], s64 offen offset:256 lds
Z 28288
s_nop 0
buffer_load_dwordx4 v228, s[12:15], s64 offen offset:384 lds
Z 6144
s_add_u32 s64, s41, 294912
buffer_load_dwordx4 v228, s[12:15], s64 offen lds
Z 14208
s_nop 0
buffer_load_dwordx4 v228, s[12:15], s64 offen offset:128 lds
Z 22272
s_nop 0
)"
R"(buffer_load_dwordx4 v228, s[12:15], s64 offen offset:256 lds
Z 30336
s_nop 0
buffer_load_dwordx4 v228, s[12:15], s64 offen offset:384 lds
s_cmp_ge_u32 s84, 7
s_cselect_b32 s79, 0, s84
s_mul_i32 s80, s86, 262144
s_add_u32 s16, s16, s80
s_addc_u32 s17, s17, 0
s_and_b32 s17, s17, 0xffff
s_mov_b32 s18, 262144
s_mov_b32 s19, 0x20000
s_mul_i32 s80, s86, 262144
s_add_u32 s20, s20, s80
s_addc_u32 s21, s21, 0
s_and_b32 s21, s21, 0xffff
)"
R"(s_mov_b32 s22, 262144
s_mov_b32 s23, 0x20000
s_mul_i32 s80, s78, 229376
s_add_u32 s24, s24, s80
s_addc_u32 s25, s25, 0
s_and_b32 s25, s25, 0xffff
s_mov_b32 s26, 229376
s_mov_b32 s27, 0x20000
s_add_u32 s35, s34, 128
s_cmp_eq_u32 s35, 896
s_cselect_b32 s35, 0, s35
s_lshl_b32 s42, s35, 12
s_add_u32 s36, s35, 128
s_cmp_eq_u32 s36, 896
s_cselect_b32 s36, 0, s36
s_lshl_b32 s43, s36, 12
)"
R"(s_add_u32 s37, s36, 128
s_cmp_eq_u32 s37, 896
s_cselect_b32 s37, 0, s37
s_lshl_b32 s44, s37, 12
s_add_u32 s38, s37, 128
s_cmp_eq_u32 s38, 896
s_cselect_b32 s38, 0, s38
s_lshl_b32 s45, s38, 12
s_add_u32 s39, s38, 128
s_cmp_eq_u32 s39, 896
s_cselect_b32 s39, 0, s39
s_lshl_b32 s46, s39, 12
s_add_u32 s40, s39, 128
s_cmp_eq_u32 s40, 896
s_cselect_b32 s40, 0, s40
s_lshl_b32 s47, s40, 12
)"
R"(s_lshl_b32 s48, s79, 7
s_add_u32 s49, s48, 128
s_cmp_eq_u32 s49, 896
s_cselect_b32 s49, 0, s49
s_add_u32 s50, s49, 128
s_cmp_eq_u32 s50, 896
s_cselect_b32 s50, 0, s50
s_add_u32 s51, s50, 128
s_cmp_eq_u32 s51, 896
s_cselect_b32 s51, 0, s51
s_add_u32 s52, s51, 128
s_cmp_eq_u32 s52, 896
s_cselect_b32 s52, 0, s52
s_add_u32 s53, s52, 128
s_cmp_eq_u32 s53, 896
s_cselect_b32 s53, 0, s53
)"
R"(s_add_u32 s54, s53, 128
s_cmp_eq_u32 s54, 896
s_cselect_b32 s54, 0, s54
s_mul_i32 s80, s86, 143360
s_add_u32 s28, s4, s80
s_addc_u32 s29, s5, 0
s_and_b32 s29, s29, 0xffff
s_mov_b32 s30, 143360
s_mov_b32 s31, 0x20000
s_mov_b32 s55, 0
s_mov_b32 s56, 20480
s_mov_b32 s57, 40960
s_mov_b32 s58, 61440
s_mov_b32 s59, 81920
s_mov_b32 s60, 102400
s_mov_b32 s61, 122880
)"
R"(s_mov_b32 s63, 0x3db8aa3b
s_mov_b32 s65, 0x42b17218
s_and_b32 s81, s76, 1
s_lshl1_add_u32 s82, s81, 6
s_mul_i32 s83, s81, 3
s_add_u32 s83, s83, 2
v_lshrrev_b32 v2, 5, v0
v_lshlrev_b32 v2, 10, v2
v_bfe_u32 v3, v0, 2, 3
v_lshl_add_u32 v2, v3, 6, v2
v_and_b32 v3, 3, v0
v_lshl_add_u32 v2, v3, 3, v2
v_bfe_u32 v3, v0, 4, 1
v_lshl_add_u32 v192, v3, 5, v2
v_xor_b32 v193, 32, v192
v_and_b32 v2, 15, v0
)"
R"(v_bfe_u32 v4, v2, 1, 1
v_bfe_u32 v5, v2, 3, 1
v_lshl_or_b32 v4, v5, 1, v4
v_bfe_u32 v6, v2, 2, 1
v_xor_b32 v5, v5, v6
v_lshl_or_b32 v4, v5, 2, v4
v_lshrrev_b32 v5, 5, v0
v_lshl_or_b32 v5, v3, 1, v5
v_xor_b32 v4, v4, v5
v_lshlrev_b32 v4, 4, v4
v_lshl_add_u32 v198, v2, 7, v4
v_xor_b32 v199, 64, v198
v_add_u32 v194, 65536, v192
v_add_u32 v200, 65536, v198
v_add_u32 v195, 65536, v193
v_add_u32 v201, 65536, v199
)"
R"(v_add_u32 v196, 131072, v192
v_add_u32 v202, 131072, v198
v_add_u32 v197, 131072, v193
v_add_u32 v203, 131072, v199
v_add_u32 v2, 32768, v198
v_lshl_add_u32 v204, s82, 11, v2
v_lshl_add_u32 v206, s83, 11, v2
v_add_u32 v2, 32768, v199
v_lshl_add_u32 v205, s82, 11, v2
v_lshl_add_u32 v207, s83, 11, v2
v_lshlrev_b32 v2, 4, v0
s_mul_i32 s80, s82, 2048
v_add_u32 v223, s80, v2
v_add_u32 v224, 2048, v223
s_mul_i32 s80, s83, 2048
v_add_u32 v225, s80, v2
)"
R"(v_lshl_add_u32 v231, s81, 12, v2
v_add_u32 v231, 57344, v231
v_lshl_add_u32 v232, s81, 14, v2
v_add_u32 v232, 98304, v232
v_add_u32 v220, s62, v2
v_add_u32 v221, 0x20000, v220
s_lshl4_add_u32 s94, s78, s82
s_lshl_b32 s94, s94, 6
s_add_u32 s94, s94, 1024
s_mov_b32 s80, 0x3f803f80
v_accvgpr_write_b32 a204, s80
v_accvgpr_write_b32 a205, s80
v_accvgpr_write_b32 a206, s80
v_accvgpr_write_b32 a207, s80
s_mul_i32 s80, s77, 32768
s_add_u32 s80, s80, 50331648
)"
R"(s_add_u32 s72, s4, s80
s_addc_u32 s73, s5, 0
s_and_b32 s73, s73, 0xffff
s_mov_b32 s74, 17408
s_mov_b32 s75, 0x20000
v_lshlrev_b32 v238, 2, v0
s_cmp_eq_u32 s76, 3
s_cselect_b32 s80, s78, 64
v_cmp_eq_u32 vcc, s80, v0
v_mov_b32 v2, 0x100000
s_nop 0
v_cndmask_b32 v252, v2, v238, vcc
buffer_load_dword v233, v238, s[72:75], 0 offen sc1
Z 53248
s_nop 0
buffer_load_dwordx4 v230, s[8:11], s35 offen lds
)"
R"(Z 55296
s_add_u32 s64, s35, 14336
buffer_load_dwordx4 v230, s[8:11], s64 offen lds
Z 57344
s_add_u32 s64, s35, 28672
buffer_load_dwordx4 v230, s[8:11], s64 offen lds
Z 59392
s_add_u32 s64, s35, 43008
buffer_load_dwordx4 v230, s[8:11], s64 offen lds
Z 61440
s_add_u32 s64, s35, 57344
buffer_load_dwordx4 v230, s[8:11], s64 offen lds
Z 63488
s_add_u32 s64, s35, 71680
buffer_load_dwordx4 v230, s[8:11], s64 offen lds
Z 65536
)"
R"(s_add_u32 s64, s35, 86016
buffer_load_dwordx4 v230, s[8:11], s64 offen lds
Z 67584
s_add_u32 s64, s35, 100352
buffer_load_dwordx4 v230, s[8:11], s64 offen lds
Z 69632
s_add_u32 s64, s35, 114688
buffer_load_dwordx4 v230, s[8:11], s64 offen lds
Z 71680
s_add_u32 s64, s35, 129024
buffer_load_dwordx4 v230, s[8:11], s64 offen lds
Z 98304
s_nop 0
buffer_load_dwordx4 v228, s[12:15], s42 offen lds
Z 106368
s_nop 0
)"
R"(buffer_load_dwordx4 v228, s[12:15], s42 offen offset:128 lds
Z 114432
s_nop 0
buffer_load_dwordx4 v228, s[12:15], s42 offen offset:256 lds
Z 122496
s_nop 0
buffer_load_dwordx4 v228, s[12:15], s42 offen offset:384 lds
Z 100352
s_add_u32 s64, s42, 32768
buffer_load_dwordx4 v228, s[12:15], s64 offen lds
Z 108416
s_nop 0
buffer_load_dwordx4 v228, s[12:15], s64 offen offset:128 lds
Z 116480
s_nop 0
buffer_load_dwordx4 v228, s[12:15], s64 offen offset:256 lds
)"
R"(Z 124544
s_nop 0
buffer_load_dwordx4 v228, s[12:15], s64 offen offset:384 lds
s_waitcnt vmcnt(19) lgkmcnt(0)
s_barrier
N 0,204,0
N 4,204,2048
N 12,205,0
N 16,205,2048
T 208,192,0
T 210,192,2048
W 0
T 212,192,4096
T 214,192,6144
T 216,193,0
T 218,193,2048
)"
R"(T 220,193,4096
T 222,193,6144
T 224,192,512
T 226,192,2560
Mvaz 0,208,0
W 6
T 228,192,4608
T 230,192,6656
Mva 0,212,12
Z 102400
T 232,193,512
T 234,193,2560
Mvaz 8,208,4
s_add_u32 s64, s42, 262144
buffer_load_dwordx4 v228, s[12:15], s64 offen lds
Mva 8,212,16
)"
R"(W 8
Z 110464
T 236,193,4608
T 238,193,6656
Mvaz 4,216,0
W 8
buffer_load_dwordx4 v228, s[12:15], s64 offen offset:128 lds
Mva 4,220,12
Z 118528
T 240,192,8192
T 242,192,10240
Mvaz 12,216,4
buffer_load_dwordx4 v228, s[12:15], s64 offen offset:256 lds
Mva 12,220,16
W 8
Z 126592
)"
R"(T 244,192,12288
T 246,192,14336
Mvaz 24,224,0
W 8
buffer_load_dwordx4 v228, s[12:15], s64 offen offset:384 lds
Mva 24,228,12
Z 104448
T 248,193,8192
T 250,193,10240
Mvaz 32,224,4
s_add_u32 s64, s42, 294912
buffer_load_dwordx4 v228, s[12:15], s64 offen lds
Mva 32,228,16
W 8
Z 112512
T 252,193,12288
)"
R"(T 254,193,14336
Mvaz 28,232,0
W 8
buffer_load_dwordx4 v228, s[12:15], s64 offen offset:128 lds
Mva 28,236,12
Z 120576
T 208,192,8704
T 210,192,10752
Mvaz 36,232,4
buffer_load_dwordx4 v228, s[12:15], s64 offen offset:256 lds
Mva 36,236,16
W 8
Z 128640
T 212,192,12800
T 214,192,14848
Mvaz 48,240,0
)"
R"(W 8
buffer_load_dwordx4 v228, s[12:15], s64 offen offset:384 lds
Mva 48,244,12
Z 73728
T 216,193,8704
T 218,193,10752
Mvaz 56,240,4
buffer_load_dwordx4 v230, s[8:11], s36 offen lds
Mva 56,244,16
W 8
Z 75776
T 220,193,12800
T 222,193,14848
Mvaz 52,248,0
W 8
s_add_u32 s64, s36, 14336
)"
R"(buffer_load_dwordx4 v230, s[8:11], s64 offen lds
Mva 52,252,12
Z 77824
T 224,192,16384
T 226,192,18432
Mvaz 60,248,4
s_add_u32 s64, s36, 28672
buffer_load_dwordx4 v230, s[8:11], s64 offen lds
Mva 60,252,16
W 8
Z 79872
T 228,192,20480
T 230,192,22528
Mvaz 72,208,0
W 8
s_add_u32 s64, s36, 43008
)"
R"(buffer_load_dwordx4 v230, s[8:11], s64 offen lds
Mva 72,212,12
Z 81920
T 232,193,16384
T 234,193,18432
Mvaz 80,208,4
s_add_u32 s64, s36, 57344
buffer_load_dwordx4 v230, s[8:11], s64 offen lds
Mva 80,212,16
W 8
Z 83968
T 236,193,20480
T 238,193,22528
Mvaz 76,216,0
W 8
s_add_u32 s64, s36, 71680
)"
R"(buffer_load_dwordx4 v230, s[8:11], s64 offen lds
Mva 76,220,12
Z 86016
T 240,192,16896
T 242,192,18944
Mvaz 84,216,4
s_add_u32 s64, s36, 86016
buffer_load_dwordx4 v230, s[8:11], s64 offen lds
Mva 84,220,16
W 8
Z 88064
T 244,192,20992
T 246,192,23040
Mvaz 96,224,0
W 8
s_add_u32 s64, s36, 100352
)"
R"(buffer_load_dwordx4 v230, s[8:11], s64 offen lds
Mva 96,228,12
Z 90112
T 248,193,16896
T 250,193,18944
Mvaz 104,224,4
s_add_u32 s64, s36, 114688
buffer_load_dwordx4 v230, s[8:11], s64 offen lds
Mva 104,228,16
W 8
Z 92160
T 252,193,20992
T 254,193,23040
Mvaz 100,232,0
W 8
s_add_u32 s64, s36, 129024
)"
R"(buffer_load_dwordx4 v230, s[8:11], s64 offen lds
Mva 100,236,12
Z 131072
T 208,192,24576
T 210,192,26624
Mvaz 108,232,4
buffer_load_dwordx4 v228, s[12:15], s43 offen lds
Mva 108,236,16
W 8
Z 139136
T 212,192,28672
T 214,192,30720
Mvaz 120,240,0
W 8
buffer_load_dwordx4 v228, s[12:15], s43 offen offset:128 lds
Mva 120,244,12
)"
R"(Z 147200
T 216,193,24576
T 218,193,26624
Mvaz 128,240,4
buffer_load_dwordx4 v228, s[12:15], s43 offen offset:256 lds
Mva 128,244,16
W 8
Z 155264
T 220,193,28672
T 222,193,30720
Mvaz 124,248,0
W 8
buffer_load_dwordx4 v228, s[12:15], s43 offen offset:384 lds
Mva 124,252,12
Z 133120
T 224,192,25088
)"
R"(T 226,192,27136
Mvaz 132,248,4
s_add_u32 s64, s43, 32768
buffer_load_dwordx4 v228, s[12:15], s64 offen lds
Mva 132,252,16
W 8
Z 141184
T 228,192,29184
T 230,192,31232
Mvaz 144,208,0
W 8
buffer_load_dwordx4 v228, s[12:15], s64 offen offset:128 lds
Mva 144,212,12
Z 149248
T 232,193,25088
T 234,193,27136
)"
R"(Mvaz 152,208,4
buffer_load_dwordx4 v228, s[12:15], s64 offen offset:256 lds
Mva 152,212,16
Z 157312
s_nop 0
buffer_load_dwordx4 v228, s[12:15], s64 offen offset:384 lds
Z 135168
s_add_u32 s64, s43, 262144
buffer_load_dwordx4 v228, s[12:15], s64 offen lds
Z 143232
s_nop 0
buffer_load_dwordx4 v228, s[12:15], s64 offen offset:128 lds
Z 151296
s_nop 0
buffer_load_dwordx4 v228, s[12:15], s64 offen offset:256 lds
Z 159360
)"
R"(s_nop 0
buffer_load_dwordx4 v228, s[12:15], s64 offen offset:384 lds
Z 137216
s_add_u32 s64, s43, 294912
buffer_load_dwordx4 v228, s[12:15], s64 offen lds
Z 145280
s_nop 0
buffer_load_dwordx4 v228, s[12:15], s64 offen offset:128 lds
Z 153344
s_nop 0
buffer_load_dwordx4 v228, s[12:15], s64 offen offset:256 lds
Z 161408
s_nop 0
buffer_load_dwordx4 v228, s[12:15], s64 offen offset:384 lds
T 236,193,29184
T 238,193,31232
)"
R"(s_waitcnt vmcnt(26) lgkmcnt(0)
s_barrier
N 24,204,20480
Mvaz 148,216,0
N 28,204,22528
N 36,205,20480
Mva 148,220,12
Z 32768
N 40,205,22528
T 240,194,32768
Mvaz 156,216,4
buffer_load_dwordx4 v230, s[8:11], s37 offen lds
Mva 156,220,16
Z 34816
T 242,194,34816
T 244,194,36864
)"
R"(Mvaz 168,224,0
s_add_u32 s64, s37, 14336
buffer_load_dwordx4 v230, s[8:11], s64 offen lds
Mva 168,228,12
Z 36864
T 246,194,38912
T 248,195,32768
Mvaz 176,224,4
s_add_u32 s64, s37, 28672
buffer_load_dwordx4 v230, s[8:11], s64 offen lds
Mva 176,228,16
Z 38912
T 250,195,34816
T 252,195,36864
Mvaz 172,232,0
s_add_u32 s64, s37, 43008
)"
R"(buffer_load_dwordx4 v230, s[8:11], s64 offen lds
Mva 172,236,12
Z 40960
T 254,195,38912
T 208,194,33280
Mvaz 180,232,4
s_add_u32 s64, s37, 57344
buffer_load_dwordx4 v230, s[8:11], s64 offen lds
Mva 180,236,16
W 7
Z 43008
T 210,194,35328
T 212,194,37376
Mva 0,240,24
W 7
s_add_u32 s64, s37, 71680
)"
R"(buffer_load_dwordx4 v230, s[8:11], s64 offen lds
Mva 0,244,36
Z 45056
T 214,194,39424
T 216,195,33280
Mva 8,240,28
s_add_u32 s64, s37, 86016
buffer_load_dwordx4 v230, s[8:11], s64 offen lds
Mva 8,244,40
W 7
Z 47104
T 218,195,35328
T 220,195,37376
Mva 4,248,24
W 7
s_add_u32 s64, s37, 100352
)"
R"(buffer_load_dwordx4 v230, s[8:11], s64 offen lds
Mva 4,252,36
Z 49152
T 222,195,39424
T 224,194,40960
Mva 12,248,28
s_add_u32 s64, s37, 114688
buffer_load_dwordx4 v230, s[8:11], s64 offen lds
Mva 12,252,40
W 7
Z 51200
T 226,194,43008
T 228,194,45056
Mva 24,208,24
W 7
s_add_u32 s64, s37, 129024
)"
R"(buffer_load_dwordx4 v230, s[8:11], s64 offen lds
Mva 24,212,36
Z 0
T 230,194,47104
T 232,195,40960
Mva 32,208,28
buffer_load_dwordx4 v228, s[12:15], s44 offen lds
Mva 32,212,40
W 7
Z 8064
T 234,195,43008
T 236,195,45056
Mva 28,216,24
W 7
buffer_load_dwordx4 v228, s[12:15], s44 offen offset:128 lds
Mva 28,220,36
)"
R"(Z 16128
T 238,195,47104
T 240,194,41472
Mva 36,216,28
buffer_load_dwordx4 v228, s[12:15], s44 offen offset:256 lds
Mva 36,220,40
W 7
Z 24192
T 242,194,43520
T 244,194,45568
Mva 48,224,24
W 7
buffer_load_dwordx4 v228, s[12:15], s44 offen offset:384 lds
Mva 48,228,36
Z 2048
T 246,194,47616
)"
R"(T 248,195,41472
Mva 56,224,28
s_add_u32 s64, s44, 32768
buffer_load_dwordx4 v228, s[12:15], s64 offen lds
Mva 56,228,40
W 7
Z 10112
T 250,195,43520
T 252,195,45568
Mva 52,232,24
W 7
buffer_load_dwordx4 v228, s[12:15], s64 offen offset:128 lds
Mva 52,236,36
Z 18176
T 254,195,47616
T 208,194,49152
)"
R"(Mva 60,232,28
buffer_load_dwordx4 v228, s[12:15], s64 offen offset:256 lds
Mva 60,236,40
W 7
Z 26240
T 210,194,51200
T 212,194,53248
Mva 72,240,24
W 7
buffer_load_dwordx4 v228, s[12:15], s64 offen offset:384 lds
Mva 72,244,36
Z 4096
T 214,194,55296
T 216,195,49152
Mva 80,240,28
s_add_u32 s64, s44, 262144
)"
R"(buffer_load_dwordx4 v228, s[12:15], s64 offen lds
Mva 80,244,40
W 7
Z 12160
T 218,195,51200
T 220,195,53248
Mva 76,248,24
W 7
buffer_load_dwordx4 v228, s[12:15], s64 offen offset:128 lds
Mva 76,252,36
Z 20224
T 222,195,55296
T 224,194,49664
Mva 84,248,28
buffer_load_dwordx4 v228, s[12:15], s64 offen offset:256 lds
Mva 84,252,40
)"
R"(W 7
Z 28288
T 226,194,51712
T 228,194,53760
Mva 96,208,24
W 7
buffer_load_dwordx4 v228, s[12:15], s64 offen offset:384 lds
Mva 96,212,36
Z 6144
T 230,194,55808
T 232,195,49664
Mva 104,208,28
s_add_u32 s64, s44, 294912
buffer_load_dwordx4 v228, s[12:15], s64 offen lds
Mva 104,212,40
W 7
)"
R"(Z 14208
T 234,195,51712
T 236,195,53760
Mva 100,216,24
W 7
buffer_load_dwordx4 v228, s[12:15], s64 offen offset:128 lds
Mva 100,220,36
Z 22272
T 238,195,55808
T 240,194,57344
Mva 108,216,28
buffer_load_dwordx4 v228, s[12:15], s64 offen offset:256 lds
Mva 108,220,40
W 7
Z 30336
T 242,194,59392
)"
R"(T 244,194,61440
Mva 120,224,24
W 7
buffer_load_dwordx4 v228, s[12:15], s64 offen offset:384 lds
Mva 120,228,36
T 246,194,63488
T 248,195,57344
Mva 128,224,28
s_mov_b32 s64, 32768
buffer_load_dwordx4 a[64:67], v226, s[16:19], s64 offen nt
Mva 128,228,40
W 7
T 250,195,59392
T 252,195,61440
Mva 124,232,24
W 7
)"
R"(buffer_load_dwordx4 a[68:71], v226, s[16:19], s64 offen offset:128 nt
Mva 124,236,36
T 254,195,63488
T 208,194,57856
Mva 132,232,28
buffer_load_dwordx4 a[72:75], v226, s[16:19], s64 offen offset:256 nt
Mva 132,236,40
W 7
T 210,194,59904
T 212,194,61952
Mva 144,240,24
W 7
buffer_load_dwordx4 a[76:79], v226, s[16:19], s64 offen offset:384 nt
Mva 144,244,36
T 214,194,64000
T 216,195,57856
)"
R"(Mva 152,240,28
s_mov_b32 s64, 40960
buffer_load_dwordx4 a[80:83], v226, s[16:19], s64 offen nt
Mva 152,244,40
buffer_load_dwordx4 a[84:87], v226, s[16:19], s64 offen offset:128 nt
buffer_load_dwordx4 a[88:91], v226, s[16:19], s64 offen offset:256 nt
buffer_load_dwordx4 a[92:95], v226, s[16:19], s64 offen offset:384 nt
T 218,195,59904
T 220,195,61952
T 222,195,64000
s_waitcnt vmcnt(34) lgkmcnt(0)
s_barrier
N 0,204,40960
Mva 148,248,24
N 4,204,43008
N 12,205,40960
)"
R"(Mva 148,252,36
Z 53248
N 16,205,43008
T 224,196,0
Mva 156,248,28
buffer_load_dwordx4 v230, s[8:11], s38 offen lds
Mva 156,252,40
Z 55296
T 226,196,2048
T 228,196,4096
Mva 168,208,24
s_add_u32 s64, s38, 14336
buffer_load_dwordx4 v230, s[8:11], s64 offen lds
Mva 168,212,36
Z 57344
T 230,196,6144
)"
R"(T 232,197,0
Mva 176,208,28
s_add_u32 s64, s38, 28672
buffer_load_dwordx4 v230, s[8:11], s64 offen lds
Mva 176,212,40
Z 59392
T 234,197,2048
T 236,197,4096
Mva 172,216,24
s_add_u32 s64, s38, 43008
buffer_load_dwordx4 v230, s[8:11], s64 offen lds
Mva 172,220,36
Z 61440
T 238,197,6144
T 240,196,512
Mva 180,216,28
)"
R"(s_add_u32 s64, s38, 57344
buffer_load_dwordx4 v230, s[8:11], s64 offen lds
Mva 180,220,40
W 7
Z 63488
T 242,196,2560
T 244,196,4608
Mva 0,224,0
W 7
s_add_u32 s64, s38, 71680
buffer_load_dwordx4 v230, s[8:11], s64 offen lds
Mva 0,228,12
Z 65536
T 246,196,6656
T 248,197,512
Mva 8,224,4
)"
R"(s_add_u32 s64, s38, 86016
buffer_load_dwordx4 v230, s[8:11], s64 offen lds
Mva 8,228,16
W 7
Z 67584
T 250,197,2560
T 252,197,4608
Mva 4,232,0
W 7
s_add_u32 s64, s38, 100352
buffer_load_dwordx4 v230, s[8:11], s64 offen lds
Mva 4,236,12
Z 69632
T 254,197,6656
T 208,196,8192
Mva 12,232,4
)"
R"(s_add_u32 s64, s38, 114688
buffer_load_dwordx4 v230, s[8:11], s64 offen lds
Mva 12,236,16
W 7
Z 71680
T 210,196,10240
T 212,196,12288
Mva 24,240,0
W 7
s_add_u32 s64, s38, 129024
buffer_load_dwordx4 v230, s[8:11], s64 offen lds
Mva 24,244,12
Z 98304
T 214,196,14336
T 216,197,8192
Mva 32,240,4
)"
R"(buffer_load_dwordx4 v228, s[12:15], s45 offen lds
Mva 32,244,16
W 7
Z 106368
T 218,197,10240
T 220,197,12288
Mva 28,248,0
W 7
buffer_load_dwordx4 v228, s[12:15], s45 offen offset:128 lds
Mva 28,252,12
Z 114432
T 222,197,14336
T 224,196,8704
Mva 36,248,4
buffer_load_dwordx4 v228, s[12:15], s45 offen offset:256 lds
Mva 36,252,16
)"
R"(W 7
Z 122496
T 226,196,10752
T 228,196,12800
Mva 48,208,0
W 7
buffer_load_dwordx4 v228, s[12:15], s45 offen offset:384 lds
Mva 48,212,12
Z 100352
T 230,196,14848
T 232,197,8704
Mva 56,208,4
s_add_u32 s64, s45, 32768
buffer_load_dwordx4 v228, s[12:15], s64 offen lds
Mva 56,212,16
W 7
)"
R"(Z 108416
T 234,197,10752
T 236,197,12800
Mva 52,216,0
W 7
buffer_load_dwordx4 v228, s[12:15], s64 offen offset:128 lds
Mva 52,220,12
Z 116480
T 238,197,14848
T 240,196,16384
Mva 60,216,4
buffer_load_dwordx4 v228, s[12:15], s64 offen offset:256 lds
Mva 60,220,16
W 7
Z 124544
T 242,196,18432
)"
R"(T 244,196,20480
Mva 72,224,0
W 7
buffer_load_dwordx4 v228, s[12:15], s64 offen offset:384 lds
Mva 72,228,12
Z 102400
T 246,196,22528
T 248,197,16384
Mva 80,224,4
s_add_u32 s64, s45, 262144
buffer_load_dwordx4 v228, s[12:15], s64 offen lds
Mva 80,228,16
W 7
Z 110464
T 250,197,18432
T 252,197,20480
)"
R"(Mva 76,232,0
W 7
buffer_load_dwordx4 v228, s[12:15], s64 offen offset:128 lds
Mva 76,236,12
Z 118528
T 254,197,22528
T 208,196,16896
Mva 84,232,4
buffer_load_dwordx4 v228, s[12:15], s64 offen offset:256 lds
Mva 84,236,16
W 7
Z 126592
T 210,196,18944
T 212,196,20992
Mva 96,240,0
W 7
)"
R"(buffer_load_dwordx4 v228, s[12:15], s64 offen offset:384 lds
Mva 96,244,12
Z 104448
T 214,196,23040
T 216,197,16896
Mva 104,240,4
s_add_u32 s64, s45, 294912
buffer_load_dwordx4 v228, s[12:15], s64 offen lds
Mva 104,244,16
W 7
Z 112512
T 218,197,18944
T 220,197,20992
Mva 100,248,0
W 7
buffer_load_dwordx4 v228, s[12:15], s64 offen offset:128 lds
)"
R"(Mva 100,252,12
Z 120576
T 222,197,23040
T 224,196,24576
Mva 108,248,4
buffer_load_dwordx4 v228, s[12:15], s64 offen offset:256 lds
Mva 108,252,16
W 7
Z 128640
T 226,196,26624
T 228,196,28672
Mva 120,208,0
W 7
buffer_load_dwordx4 v228, s[12:15], s64 offen offset:384 lds
Mva 120,212,12
T 230,196,30720
)"
R"(T 232,197,24576
Mva 128,208,4
s_mov_b32 s64, 49152
buffer_load_dwordx4 a[96:99], v226, s[16:19], s64 offen nt
Mva 128,212,16
W 7
T 234,197,26624
T 236,197,28672
Mva 124,216,0
W 7
buffer_load_dwordx4 a[100:103], v226, s[16:19], s64 offen offset:128 nt
Mva 124,220,12
T 238,197,30720
T 240,196,25088
Mva 132,216,4
buffer_load_dwordx4 a[104:107], v226, s[16:19], s64 offen offset:256 nt
)"
R"(Mva 132,220,16
W 7
T 242,196,27136
T 244,196,29184
Mva 144,224,0
W 7
buffer_load_dwordx4 a[108:111], v226, s[16:19], s64 offen offset:384 nt
Mva 144,228,12
T 246,196,31232
T 248,197,25088
Mva 152,224,4
s_mov_b32 s64, 57344
buffer_load_dwordx4 a[112:115], v226, s[16:19], s64 offen nt
Mva 152,228,16
buffer_load_dwordx4 a[116:119], v226, s[16:19], s64 offen offset:128 nt
buffer_load_dwordx4 a[120:123], v226, s[16:19], s64 offen offset:256 nt
)"
R"(buffer_load_dwordx4 a[124:127], v226, s[16:19], s64 offen offset:384 nt
T 250,197,27136
T 252,197,29184
T 254,197,31232
s_waitcnt vmcnt(42) lgkmcnt(0)
s_barrier
N 24,204,0
Mva 148,232,0
N 28,204,2048
N 36,205,0
Mva 148,236,12
Z 73728
N 40,205,2048
T 208,192,0
Mva 156,232,4
buffer_load_dwordx4 v230, s[8:11], s39 offen lds
)"
R"(Mva 156,236,16
Z 75776
T 210,192,2048
T 212,192,4096
Mva 168,240,0
s_add_u32 s64, s39, 14336
buffer_load_dwordx4 v230, s[8:11], s64 offen lds
Mva 168,244,12
Z 77824
T 214,192,6144
T 216,193,0
Mva 176,240,4
s_add_u32 s64, s39, 28672
buffer_load_dwordx4 v230, s[8:11], s64 offen lds
Mva 176,244,16
Z 79872
)"
R"(T 218,193,2048
T 220,193,4096
Mva 172,248,0
s_add_u32 s64, s39, 43008
buffer_load_dwordx4 v230, s[8:11], s64 offen lds
Mva 172,252,12
Z 81920
T 222,193,6144
T 224,192,512
Mva 180,248,4
s_add_u32 s64, s39, 57344
buffer_load_dwordx4 v230, s[8:11], s64 offen lds
Mva 180,252,16
W 7
Z 83968
T 226,192,2560
)"
R"(T 228,192,4608
Mva 0,208,24
W 7
s_add_u32 s64, s39, 71680
buffer_load_dwordx4 v230, s[8:11], s64 offen lds
Mva 0,212,36
Z 86016
T 230,192,6656
T 232,193,512
Mva 8,208,28
s_add_u32 s64, s39, 86016
buffer_load_dwordx4 v230, s[8:11], s64 offen lds
Mva 8,212,40
W 7
Z 88064
T 234,193,2560
)"
R"(T 236,193,4608
Mva 4,216,24
W 7
s_add_u32 s64, s39, 100352
buffer_load_dwordx4 v230, s[8:11], s64 offen lds
Mva 4,220,36
Z 90112
T 238,193,6656
T 240,192,8192
Mva 12,216,28
s_add_u32 s64, s39, 114688
buffer_load_dwordx4 v230, s[8:11], s64 offen lds
Mva 12,220,40
W 7
Z 92160
T 242,192,10240
)"
R"(T 244,192,12288
Mva 24,224,24
W 7
s_add_u32 s64, s39, 129024
buffer_load_dwordx4 v230, s[8:11], s64 offen lds
Mva 24,228,36
Z 131072
T 246,192,14336
T 248,193,8192
Mva 32,224,28
buffer_load_dwordx4 v228, s[12:15], s46 offen lds
Mva 32,228,40
W 7
Z 139136
T 250,193,10240
T 252,193,12288
)"
R"(Mva 28,232,24
W 7
buffer_load_dwordx4 v228, s[12:15], s46 offen offset:128 lds
Mva 28,236,36
Z 147200
T 254,193,14336
T 208,192,8704
Mva 36,232,28
buffer_load_dwordx4 v228, s[12:15], s46 offen offset:256 lds
Mva 36,236,40
W 7
Z 155264
T 210,192,10752
T 212,192,12800
Mva 48,240,24
W 7
)"
R"(buffer_load_dwordx4 v228, s[12:15], s46 offen offset:384 lds
Mva 48,244,36
Z 133120
T 214,192,14848
T 216,193,8704
Mva 56,240,28
s_add_u32 s64, s46, 32768
buffer_load_dwordx4 v228, s[12:15], s64 offen lds
Mva 56,244,40
W 7
Z 141184
T 218,193,10752
T 220,193,12800
Mva 52,248,24
W 7
buffer_load_dwordx4 v228, s[12:15], s64 offen offset:128 lds
)"
R"(Mva 52,252,36
Z 149248
T 222,193,14848
T 224,192,16384
Mva 60,248,28
buffer_load_dwordx4 v228, s[12:15], s64 offen offset:256 lds
Mva 60,252,40
W 7
Z 157312
T 226,192,18432
T 228,192,20480
Mva 72,208,24
W 7
buffer_load_dwordx4 v228, s[12:15], s64 offen offset:384 lds
Mva 72,212,36
Z 135168
)"
R"(T 230,192,22528
T 232,193,16384
Mva 80,208,28
s_add_u32 s64, s46, 262144
buffer_load_dwordx4 v228, s[12:15], s64 offen lds
Mva 80,212,40
W 7
Z 143232
T 234,193,18432
T 236,193,20480
Mva 76,216,24
W 7
buffer_load_dwordx4 v228, s[12:15], s64 offen offset:128 lds
Mva 76,220,36
Z 151296
T 238,193,22528
)"
R"(T 240,192,16896
Mva 84,216,28
buffer_load_dwordx4 v228, s[12:15], s64 offen offset:256 lds
Mva 84,220,40
W 7
Z 159360
T 242,192,18944
T 244,192,20992
Mva 96,224,24
W 7
buffer_load_dwordx4 v228, s[12:15], s64 offen offset:384 lds
Mva 96,228,36
Z 137216
T 246,192,23040
T 248,193,16896
Mva 104,224,28
)"
R"(s_add_u32 s64, s46, 294912
buffer_load_dwordx4 v228, s[12:15], s64 offen lds
Mva 104,228,40
W 7
Z 145280
T 250,193,18944
T 252,193,20992
Mva 100,232,24
W 7
buffer_load_dwordx4 v228, s[12:15], s64 offen offset:128 lds
Mva 100,236,36
Z 153344
T 254,193,23040
T 208,192,24576
Mva 108,232,28
buffer_load_dwordx4 v228, s[12:15], s64 offen offset:256 lds
)"
R"(Mva 108,236,40
W 7
Z 161408
T 210,192,26624
T 212,192,28672
Mva 120,240,24
W 7
buffer_load_dwordx4 v228, s[12:15], s64 offen offset:384 lds
Mva 120,244,36
T 214,192,30720
T 216,193,24576
Mva 128,240,28
buffer_load_dwordx4 a[128:131], v227, s[20:23], 0 offen nt
Mva 128,244,40
W 7
T 218,193,26624
)"
R"(T 220,193,28672
Mva 124,248,24
W 7
buffer_load_dwordx4 a[132:135], v227, s[20:23], 0 offen offset:128 nt
Mva 124,252,36
T 222,193,30720
T 224,192,25088
Mva 132,248,28
buffer_load_dwordx4 a[136:139], v227, s[20:23], 0 offen offset:256 nt
Mva 132,252,40
W 7
T 226,192,27136
T 228,192,29184
Mva 144,208,24
W 7
buffer_load_dwordx4 a[140:143], v227, s[20:23], 0 offen offset:384 nt
)"
R"(Mva 144,212,36
T 230,192,31232
T 232,193,25088
Mva 152,208,28
s_mov_b32 s64, 8192
buffer_load_dwordx4 a[144:147], v227, s[20:23], s64 offen nt
Mva 152,212,40
buffer_load_dwordx4 a[148:151], v227, s[20:23], s64 offen offset:128 nt
buffer_load_dwordx4 a[152:155], v227, s[20:23], s64 offen offset:256 nt
buffer_load_dwordx4 a[156:159], v227, s[20:23], s64 offen offset:384 nt
T 234,193,27136
T 236,193,29184
T 238,193,31232
s_waitcnt vmcnt(42) lgkmcnt(0)
s_barrier
N 0,204,20480
)"
R"(Mva 148,216,24
N 4,204,22528
N 12,205,20480
Mva 148,220,36
Z 32768
N 16,205,22528
T 240,194,32768
Mva 156,216,28
buffer_load_dwordx4 v230, s[8:11], s40 offen lds
Mva 156,220,40
Z 34816
T 242,194,34816
T 244,194,36864
Mva 168,224,24
s_add_u32 s64, s40, 14336
buffer_load_dwordx4 v230, s[8:11], s64 offen lds
)"
R"(Mva 168,228,36
Z 36864
T 246,194,38912
T 248,195,32768
Mva 176,224,28
s_add_u32 s64, s40, 28672
buffer_load_dwordx4 v230, s[8:11], s64 offen lds
Mva 176,228,40
Z 38912
T 250,195,34816
T 252,195,36864
Mva 172,232,24
s_add_u32 s64, s40, 43008
buffer_load_dwordx4 v230, s[8:11], s64 offen lds
Mva 172,236,36
Z 40960
)"
R"(T 254,195,38912
T 208,194,33280
Mva 180,232,28
s_add_u32 s64, s40, 57344
buffer_load_dwordx4 v230, s[8:11], s64 offen lds
Mva 180,236,40
W 7
Z 43008
T 210,194,35328
T 212,194,37376
Mva 0,240,0
W 7
s_add_u32 s64, s40, 71680
buffer_load_dwordx4 v230, s[8:11], s64 offen lds
Mva 0,244,12
Z 45056
)"
R"(T 214,194,39424
T 216,195,33280
Mva 8,240,4
s_add_u32 s64, s40, 86016
buffer_load_dwordx4 v230, s[8:11], s64 offen lds
Mva 8,244,16
W 7
Z 47104
T 218,195,35328
T 220,195,37376
Mva 4,248,0
W 7
s_add_u32 s64, s40, 100352
buffer_load_dwordx4 v230, s[8:11], s64 offen lds
Mva 4,252,12
Z 49152
)"
R"(T 222,195,39424
T 224,194,40960
Mva 12,248,4
s_add_u32 s64, s40, 114688
buffer_load_dwordx4 v230, s[8:11], s64 offen lds
Mva 12,252,16
W 7
Z 51200
T 226,194,43008
T 228,194,45056
Mva 24,208,0
W 7
s_add_u32 s64, s40, 129024
buffer_load_dwordx4 v230, s[8:11], s64 offen lds
Mva 24,212,12
Z 0
)"
R"(T 230,194,47104
T 232,195,40960
Mva 32,208,4
buffer_load_dwordx4 v228, s[12:15], s47 offen lds
Mva 32,212,16
W 7
Z 8064
T 234,195,43008
T 236,195,45056
Mva 28,216,0
W 7
buffer_load_dwordx4 v228, s[12:15], s47 offen offset:128 lds
Mva 28,220,12
Z 16128
T 238,195,47104
T 240,194,41472
)"
R"(Mva 36,216,4
buffer_load_dwordx4 v228, s[12:15], s47 offen offset:256 lds
Mva 36,220,16
W 7
Z 24192
T 242,194,43520
T 244,194,45568
Mva 48,224,0
W 7
buffer_load_dwordx4 v228, s[12:15], s47 offen offset:384 lds
Mva 48,228,12
Z 2048
T 246,194,47616
T 248,195,41472
Mva 56,224,4
s_add_u32 s64, s47, 32768
)"
R"(buffer_load_dwordx4 v228, s[12:15], s64 offen lds
Mva 56,228,16
W 7
Z 10112
T 250,195,43520
T 252,195,45568
Mva 52,232,0
W 7
buffer_load_dwordx4 v228, s[12:15], s64 offen offset:128 lds
Mva 52,236,12
Z 18176
T 254,195,47616
T 208,194,49152
Mva 60,232,4
buffer_load_dwordx4 v228, s[12:15], s64 offen offset:256 lds
Mva 60,236,16
)"
R"(W 7
Z 26240
T 210,194,51200
T 212,194,53248
Mva 72,240,0
W 7
buffer_load_dwordx4 v228, s[12:15], s64 offen offset:384 lds
Mva 72,244,12
Z 4096
T 214,194,55296
T 216,195,49152
Mva 80,240,4
s_add_u32 s64, s47, 262144
buffer_load_dwordx4 v228, s[12:15], s64 offen lds
Mva 80,244,16
W 7
)"
R"(Z 12160
T 218,195,51200
T 220,195,53248
Mva 76,248,0
W 7
buffer_load_dwordx4 v228, s[12:15], s64 offen offset:128 lds
Mva 76,252,12
Z 20224
T 222,195,55296
T 224,194,49664
Mva 84,248,4
buffer_load_dwordx4 v228, s[12:15], s64 offen offset:256 lds
Mva 84,252,16
W 7
Z 28288
T 226,194,51712
)"
R"(T 228,194,53760
Mva 96,208,0
W 7
buffer_load_dwordx4 v228, s[12:15], s64 offen offset:384 lds
Mva 96,212,12
Z 6144
T 230,194,55808
T 232,195,49664
Mva 104,208,4
s_add_u32 s64, s47, 294912
buffer_load_dwordx4 v228, s[12:15], s64 offen lds
Mva 104,212,16
W 7
Z 14208
T 234,195,51712
T 236,195,53760
)"
R"(Mva 100,216,0
W 7
buffer_load_dwordx4 v228, s[12:15], s64 offen offset:128 lds
Mva 100,220,12
Z 22272
T 238,195,55808
T 240,194,57344
Mva 108,216,4
buffer_load_dwordx4 v228, s[12:15], s64 offen offset:256 lds
Mva 108,220,16
W 7
Z 30336
T 242,194,59392
T 244,194,61440
Mva 120,224,0
W 7
)"
R"(buffer_load_dwordx4 v228, s[12:15], s64 offen offset:384 lds
Mva 120,228,12
T 246,194,63488
T 248,195,57344
Mva 128,224,4
s_mov_b32 s64, 16384
buffer_load_dwordx4 a[160:163], v227, s[20:23], s64 offen nt
Mva 128,228,16
W 7
T 250,195,59392
T 252,195,61440
Mva 124,232,0
W 7
buffer_load_dwordx4 a[164:167], v227, s[20:23], s64 offen offset:128 nt
Mva 124,236,12
T 254,195,63488
)"
R"(T 208,194,57856
Mva 132,232,4
buffer_load_dwordx4 a[168:171], v227, s[20:23], s64 offen offset:256 nt
Mva 132,236,16
W 7
T 210,194,59904
T 212,194,61952
Mva 144,240,0
W 7
buffer_load_dwordx4 a[172:175], v227, s[20:23], s64 offen offset:384 nt
Mva 144,244,12
T 214,194,64000
T 216,195,57856
Mva 152,240,4
s_mov_b32 s64, 24576
buffer_load_dwordx4 a[176:179], v227, s[20:23], s64 offen nt
)"
R"(Mva 152,244,16
buffer_load_dwordx4 a[180:183], v227, s[20:23], s64 offen offset:128 nt
buffer_load_dwordx4 a[184:187], v227, s[20:23], s64 offen offset:256 nt
buffer_load_dwordx4 a[188:191], v227, s[20:23], s64 offen offset:384 nt
T 218,195,59904
T 220,195,61952
T 222,195,64000
s_waitcnt vmcnt(42) lgkmcnt(0)
s_barrier
N 24,204,40960
Mva 148,248,0
N 28,204,43008
N 36,205,40960
Mva 148,252,12
Z 98304
N 40,205,43008
)"
R"(T 224,196,0
Mva 156,248,4
buffer_load_dwordx4 v226, s[16:19], 0 offen nt lds
Mva 156,252,16
T 226,196,2048
T 228,196,4096
Mva 168,208,0
Z 100224
T 230,196,6144
T 232,197,0
Mva 168,212,12
buffer_load_dwordx4 v226, s[16:19], 0 offen offset:128 nt lds
Mva 176,208,4
T 234,197,2048
T 236,197,4096
Mva 176,212,16
)"
R"(Z 102144
T 238,197,6144
T 240,196,512
Mva 172,216,0
buffer_load_dwordx4 v226, s[16:19], 0 offen offset:256 nt lds
Mva 172,220,12
T 242,196,2560
T 244,196,4608
Mva 180,216,4
Z 104064
T 246,196,6656
T 248,197,512
Mva 180,220,16
W 11
buffer_load_dwordx4 v226, s[16:19], 0 offen offset:384 nt lds
Mva 0,224,24
)"
R"(W 9
T 250,197,2560
T 252,197,4608
Mva 0,228,36
Z 106496
T 254,197,6656
T 208,196,8192
Mva 8,224,28
s_mov_b32 s64, 8192
buffer_load_dwordx4 v226, s[16:19], s64 offen nt lds
Mva 8,228,40
W 10
T 210,196,10240
T 212,196,12288
Mva 4,232,24
W 10
)"
R"(Z 108416
T 214,196,14336
T 216,197,8192
Mva 4,236,36
buffer_load_dwordx4 v226, s[16:19], s64 offen offset:128 nt lds
Mva 12,232,28
T 218,197,10240
T 220,197,12288
Mva 12,236,40
W 10
Z 110336
T 222,197,14336
T 224,196,8704
Mva 24,240,24
buffer_load_dwordx4 v226, s[16:19], s64 offen offset:256 nt lds
Mva 24,244,36
)"
R"(T 226,196,10752
T 228,196,12800
Mva 32,240,28
T 230,196,14848
T 232,197,8704
Mva 32,244,40
W 12
Z 112256
T 234,197,10752
T 236,197,12800
Mva 28,248,24
buffer_load_dwordx4 v226, s[16:19], s64 offen offset:384 nt lds
Mva 28,252,36
T 238,197,14848
T 240,196,16384
Mva 36,248,28
)"
R"(Z 114688
T 242,196,18432
T 244,196,20480
Mva 36,252,40
W 12
s_mov_b32 s64, 16384
buffer_load_dwordx4 v226, s[16:19], s64 offen nt lds
Mva 48,208,24
T 246,196,22528
T 248,197,16384
Mva 48,212,36
Z 116608
T 250,197,18432
T 252,197,20480
Mva 56,208,28
buffer_load_dwordx4 v226, s[16:19], s64 offen offset:128 nt lds
)"
R"(Mva 56,212,40
Z 118528
T 254,197,22528
T 208,196,16896
Mva 52,216,24
W 10
buffer_load_dwordx4 v226, s[16:19], s64 offen offset:256 nt lds
Mva 52,220,36
Z 120448
T 210,196,18944
T 212,196,20992
Mva 60,216,28
buffer_load_dwordx4 v226, s[16:19], s64 offen offset:384 nt lds
Mva 60,220,40
T 214,196,23040
T 216,197,16896
)"
R"(Mva 72,224,24
Z 122880
T 218,197,18944
T 220,197,20992
Mva 72,228,36
s_mov_b32 s64, 24576
buffer_load_dwordx4 v226, s[16:19], s64 offen nt lds
Mva 80,224,28
T 222,197,23040
T 224,196,24576
Mva 80,228,40
Z 124800
T 226,196,26624
T 228,196,28672
Mva 76,232,24
W 10
)"
R"(buffer_load_dwordx4 v226, s[16:19], s64 offen offset:128 nt lds
Mva 76,236,36
T 230,196,30720
Mva 84,232,28
T 232,197,24576
T 234,197,26624
Mva 84,236,40
Z 126720
T 236,197,28672
T 238,197,30720
Mva 96,240,24
buffer_load_dwordx4 v226, s[16:19], s64 offen offset:256 nt lds
Mva 96,244,36
Mva 104,240,28
Z 128640
T 240,196,25088
)"
R"(T 242,196,27136
Mva 104,244,40
buffer_load_dwordx4 v226, s[16:19], s64 offen offset:384 nt lds
Mva 100,248,24
T 244,196,29184
T 246,196,31232
Mva 100,252,36
Mva 108,248,28
T 248,197,25088
T 250,197,27136
Mva 108,252,40
W 8
T 252,197,29184
T 254,197,31232
Mva 120,208,24
Mva 120,212,36
)"
R"(Mva 128,208,28
Mva 128,212,40
Mva 124,216,24
Mva 124,220,36
Mva 132,216,28
Mva 132,220,40
Mva 144,224,24
Mva 144,228,36
Mva 152,224,28
Mva 152,228,40
s_waitcnt vmcnt(24) lgkmcnt(0)
s_barrier
N 0,204,0
Mva 148,232,24
N 4,204,2048
N 12,205,0
)"
R"(Mva 148,236,36
Z 65536
N 16,205,2048
T 208,192,0
Mva 156,232,28
s_mov_b32 s64, 65536
buffer_load_dwordx4 v226, s[16:19], s64 offen nt lds
Mva 156,236,40
T 210,192,2048
T 212,192,4096
Mva 168,240,24
Z 67456
T 214,192,6144
T 216,193,0
Mva 168,244,36
buffer_load_dwordx4 v226, s[16:19], s64 offen offset:128 nt lds
)"
R"(Mva 176,240,28
T 218,193,2048
T 220,193,4096
Mva 176,244,40
Z 69376
T 222,193,6144
T 224,192,512
Mva 172,248,24
buffer_load_dwordx4 v226, s[16:19], s64 offen offset:256 nt lds
Mva 172,252,36
T 226,192,2560
T 228,192,4608
Mva 180,248,28
Z 71296
T 230,192,6656
T 232,193,512
)"
R"(Mva 180,252,40
W 11
buffer_load_dwordx4 v226, s[16:19], s64 offen offset:384 nt lds
Mva 0,208,0
W 9
T 234,193,2560
T 236,193,4608
Mva 0,212,12
Z 73728
T 238,193,6656
T 240,192,8192
Mva 8,208,4
s_mov_b32 s64, 73728
buffer_load_dwordx4 v226, s[16:19], s64 offen nt lds
Mva 8,212,16
W 10
)"
R"(T 242,192,10240
T 244,192,12288
Mva 4,216,0
W 10
Z 75648
T 246,192,14336
T 248,193,8192
Mva 4,220,12
buffer_load_dwordx4 v226, s[16:19], s64 offen offset:128 nt lds
C 0,0,1
Mva 12,216,4
T 250,193,10240
T 252,193,12288
C 1,2,3
Mva 12,220,16
W 10
)"
R"(Z 77568
T 254,193,14336
T 208,192,8704
C 8,8,9
Mva 24,224,0
buffer_load_dwordx4 v226, s[16:19], s64 offen offset:256 nt lds
C 9,10,11
Mva 24,228,12
Z 79488
T 210,192,10752
T 212,192,12800
C 2,4,5
Mva 32,224,4
buffer_load_dwordx4 v226, s[16:19], s64 offen offset:384 nt lds
C 3,6,7
Mva 32,228,16
)"
R"(W 10
T 214,192,14848
T 216,193,8704
C 10,12,13
Mva 28,232,0
Z 81920
T 218,193,10752
T 220,193,12800
C 11,14,15
Mva 28,236,12
s_mov_b32 s64, 81920
buffer_load_dwordx4 v226, s[16:19], s64 offen nt lds
C 24,24,25
Mva 36,232,4
T 222,193,14848
T 224,192,16384
)"
R"(C 25,26,27
Mva 36,236,16
W 10
Z 83840
T 226,192,18432
T 228,192,20480
C 32,32,33
Mva 48,240,0
buffer_load_dwordx4 v226, s[16:19], s64 offen offset:128 nt lds
C 33,34,35
Mva 48,244,12
T 230,192,22528
T 232,193,16384
C 26,28,29
Mva 56,240,4
Z 85760
)"
R"(T 234,193,18432
v_permlane16_swap_b32 v8, v10
Mva 56,244,16
buffer_load_dwordx4 v226, s[16:19], s64 offen offset:256 nt lds
C 34,36,37
Mva 52,248,0
W 9
T 236,193,20480
T 238,193,22528
C 35,38,39
Mva 52,252,12
Z 87680
T 240,192,16896
v_permlane16_swap_b32 v9, v11
Mva 60,248,4
buffer_load_dwordx4 v226, s[16:19], s64 offen offset:384 nt lds
)"
R"(C 27,30,31
Mva 60,252,16
T 242,192,18944
T 244,192,20992
C 56,56,57
Mva 72,208,0
Z 90112
T 246,192,23040
v_permlane16_swap_b32 v0, v2
Mva 72,212,12
s_mov_b32 s64, 90112
buffer_load_dwordx4 v226, s[16:19], s64 offen nt lds
C 57,58,59
Mva 80,208,4
T 248,193,16896
T 250,193,18944
)"
R"(C 48,48,49
Mva 80,212,16
W 8
Z 92032
T 252,193,20992
v_permlane16_swap_b32 v1, v3
Mva 76,216,0
buffer_load_dwordx4 v226, s[16:19], s64 offen offset:128 nt lds
C 58,60,61
Mva 76,220,12
T 254,193,23040
T 208,192,24576
C 59,62,63
Mva 84,216,4
T 210,192,26624
v_permlane16_swap_b32 v32, v34
)"
R"(Mva 84,220,16
Z 93952
T 212,192,28672
v_permlane16_swap_b32 v33, v35
Mva 96,224,0
buffer_load_dwordx4 v226, s[16:19], s64 offen offset:256 nt lds
C 49,50,51
Mva 96,228,12
T 214,192,30720
T 216,193,24576
C 50,52,53
Mva 104,224,4
Z 95872
T 218,193,26624
v_permlane16_swap_b32 v24, v26
Mva 104,228,16
)"
R"(buffer_load_dwordx4 v226, s[16:19], s64 offen offset:384 nt lds
C 51,54,55
Mva 100,232,0
W 7
T 220,193,28672
T 222,193,30720
C 80,80,81
Mva 100,236,12
T 224,192,25088
v_permlane16_swap_b32 v25, v27
Mva 108,232,4
T 226,192,27136
v_permlane16_swap_b32 v56, v58
Mva 108,236,16
T 228,192,29184
v_permlane16_swap_b32 v57, v59
)"
R"(Mva 120,240,0
T 230,192,31232
v_permlane16_swap_b32 v48, v50
Mva 120,244,12
T 232,193,25088
v_permlane16_swap_b32 v49, v51
Mva 128,240,4
T 234,193,27136
C 81,82,83
C 72,72,73
Mva 128,244,16
T 236,193,29184
C 82,84,85
C 83,86,87
Mva 124,248,0
W 9
)"
R"(T 238,193,31232
v_permlane16_swap_b32 v80, v82
Mva 124,252,12
ds_write_b128 v221, a[64:67] offset:0
C 73,74,75
Mva 132,248,4
v_permlane16_swap_b32 v81, v83
C 74,76,77
Mva 132,252,16
C 75,78,79
v_permlane16_swap_b32 v72, v74
Mva 144,208,0
ds_write_b128 v221, a[68:71] offset:2048
C 104,104,105
Mva 144,212,12
v_permlane16_swap_b32 v73, v75
)"
R"(C 105,106,107
Mva 152,208,4
C 106,108,109
C 107,110,111
C 96,96,97
Mva 152,212,16
s_waitcnt vmcnt(16) lgkmcnt(0)
s_barrier
N 240,200,32768
C 97,98,99
Mva 148,216,0
Z 32768
s_mov_b32 s64, 32768
buffer_load_dwordx4 v227, s[20:23], s64 offen nt lds
C 98,100,101
Mva 148,220,12
)"
R"(ds_write_b128 v221, a[72:75] offset:4096
C 99,102,103
Mva 156,216,4
Z 40832
N 244,201,32768
v_permlane16_swap_b32 v104, v106
Mva 156,220,16
buffer_load_dwordx4 v227, s[20:23], s64 offen offset:128 nt lds
C 128,128,129
Mva 168,224,0
N 248,200,34816
v_permlane16_swap_b32 v105, v107
Mva 168,228,12
N 252,201,34816
v_permlane16_swap_b32 v96, v98
Mva 176,224,4
)"
R"(Z 48896
ds_write_b128 v221, a[76:79] offset:6144
C 129,130,131
Mva 176,228,16
buffer_load_dwordx4 v227, s[20:23], s64 offen offset:256 nt lds
C 130,132,133
Mva 172,232,0
N 208,200,40960
v_permlane16_swap_b32 v97, v99
Mva 172,236,12
ds_write_b128 v221, a[80:83] offset:8192
C 131,134,135
Mva 180,232,4
Z 56960
N 212,201,40960
v_permlane16_swap_b32 v128, v130
)"
R"(Mva 180,236,16
W 6
buffer_load_dwordx4 v227, s[20:23], s64 offen offset:384 nt lds
C 120,120,121
Mvvz 4,240,0
N 216,200,43008
v_permlane16_swap_b32 v129, v131
Mvv 4,244,24
W 6
N 220,201,43008
C 121,122,123
C 152,152,153
Mvv 4,248,48
W 6
Z 34816
ds_write_b128 v221, a[84:87] offset:10240
)"
R"(C 122,124,125
Mvv 4,252,72
s_mov_b32 s64, 40960
buffer_load_dwordx4 v227, s[20:23], s64 offen nt lds
C 123,126,127
Mvvz 12,240,8
N 224,200,49152
v_permlane16_swap_b32 v120, v122
Mvv 12,244,32
ds_write_b128 v221, a[88:91] offset:12288
C 153,154,155
Mvv 12,248,56
Z 42880
N 228,201,49152
v_permlane16_swap_b32 v121, v123
Mvv 12,252,80
)"
R"(W 6
buffer_load_dwordx4 v227, s[20:23], s64 offen offset:128 nt lds
C 154,156,157
Mvvz 28,208,0
N 232,200,51200
v_permlane16_swap_b32 v152, v154
Mvv 28,212,24
W 6
N 236,201,51200
C 155,158,159
C 144,144,145
Mvv 28,216,48
W 6
Z 50944
ds_write_b128 v221, a[92:95] offset:14336
C 145,146,147
)"
R"(Mvv 28,220,72
buffer_load_dwordx4 v227, s[20:23], s64 offen offset:256 nt lds
C 146,148,149
Mvvz 36,208,8
N 240,200,57344
v_permlane16_swap_b32 v153, v155
Mvv 36,212,32
ds_write_b128 v221, a[96:99] offset:16384
C 147,150,151
Mvv 36,216,56
Z 59008
N 244,201,57344
v_permlane16_swap_b32 v144, v146
Mvv 36,220,80
W 6
buffer_load_dwordx4 v227, s[20:23], s64 offen offset:384 nt lds
)"
R"(C 176,176,177
Mvvz 52,224,0
N 248,200,59392
v_permlane16_swap_b32 v145, v147
Mvv 52,228,24
W 6
N 252,201,59392
C 177,178,179
C 168,168,169
Mvv 52,232,48
W 6
Z 36864
ds_write_b128 v221, a[100:103] offset:18432
C 178,180,181
Mvv 52,236,72
s_mov_b32 s64, 49152
)"
R"(buffer_load_dwordx4 v227, s[20:23], s64 offen nt lds
C 179,182,183
Mvvz 60,224,8
N 208,200,36864
v_permlane16_swap_b32 v176, v178
Mvv 60,228,32
Z 44928
ds_write_b128 v221, a[104:107] offset:20480
C 169,170,171
Mvv 60,232,56
buffer_load_dwordx4 v227, s[20:23], s64 offen offset:128 nt lds
C 170,172,173
Mvv 60,236,80
W 5
N 212,201,36864
v_permlane16_swap_b32 v177, v179
)"
R"(Mvvz 76,240,0
N 216,200,38912
v_permlane16_swap_b32 v168, v170
Mvv 76,244,24
W 6
Z 52992
N 220,201,38912
C 171,174,175
Mvv 76,248,48
W 6
buffer_load_dwordx4 v227, s[20:23], s64 offen offset:256 nt lds
Mvv 76,252,72
ds_write_b128 v221, a[108:111] offset:22528
Mvvz 84,240,8
v_permlane16_swap_b32 v169, v171
Mvv 84,244,32
)"
R"(Z 61056
N 224,200,45056
N 228,201,45056
Mvv 84,248,56
buffer_load_dwordx4 v227, s[20:23], s64 offen offset:384 nt lds
Mvv 84,252,80
W 6
ds_write_b128 v221, a[112:115] offset:24576
Mvv 4,208,96
W 6
N 232,200,47104
N 236,201,47104
Mvv 4,212,120
W 7
Z 38912
N 240,200,53248
)"
R"(N 244,201,53248
Mvv 4,216,144
W 8
s_mov_b32 s64, 57344
buffer_load_dwordx4 v227, s[20:23], s64 offen nt lds
Mvv 4,220,168
ds_write_b128 v221, a[116:119] offset:26624
Mvv 12,208,104
N 248,200,55296
N 252,201,55296
Mvv 12,212,128
N 208,200,61440
N 212,201,61440
Mvv 12,216,152
ds_write_b128 v221, a[120:123] offset:28672
Mvv 12,220,176
)"
R"(W 11
N 216,200,63488
v_max_f32 v217, v4, v5
Mvv 28,224,96
N 220,201,63488
v_max3_f32 v217, v217, v6, v7
Mvv 28,228,120
W 10
ds_write_b128 v221, a[124:127] offset:30720
Mvv 28,232,144
Mvv 28,236,168
v_max_f32 v218, v12, v13
Mvv 36,224,104
v_max3_f32 v218, v218, v14, v15
Mvv 36,228,128
Mvv 36,232,152
)"
R"(Mvv 36,236,176
W 3
s_nop 0
v_max3_f32 v217, v217, v28, v29
Mvv 52,240,96
v_max3_f32 v217, v217, v30, v31
Mvv 52,244,120
Mvv 52,248,144
Mvv 52,252,168
W 0
s_barrier
N 224,202,0
v_max3_f32 v218, v218, v36, v37
Mvv 60,240,104
Z 46976
s_nop 0
)"
R"(buffer_load_dwordx4 v227, s[20:23], s64 offen offset:128 nt lds
v_max3_f32 v218, v218, v38, v39
Mvv 60,244,128
N 228,203,0
N 232,202,2048
Mvv 60,248,152
Z 55040
N 236,203,2048
N 240,202,4096
Mvv 60,252,176
buffer_load_dwordx4 v227, s[20:23], s64 offen offset:256 nt lds
v_max3_f32 v217, v217, v52, v53
Mvv 76,208,96
N 244,203,4096
v_max3_f32 v217, v217, v54, v55
Mvv 76,212,120
)"
R"(N 248,202,6144
N 252,203,6144
Mvv 76,216,144
Z 63104
Mvv 76,220,168
buffer_load_dwordx4 v227, s[20:23], s64 offen offset:384 nt lds
v_max3_f32 v218, v218, v60, v61
Mvv 84,208,104
N 208,202,8192
v_max3_f32 v218, v218, v62, v63
Mvv 84,212,128
N 212,203,8192
Mvv 84,216,152
Z 98304
N 216,202,10240
Mvv 84,220,176
)"
R"(W 6
s_mov_b32 s64, 98304
buffer_load_dwordx4 v226, s[16:19], s64 offen nt lds
v_max3_f32 v217, v217, v76, v77
Mvvz 100,224,0
N 220,203,10240
v_max3_f32 v217, v217, v78, v79
Mvv 100,228,24
v_mov_b32 v238, v217
Mvv 100,232,48
Z 100224
Mvv 100,236,72
buffer_load_dwordx4 v226, s[16:19], s64 offen offset:128 nt lds
v_max3_f32 v218, v218, v84, v85
Mvvz 108,224,8
N 224,202,12288
)"
R"(v_permlane32_swap_b32 v217, v238
Mvv 108,228,32
N 228,203,12288
v_max3_f32 v218, v218, v86, v87
v_max_f32 v217, v217, v238
Mvv 108,232,56
Z 102144
N 232,202,14336
v_mov_b32 v239, v218
v_mov_b32 v238, v217
Mvv 108,236,80
buffer_load_dwordx4 v226, s[16:19], s64 offen offset:256 nt lds
Mvv 100,240,96
W 4
ds_write_b128 v220, a[128:131] offset:0
Mvv 100,244,120
)"
R"(v_permlane32_swap_b32 v218, v239
Mvv 100,248,144
Z 104064
N 236,203,14336
v_permlane16_swap_b32 v217, v238
Mvv 100,252,168
buffer_load_dwordx4 v226, s[16:19], s64 offen offset:384 nt lds
v_max_f32 v218, v218, v239
Mvv 108,240,104
ds_write_b128 v220, a[132:135] offset:8192
v_mov_b32 v239, v218
Mvv 108,244,128
N 240,202,16384
v_permlane16_swap_b32 v218, v239
Mvv 108,248,152
Z 106496
)"
R"(N 244,203,16384
v_max_f32 v217, v217, v238
v_max_f32 v218, v218, v239
Mvv 108,252,176
s_mov_b32 s64, 106496
buffer_load_dwordx4 v226, s[16:19], s64 offen nt lds
v_mov_b32 v208, v217
Mvvz 124,208,0
ds_write_b128 v220, a[136:139] offset:16384
v_mov_b32 v209, v218
Mvv 124,212,24
N 248,202,18432
v_mul_f32 v214, -s63, v217
v_mul_f32 v215, -s63, v218
Mvv 124,216,48
W 6
)"
R"(Z 108416
N 252,203,18432
v_add_f32 v211, s65, v217
v_add_f32 v212, s65, v218
Mvv 124,220,72
buffer_load_dwordx4 v226, s[16:19], s64 offen offset:128 nt lds
F 4,214
Mvvz 132,208,8
ds_write_b128 v220, a[140:143] offset:24576
F 5,214
Mvv 132,212,32
Z 110336
N 208,202,20480
E 4
Mvv 132,216,56
buffer_load_dwordx4 v226, s[16:19], s64 offen offset:256 nt lds
)"
R"(F 6,214
Mvv 132,220,80
ds_write_b128 v220, a[144:147] offset:2048
F 7,214
Mvv 124,224,96
N 212,203,20480
E 5
Mvv 124,228,120
Z 112256
N 216,202,22528
E 6
Mvv 124,232,144
W 6
buffer_load_dwordx4 v226, s[16:19], s64 offen offset:384 nt lds
F 28,214
Mvv 124,236,168
)"
R"(ds_write_b128 v220, a[148:151] offset:10240
F 29,214
Mvv 132,224,104
N 220,203,22528
E 7
Mvv 132,228,128
Z 114688
N 224,202,24576
E 28
Mvv 132,232,152
s_mov_b32 s64, 114688
buffer_load_dwordx4 v226, s[16:19], s64 offen nt lds
F 30,214
Mvv 132,236,176
ds_write_b128 v220, a[152:155] offset:18432
F 31,214
)"
R"(Mvvz 148,240,0
N 228,203,24576
E 29
Mvv 148,244,24
Z 116608
N 232,202,26624
E 30
Mvv 148,248,48
W 6
buffer_load_dwordx4 v226, s[16:19], s64 offen offset:128 nt lds
C 4,4,5
Mvv 148,252,72
ds_write_b128 v220, a[156:159] offset:26624
C 5,6,7
Mvvz 156,240,8
N 236,203,26624
)"
R"(E 31
Mvv 156,244,32
N 240,202,28672
C 6,28,29
C 7,30,31
Mvv 156,248,56
ds_write_b128 v220, a[160:163] offset:4096
F 12,215
Mvv 156,252,80
N 244,203,28672
E 12
Mvv 148,208,96
N 248,202,30720
F 13,215
F 14,215
Mvv 148,212,120
)"
R"(ds_write_b128 v220, a[164:167] offset:12288
F 15,215
Mvv 148,216,144
W 7
N 252,203,30720
E 13
Mvv 148,220,168
E 14
F 36,215
Mvv 156,208,104
ds_write_b128 v220, a[168:171] offset:20480
F 37,215
Mvv 156,212,128
E 15
F 38,215
Mvv 156,216,152
)"
R"(E 36
F 39,215
Mvv 156,220,176
ds_write_b128 v220, a[172:175] offset:28672
C 12,12,13
Mvvz 172,224,0
E 37
C 13,14,15
Mvv 172,228,24
E 38
C 14,36,37
Mvv 172,232,48
W 3
ds_write_b128 v220, a[176:179] offset:6144
F 52,214
Mvv 172,236,72
)"
R"(ds_write_b128 v220, a[180:183] offset:14336
ds_write_b128 v220, a[184:187] offset:22528
ds_write_b128 v220, a[188:191] offset:30720
W 0
s_barrier
T 208,192,0
F 53,214
Mvvz 180,224,8
Z 118528
s_nop 0
buffer_load_dwordx4 v226, s[16:19], s64 offen offset:256 nt lds
F 54,214
Mvv 180,228,32
T 210,192,2048
T 212,193,0
F 55,214
)"
R"(Mvv 180,232,56
T 214,193,2048
T 216,192,512
F 76,214
Mvv 180,236,80
Z 120448
T 218,192,2560
T 220,193,512
F 77,214
Mvv 172,240,96
buffer_load_dwordx4 v226, s[16:19], s64 offen offset:384 nt lds
F 78,214
Mvv 172,244,120
T 222,193,2560
T 224,192,8192
F 79,214
)"
R"(Mvv 172,248,144
T 226,192,10240
E 39
Mvv 172,252,168
Z 122880
T 228,193,8192
E 52
Mvv 180,240,104
s_mov_b32 s64, 122880
buffer_load_dwordx4 v226, s[16:19], s64 offen nt lds
C 15,38,39
Mvv 180,244,128
T 230,193,10240
T 232,192,8704
F 60,215
Mvv 180,248,152
)"
R"(T 234,192,10752
E 53
Mvv 180,252,176
W 9
Z 124800
T 236,193,8704
E 54
Mavz 0,208,4
buffer_load_dwordx4 v226, s[16:19], s64 offen offset:128 nt lds
C 52,52,53
Mavz 4,208,12
T 238,193,10752
T 240,192,4096
F 61,215
Mavz 8,212,4
T 242,192,6144
)"
R"(E 55
Mavz 12,212,12
W 8
Z 126720
T 244,193,4096
E 76
Mavz 16,216,4
buffer_load_dwordx4 v226, s[16:19], s64 offen offset:256 nt lds
C 53,54,55
Mavz 20,216,12
T 246,193,6144
T 248,192,4608
F 62,215
Mavz 24,220,4
T 250,192,6656
E 77
)"
R"(Mavz 28,220,12
Z 128640
T 252,193,4608
E 78
Mavz 32,224,4
buffer_load_dwordx4 v226, s[16:19], s64 offen offset:384 nt lds
C 54,76,77
Mavz 36,224,12
W 8
T 254,193,6656
T 208,192,12288
F 63,215
Mavz 40,228,4
T 210,192,14336
E 79
Mavz 44,228,12
)"
R"(Z 131072
T 212,193,12288
E 60
Mavz 48,232,4
s_mov_b32 s64, 65536
buffer_load_dwordx4 v227, s[20:23], s64 offen nt lds
C 55,78,79
Mavz 52,232,12
W 8
T 214,193,14336
T 216,192,12800
F 84,215
Mavz 56,236,4
T 218,192,14848
E 61
Mavz 60,236,12
)"
R"(Z 139136
T 220,193,12800
E 62
Mav 0,240,52
buffer_load_dwordx4 v227, s[20:23], s64 offen offset:128 nt lds
F 85,215
C 60,60,61
E 63
s_nop 0
C 61,62,63
E 84
E 85
s_nop 0
C 62,84,85
F 86,215
E 86
)"
R"(F 87,215
E 87
s_nop 0
C 63,86,87
s_nop 1
Mav 4,240,60
W 8
T 222,193,14848
T 224,192,16384
v_max_f32 v217, v100, v101
Mav 8,244,52
T 226,192,18432
v_max_f32 v218, v108, v109
v_max3_f32 v217, v217, v102, v103
Mav 12,244,60
Z 147200
)"
R"(T 228,192,20480
v_max3_f32 v218, v218, v110, v111
v_max3_f32 v217, v217, v124, v125
Mav 16,248,52
buffer_load_dwordx4 v227, s[20:23], s64 offen offset:256 nt lds
v_max3_f32 v218, v218, v132, v133
Mav 20,248,60
W 8
T 230,192,22528
T 232,193,16384
v_max3_f32 v217, v217, v126, v127
Mav 24,252,52
T 234,193,18432
v_max3_f32 v218, v218, v134, v135
v_max3_f32 v217, v217, v148, v149
Mav 28,252,60
)"
R"(T 236,193,20480
v_max3_f32 v218, v218, v156, v157
v_max3_f32 v217, v217, v150, v151
Mav 32,208,52
Z 155264
T 238,193,22528
v_max3_f32 v218, v218, v158, v159
v_max3_f32 v217, v217, v172, v173
Mav 36,208,60
W 9
buffer_load_dwordx4 v227, s[20:23], s64 offen offset:384 nt lds
v_max3_f32 v218, v218, v180, v181
Mav 40,212,52
T 240,192,16896
T 242,192,18944
v_max3_f32 v217, v217, v174, v175
)"
R"(Mav 44,212,60
T 244,192,20992
v_max3_f32 v218, v218, v182, v183
v_cmp_gt_f32 vcc, v217, v211
Mav 48,216,52
Z 133120
T 246,192,23040
v_cmp_gt_f32 s[68:69], v218, v212
Mav 52,216,60
W 9
s_mov_b32 s64, 73728
buffer_load_dwordx4 v227, s[20:23], s64 offen nt lds
s_or_b64 vcc, vcc, s[68:69]
Mav 56,220,52
s_cbranch_vccnz .LrareA2_1
.LbackA2_1:
)"
R"(T 248,193,16896
T 250,193,18944
F 100,214
Mav 60,220,60
T 252,193,20992
E 100
Mavz 64,224,4
W 8
Z 141184
T 254,193,23040
F 101,214
F 102,214
Mav 64,228,52
buffer_load_dwordx4 v227, s[20:23], s64 offen offset:128 nt lds
F 103,214
Mavz 68,224,12
)"
R"(T 208,192,24576
T 210,192,26624
F 124,214
Mav 68,228,60
T 212,192,28672
E 101
Mavz 72,232,4
W 8
Z 149248
T 214,192,30720
E 102
Mav 72,236,52
buffer_load_dwordx4 v227, s[20:23], s64 offen offset:256 nt lds
F 125,214
Mavz 76,232,12
T 216,193,24576
)"
R"(T 218,193,26624
F 126,214
Mav 76,236,60
T 220,193,28672
E 103
Mavz 192,204,4
T 222,193,30720
E 124
Mav 192,204,52
T 224,192,25088
E 125
Mavz 196,204,12
T 226,192,27136
E 126
Mav 196,204,60
T 228,192,29184
)"
R"(F 127,214
C 100,100,101
Mavz 80,240,4
W 8
T 230,192,31232
E 127
Mav 80,244,52
T 232,193,25088
C 101,102,103
F 108,215
Mavz 84,240,12
T 234,193,27136
E 108
Mav 84,244,60
T 236,193,29184
C 102,124,125
)"
R"(C 103,126,127
Mavz 88,248,4
T 238,193,31232
F 109,215
F 110,215
Mav 88,252,52
E 109
F 111,215
Mavz 92,248,12
E 110
F 132,215
Mav 92,252,60
E 111
F 133,215
Mavz 96,208,4
W 5
)"
R"(E 132
F 134,215
Mav 96,212,52
E 133
F 135,215
Mavz 100,208,12
E 134
C 108,108,109
Mav 100,212,60
s_waitcnt vmcnt(39) lgkmcnt(0)
s_barrier
v_and_b32 v207, 63, v255
v_lshrrev_b32 v206, 4, v207
v_lshlrev_b32 v206, 20, v206
v_lshl_add_u32 v206, v207, 2, v206
v_add_u32 v206, s94, v206
)"
R"(v_lshlrev_b32 v207, 2, v207
N 240,200,0
C 109,110,111
Mavz 104,216,4
Z 157312
s_nop 0
buffer_load_dwordx4 v227, s[20:23], s64 offen offset:384 nt lds
C 110,132,133
Mav 104,220,52
N 244,201,0
E 135
Mavz 108,216,12
Z 135168
N 248,200,2048
C 111,134,135
F 148,214
)"
R"(Mav 108,220,60
s_mov_b32 s64, 81920
buffer_load_dwordx4 v227, s[20:23], s64 offen nt lds
F 149,214
Mavz 112,224,4
N 252,201,2048
E 148
Mav 112,228,52
N 208,200,4096
E 149
Mavz 116,224,12
Z 143232
N 212,201,4096
F 150,214
F 151,214
Mav 116,228,60
)"
R"(buffer_load_dwordx4 v227, s[20:23], s64 offen offset:128 nt lds
F 172,214
Mavz 120,232,4
N 216,200,6144
E 150
Mav 120,236,52
N 220,201,6144
E 151
Mavz 124,232,12
Z 151296
N 224,200,8192
E 172
Mav 124,236,60
s_cbranch_vccnz .LrareB2_1
.LbackB2_1:
W 6
)"
R"(buffer_load_dwordx4 v227, s[20:23], s64 offen offset:256 nt lds
F 173,214
Mvvz 4,240,0
N 228,201,8192
E 173
Mvv 4,244,24
N 232,200,10240
F 174,214
F 175,214
Mvv 4,248,48
W 6
Z 159360
N 236,201,10240
E 174
Mvv 4,252,72
buffer_load_dwordx4 v227, s[20:23], s64 offen offset:384 nt lds
)"
R"(C 148,148,149
Mvvz 12,240,8
N 240,200,12288
E 175
Mvv 12,244,32
N 244,201,12288
C 149,150,151
F 156,215
Mvv 12,248,56
Z 137216
N 248,200,14336
E 156
Mvv 12,252,80
s_mov_b32 s64, 90112
buffer_load_dwordx4 v227, s[20:23], s64 offen nt lds
C 150,172,173
)"
R"(Mvv 4,208,96
W 6
N 252,201,14336
C 151,174,175
F 157,215
Mvv 4,212,120
E 157
F 158,215
Mvv 4,216,144
Z 145280
E 158
F 159,215
Mvv 4,220,168
buffer_load_dwordx4 v227, s[20:23], s64 offen offset:128 nt lds
F 180,215
Mvv 12,208,104
)"
R"(N 208,200,16384
E 159
Mvv 12,212,128
N 212,201,16384
E 180
Mvv 12,216,152
Z 153344
N 216,200,18432
F 181,215
F 182,215
Mvv 12,220,176
buffer_load_dwordx4 v227, s[20:23], s64 offen offset:256 nt lds
F 183,215
Mvvz 28,224,0
W 4
N 220,201,18432
)"
R"(E 181
Mvv 28,228,24
E 182
C 156,156,157
Mvv 28,232,48
Z 161408
E 183
C 157,158,159
Mvv 28,236,72
buffer_load_dwordx4 v227, s[20:23], s64 offen offset:384 nt lds
C 158,180,181
Mvvz 36,224,8
N 224,200,20480
C 159,182,183
v_max_f32 v217, v4, v5
Mvv 36,228,32
)"
R"(Z 0
N 228,201,20480
v_max_f32 v218, v12, v13
v_max3_f32 v217, v217, v6, v7
Mvv 36,232,56
s_mov_b32 s64, 131072
buffer_load_dwordx4 v226, s[16:19], s64 offen nt lds
v_max3_f32 v218, v218, v14, v15
Mvv 36,236,80
N 232,200,22528
N 236,201,22528
Mvv 28,240,96
Mvv 28,244,120
Z 1920
Mvv 28,248,144
W 4
)"
R"(buffer_load_dwordx4 v226, s[16:19], s64 offen offset:128 nt lds
Mvv 28,252,168
Mvv 36,240,104
N 240,200,24576
Mvv 36,244,128
Z 3840
N 244,201,24576
Mvv 36,248,152
buffer_load_dwordx4 v226, s[16:19], s64 offen offset:256 nt lds
Mvv 36,252,176
N 248,200,26624
v_max3_f32 v217, v217, v28, v29
Mvvz 52,208,0
N 252,201,26624
v_max3_f32 v217, v217, v30, v31
Mvv 52,212,24
)"
R"(Z 5760
Mvv 52,216,48
buffer_load_dwordx4 v226, s[16:19], s64 offen offset:384 nt lds
Mvv 52,220,72
v_max3_f32 v218, v218, v36, v37
Mvvz 60,208,8
N 208,200,28672
v_max3_f32 v218, v218, v38, v39
Mvv 60,212,32
N 212,201,28672
Mvv 60,216,56
N 216,200,30720
Mvv 60,220,80
W 5
N 220,201,30720
Mvv 52,224,96
)"
R"(Mvv 52,228,120
Mvv 52,232,144
Mvv 52,236,168
Mvv 60,224,104
Mvv 60,228,128
Mvv 60,232,152
Mvv 60,236,176
s_nop 3
v_max3_f32 v217, v217, v52, v53
Mvvz 76,240,0
v_max3_f32 v217, v217, v54, v55
Mvv 76,244,24
W 0
Mvv 76,248,48
Mvv 76,252,72
s_waitcnt vmcnt(36) lgkmcnt(0)
)"
R"(s_barrier
T 224,192,32768
v_max3_f32 v218, v218, v60, v61
Mvvz 84,240,8
Z 8192
s_mov_b32 s64, 139264
buffer_load_dwordx4 v226, s[16:19], s64 offen nt lds
v_max3_f32 v218, v218, v62, v63
Mvv 84,244,32
s_mul_i32 s80, s77, 2293760
T 226,192,34816
T 228,192,36864
Mvv 84,248,56
s_add_u32 s8, s4, s80
s_addc_u32 s9, s5, 0
T 230,192,38912
)"
R"(T 232,193,32768
Mvv 84,252,80
Z 10112
T 234,193,34816
T 236,193,36864
Mvv 76,208,96
buffer_load_dwordx4 v226, s[16:19], s64 offen offset:128 nt lds
Mvv 76,212,120
s_and_b32 s9, s9, 0xffff
T 238,193,38912
T 240,192,33280
Mvv 76,216,144
s_mov_b32 s10, 2293760
T 242,192,35328
T 244,192,37376
Mvv 76,220,168
)"
R"(Z 12032
T 246,192,39424
T 248,193,33280
Mvv 84,208,104
buffer_load_dwordx4 v226, s[16:19], s64 offen offset:256 nt lds
Mvv 84,212,128
s_mov_b32 s11, 0x20000
T 250,193,35328
T 252,193,37376
Mvv 84,216,152
s_mul_i32 s80, s77, 143360
T 254,193,39424
T 208,192,40960
Mvv 84,220,176
W 12
Z 13952
)"
R"(T 210,192,43008
v_max3_f32 v217, v217, v76, v77
Mav 0,224,100
buffer_load_dwordx4 v226, s[16:19], s64 offen offset:384 nt lds
v_max3_f32 v217, v217, v78, v79
Mav 0,228,148
s_add_u32 s12, s6, s80
s_addc_u32 s13, s7, 0
T 212,192,45056
v_cmp_gt_f32 vcc, v217, v211
Mav 4,224,108
s_and_b32 s13, s13, 0xffff
T 214,192,47104
T 216,193,40960
Mav 4,228,156
W 10
)"
R"(Z 16384
T 218,193,43008
v_max3_f32 v218, v218, v84, v85
Mav 8,232,100
s_mov_b32 s64, 147456
buffer_load_dwordx4 v226, s[16:19], s64 offen nt lds
v_max3_f32 v218, v218, v86, v87
Mav 8,236,148
s_mov_b32 s14, 143360
T 220,193,45056
v_cmp_gt_f32 s[68:69], v218, v212
Mav 12,232,108
s_mov_b32 s15, 0x20000
T 222,193,47104
s_or_b64 vcc, vcc, s[68:69]
Mav 12,236,156
)"
R"(Z 18304
s_cbranch_vccnz .LrareA2_2
.LbackA2_2:
T 224,192,41472
F 4,214
F 5,214
Mav 16,240,100
W 7
buffer_load_dwordx4 v226, s[16:19], s64 offen offset:128 nt lds
F 6,214
Mav 16,244,148
s_lshl2_add_u32 s34, s78, s76
T 226,192,43520
E 4
Mav 20,240,108
s_add_u32 s37, s34, 76
)"
R"(T 228,192,45568
E 5
Mav 20,244,156
Z 20224
T 230,192,47616
E 6
Mav 24,248,100
buffer_load_dwordx4 v226, s[16:19], s64 offen offset:256 nt lds
F 7,214
Mav 24,252,148
s_add_u32 s40, s78, 64
T 232,193,41472
E 7
Mav 28,248,108
s_mul_i32 s80, s34, 6554
T 234,193,43520
)"
R"(F 28,214
F 29,214
Mav 28,252,156
Z 22144
T 236,193,45568
E 28
Mav 32,208,100
W 6
buffer_load_dwordx4 v226, s[16:19], s64 offen offset:384 nt lds
F 30,214
Mav 32,212,148
s_lshr_b32 s80, s80, 16
T 238,193,47616
T 240,192,49152
F 31,214
Mav 36,208,108
)"
R"(s_mul_i32 s81, s80, 10
T 242,192,51200
E 29
Mav 36,212,156
Z 24576
T 244,192,53248
E 30
Mav 40,216,100
s_mov_b32 s64, 155648
buffer_load_dwordx4 v226, s[16:19], s64 offen nt lds
C 4,4,5
Mav 40,220,148
s_sub_u32 s81, s34, s81
T 246,192,55296
T 248,193,49152
C 5,6,7
)"
R"(Mav 44,216,108
s_and_b32 s82, s80, 1
T 250,193,51200
E 31
Mav 44,220,156
W 8
s_lshr_b32 s80, s80, 1
T 252,193,53248
C 6,28,29
C 7,30,31
Mav 48,224,100
Z 26496
T 254,193,55296
F 12,215
F 13,215
Mav 48,228,148
)"
R"(buffer_load_dwordx4 v226, s[16:19], s64 offen offset:128 nt lds
F 14,215
Mav 52,224,108
s_mul_i32 s34, s80, 20
T 208,192,49664
T 210,192,51712
F 15,215
Mav 52,228,156
s_add_u32 s34, s34, s82
T 212,192,53760
E 12
Mav 56,232,100
W 8
Z 28416
T 214,192,55808
E 13
)"
R"(Mav 56,236,148
buffer_load_dwordx4 v226, s[16:19], s64 offen offset:256 nt lds
F 36,215
Mav 60,232,108
s_lshl1_add_u32 s34, s81, s34
T 216,193,49664
T 218,193,51712
F 37,215
Mav 60,236,156
s_lshl_b32 s34, s34, 10
T 220,193,53760
E 14
Mav 64,240,100
W 8
Z 30336
T 222,193,55808
)"
R"(E 15
Mav 64,244,148
buffer_load_dwordx4 v226, s[16:19], s64 offen offset:384 nt lds
F 38,215
Mav 68,240,108
s_add_u32 s80, s80, s79
T 224,192,57344
T 226,192,59392
F 39,215
Mav 68,244,156
s_cmp_ge_u32 s80, 7
s_cselect_b32 s35, 7, 0
T 228,192,61440
E 36
Mav 72,248,100
W 8
)"
R"(Z 65536
T 230,192,63488
E 37
Mav 72,252,148
s_mov_b32 s64, 98304
buffer_load_dwordx4 v227, s[20:23], s64 offen nt lds
C 12,12,13
Mav 76,248,108
s_sub_u32 s80, s80, s35
T 232,193,57344
T 234,193,59392
C 13,14,15
Mav 76,252,156
s_lshl1_add_u32 s80, s80, s82
T 236,193,61440
E 38
)"
R"(Mav 192,204,100
s_lshl_b32 s35, s80, 6
T 238,193,63488
E 39
Mav 192,204,148
s_mul_i32 s80, s81, 14336
T 240,192,57856
C 14,36,37
C 15,38,39
Mav 196,204,108
s_add_u32 s35, s35, s80
T 242,192,59904
F 52,214
F 53,214
Mav 196,204,156
s_lshl_b32 s36, s81, 6
)"
R"(T 244,192,61952
E 52
Mav 80,208,100
W 8
s_mul_i32 s80, s37, 6554
T 246,192,64000
E 53
Mav 80,212,148
s_lshr_b32 s80, s80, 16
T 248,193,57856
F 54,214
F 55,214
Mav 84,208,108
s_mul_i32 s81, s80, 10
T 250,193,59904
E 54
)"
R"(Mav 84,212,156
s_sub_u32 s81, s37, s81
T 252,193,61952
E 55
Mav 88,216,100
s_and_b32 s82, s80, 1
T 254,193,64000
F 76,214
F 77,214
Mav 88,220,148
s_lshr_b32 s80, s80, 1
E 76
F 78,214
Mav 92,216,108
s_mul_i32 s37, s80, 20
E 77
)"
R"(F 79,214
Mav 92,220,156
s_add_u32 s37, s37, s82
E 78
C 52,52,53
Mav 96,224,100
W 5
s_lshl1_add_u32 s37, s81, s37
E 79
C 53,54,55
Mav 96,228,148
s_lshl_b32 s37, s37, 10
C 54,76,77
C 55,78,79
F 60,215
Mav 100,224,108
)"
R"(s_add_u32 s80, s80, s79
E 60
F 61,215
Mav 100,228,156
s_cmp_ge_u32 s80, 7
s_cselect_b32 s38, 7, 0
s_waitcnt vmcnt(33) lgkmcnt(0)
s_barrier
N 208,200,32768
F 62,215
Mav 104,232,100
Z 73600
s_nop 0
buffer_load_dwordx4 v227, s[20:23], s64 offen offset:128 nt lds
F 63,215
Mav 104,236,148
)"
R"(s_sub_u32 s80, s80, s38
N 212,201,32768
E 61
Mav 108,232,108
Z 81664
N 216,200,34816
E 62
Mav 108,236,156
buffer_load_dwordx4 v227, s[20:23], s64 offen offset:256 nt lds
F 84,215
Mav 112,240,100
s_lshl1_add_u32 s80, s80, s82
N 220,201,34816
E 63
Mav 112,244,148
s_lshl_b32 s38, s80, 6
)"
R"(N 224,200,36864
E 84
Mav 116,240,108
Z 89728
N 228,201,36864
F 85,215
F 86,215
Mav 116,244,156
buffer_load_dwordx4 v227, s[20:23], s64 offen offset:384 nt lds
F 87,215
Mav 120,248,100
s_mul_i32 s80, s81, 14336
N 232,200,38912
E 85
Mav 120,252,148
s_add_u32 s38, s38, s80
)"
R"(N 236,201,38912
E 86
Mav 124,248,108
Z 67584
N 240,200,40960
E 87
Mav 124,252,156
s_cbranch_vccnz .LrareB2_2
.LbackB2_2:
W 6
s_mov_b32 s64, 106496
buffer_load_dwordx4 v227, s[20:23], s64 offen nt lds
C 60,60,61
Mvvz 100,208,0
s_lshl_b32 s39, s81, 6
N 244,201,40960
)"
R"(C 61,62,63
Mvv 100,212,24
s_mul_i32 s80, s40, 6554
N 248,200,43008
C 62,84,85
C 63,86,87
Mvv 100,216,48
W 6
Z 75648
N 252,201,43008
Mvv 100,220,72
buffer_load_dwordx4 v227, s[20:23], s64 offen offset:128 nt lds
Mvvz 108,208,8
s_lshr_b32 s80, s80, 16
N 208,200,45056
Mvv 108,212,32
)"
R"(s_mul_i32 s81, s80, 10
N 212,201,45056
Mvv 108,216,56
Z 83712
N 216,200,47104
Mvv 108,220,80
buffer_load_dwordx4 v227, s[20:23], s64 offen offset:256 nt lds
Mvv 100,224,96
W 6
s_sub_u32 s81, s40, s81
N 220,201,47104
Mvv 100,228,120
s_and_b32 s82, s80, 1
Mvv 100,232,144
Z 91776
Mvv 100,236,168
)"
R"(buffer_load_dwordx4 v227, s[20:23], s64 offen offset:384 nt lds
Mvv 108,224,104
s_lshr_b32 s80, s80, 1
N 224,200,49152
Mvv 108,228,128
s_mul_i32 s40, s80, 20
N 228,201,49152
Mvv 108,232,152
Z 69632
N 232,200,51200
Mvv 108,236,176
s_mov_b32 s64, 114688
buffer_load_dwordx4 v227, s[20:23], s64 offen nt lds
v_max_f32 v217, v100, v101
Mvvz 124,240,0
W 4
)"
R"(s_add_u32 s40, s40, s82
N 236,201,51200
v_max3_f32 v217, v217, v102, v103
Mvv 124,244,24
s_lshl1_add_u32 s40, s81, s40
Mvv 124,248,48
Z 77696
Mvv 124,252,72
buffer_load_dwordx4 v227, s[20:23], s64 offen offset:128 nt lds
v_max_f32 v218, v108, v109
Mvvz 132,240,8
s_lshl_b32 s40, s40, 10
N 240,200,53248
v_max3_f32 v218, v218, v110, v111
Mvv 132,244,32
Z 85760
)"
R"(N 244,201,53248
Mvv 132,248,56
buffer_load_dwordx4 v227, s[20:23], s64 offen offset:256 nt lds
Mvv 132,252,80
s_add_u32 s80, s80, s79
N 248,200,55296
N 252,201,55296
Mvv 124,208,96
s_cmp_ge_u32 s80, 7
s_cselect_b32 s41, 7, 0
Mvv 124,212,120
Z 93824
Mvv 124,216,144
W 4
buffer_load_dwordx4 v227, s[20:23], s64 offen offset:384 nt lds
Mvv 124,220,168
)"
R"(s_sub_u32 s80, s80, s41
Mvv 132,208,104
s_lshl1_add_u32 s80, s80, s82
N 208,200,57344
Mvv 132,212,128
Z 71680
N 212,201,57344
Mvv 132,216,152
s_mov_b32 s64, 122880
buffer_load_dwordx4 v227, s[20:23], s64 offen nt lds
Mvv 132,220,176
s_lshl_b32 s41, s80, 6
N 216,200,59392
v_max3_f32 v217, v217, v124, v125
Mvvz 148,224,0
s_mul_i32 s80, s81, 14336
)"
R"(N 220,201,59392
v_max3_f32 v217, v217, v126, v127
Mvv 148,228,24
Z 79744
Mvv 148,232,48
buffer_load_dwordx4 v227, s[20:23], s64 offen offset:128 nt lds
Mvv 148,236,72
s_add_u32 s41, s41, s80
v_max3_f32 v218, v218, v132, v133
Mvvz 156,224,8
s_lshl_b32 s42, s81, 6
N 224,200,61440
v_max3_f32 v218, v218, v134, v135
Mvv 156,228,32
N 228,201,61440
Mvv 156,232,56
)"
R"(N 232,200,63488
Mvv 156,236,80
W 5
N 236,201,63488
Mvv 148,240,96
Mvv 148,244,120
Mvv 148,248,144
Mvv 148,252,168
Mvv 156,240,104
Mvv 156,244,128
Mvv 156,248,152
Mvv 156,252,176
s_nop 3
v_max3_f32 v217, v217, v148, v149
Mvvz 172,208,0
v_max3_f32 v217, v217, v150, v151
)"
R"(Mvv 172,212,24
W 0
Mvv 172,216,48
Mvv 172,220,72
s_waitcnt vmcnt(30) lgkmcnt(0)
s_barrier
T 240,196,0
v_max3_f32 v218, v218, v156, v157
Mvvz 180,208,8
Z 87808
s_nop 0
buffer_load_dwordx4 v227, s[20:23], s64 offen offset:256 nt lds
v_max3_f32 v218, v218, v158, v159
Mvv 180,212,32
T 242,196,2048
T 244,196,4096
)"
R"(Mvv 180,216,56
T 246,196,6144
T 248,197,0
Mvv 180,220,80
Z 95872
T 250,197,2048
T 252,197,4096
Mvv 172,224,96
buffer_load_dwordx4 v227, s[20:23], s64 offen offset:384 nt lds
Mvv 172,228,120
T 254,197,6144
T 208,196,512
Mvv 172,232,144
T 210,196,2560
T 212,196,4608
Mvv 172,236,168
)"
R"(Z 32768
T 214,196,6656
T 216,197,512
Mvv 180,224,104
s_mov_b32 s64, 163840
buffer_load_dwordx4 v226, s[16:19], s64 offen nt lds
Mvv 180,228,128
T 218,197,2560
T 220,197,4608
Mvv 180,232,152
T 222,197,6656
T 224,196,8192
Mvv 180,236,176
W 12
Z 34688
T 226,196,10240
)"
R"(v_max3_f32 v217, v217, v172, v173
Mav 0,240,4
buffer_load_dwordx4 v226, s[16:19], s64 offen offset:128 nt lds
v_max3_f32 v217, v217, v174, v175
Mav 0,244,52
T 228,196,12288
v_cmp_gt_f32 vcc, v217, v211
Mav 4,240,12
T 230,196,14336
T 232,197,8192
Mav 4,244,60
W 10
Z 36608
T 234,197,10240
v_max3_f32 v218, v218, v180, v181
Mav 8,248,4
)"
R"(buffer_load_dwordx4 v226, s[16:19], s64 offen offset:256 nt lds
v_max3_f32 v218, v218, v182, v183
Mav 8,252,52
T 236,197,12288
v_cmp_gt_f32 s[68:69], v218, v212
Mav 12,248,12
T 238,197,14336
s_or_b64 vcc, vcc, s[68:69]
Mav 12,252,60
Z 38528
s_cbranch_vccnz .LrareA2_3
.LbackA2_3:
T 240,196,8704
F 100,214
F 101,214
Mav 16,208,4
)"
R"(W 7
buffer_load_dwordx4 v226, s[16:19], s64 offen offset:384 nt lds
F 102,214
Mav 16,212,52
T 242,196,10752
E 100
Mav 20,208,12
T 244,196,12800
E 101
Mav 20,212,60
Z 40960
T 246,196,14848
E 102
Mav 24,216,4
s_mov_b32 s64, 172032
buffer_load_dwordx4 v226, s[16:19], s64 offen nt lds
)"
R"(F 103,214
Mav 24,220,52
T 248,197,8704
E 103
Mav 28,216,12
T 250,197,10752
F 124,214
F 125,214
Mav 28,220,60
Z 42880
T 252,197,12800
E 124
Mav 32,224,4
W 6
buffer_load_dwordx4 v226, s[16:19], s64 offen offset:128 nt lds
F 126,214
)"
R"(Mav 32,228,52
T 254,197,14848
T 208,196,16384
F 127,214
Mav 36,224,12
T 210,196,18432
E 125
Mav 36,228,60
Z 44800
T 212,196,20480
E 126
Mav 40,232,4
buffer_load_dwordx4 v226, s[16:19], s64 offen offset:256 nt lds
C 100,100,101
Mav 40,236,52
T 214,196,22528
)"
R"(T 216,197,16384
C 101,102,103
Mav 44,232,12
T 218,197,18432
E 127
Mav 44,236,60
W 8
T 220,197,20480
C 102,124,125
C 103,126,127
Mav 48,240,4
Z 46720
T 222,197,22528
F 108,215
F 109,215
Mav 48,244,52
)"
R"(buffer_load_dwordx4 v226, s[16:19], s64 offen offset:384 nt lds
F 110,215
Mav 52,240,12
T 224,196,16896
T 226,196,18944
F 111,215
Mav 52,244,60
T 228,196,20992
E 108
Mav 56,248,4
W 8
Z 49152
T 230,196,23040
E 109
Mav 56,252,52
s_mov_b32 s64, 180224
)"
R"(buffer_load_dwordx4 v226, s[16:19], s64 offen nt lds
F 132,215
Mav 60,248,12
T 232,197,16896
T 234,197,18944
F 133,215
Mav 60,252,60
T 236,197,20992
E 110
Mav 64,208,4
W 8
Z 51072
T 238,197,23040
E 111
Mav 64,212,52
buffer_load_dwordx4 v226, s[16:19], s64 offen offset:128 nt lds
)"
R"(F 134,215
Mav 68,208,12
T 240,196,24576
T 242,196,26624
F 135,215
Mav 68,212,60
T 244,196,28672
E 132
Mav 72,216,4
W 8
Z 52992
T 246,196,30720
E 133
Mav 72,220,52
buffer_load_dwordx4 v226, s[16:19], s64 offen offset:256 nt lds
C 108,108,109
)"
R"(Mav 76,216,12
T 248,197,24576
T 250,197,26624
C 109,110,111
Mav 76,220,60
T 252,197,28672
E 134
Mav 192,204,4
T 254,197,30720
E 135
Mav 192,204,52
T 208,196,25088
C 110,132,133
C 111,134,135
Mav 196,204,12
T 210,196,27136
)"
R"(F 148,214
F 149,214
Mav 196,204,60
T 212,196,29184
E 148
Mav 80,224,4
W 8
T 214,196,31232
E 149
Mav 80,228,52
T 216,197,25088
F 150,214
F 151,214
Mav 84,224,12
T 218,197,27136
E 150
)"
R"(Mav 84,228,60
T 220,197,29184
E 151
Mav 88,232,4
T 222,197,31232
F 172,214
F 173,214
Mav 88,236,52
E 172
F 174,214
Mav 92,232,12
E 173
F 175,214
Mav 92,236,60
E 174
C 148,148,149
)"
R"(Mav 96,240,4
W 5
E 175
C 149,150,151
Mav 96,244,52
C 150,172,173
C 151,174,175
F 156,215
Mav 100,240,12
E 156
F 157,215
Mav 100,244,60
s_waitcnt vmcnt(27) lgkmcnt(0)
s_barrier
N 224,198,0
F 158,215
)"
R"(Mav 104,248,4
Z 54912
s_nop 0
buffer_load_dwordx4 v226, s[16:19], s64 offen offset:384 nt lds
F 159,215
Mav 104,252,52
N 228,199,0
E 157
Mav 108,248,12
Z 57344
N 232,198,2048
E 158
Mav 108,252,60
s_mov_b32 s64, 188416
buffer_load_dwordx4 v226, s[16:19], s64 offen nt lds
F 180,215
)"
R"(Mav 112,208,4
N 236,199,2048
E 159
Mav 112,212,52
N 240,198,4096
E 180
Mav 116,208,12
Z 59264
N 244,199,4096
F 181,215
F 182,215
Mav 116,212,60
buffer_load_dwordx4 v226, s[16:19], s64 offen offset:128 nt lds
F 183,215
Mav 120,216,4
N 248,198,6144
)"
R"(E 181
Mav 120,220,52
N 252,199,6144
E 182
Mav 124,216,12
Z 61184
N 208,198,8192
E 183
Mav 124,220,60
s_cbranch_vccnz .LrareB2_3
.LbackB2_3:
W 6
buffer_load_dwordx4 v226, s[16:19], s64 offen offset:256 nt lds
C 156,156,157
Mvvz 4,224,0
N 212,199,8192
)"
R"(C 157,158,159
Mvv 4,228,24
N 216,198,10240
C 158,180,181
C 159,182,183
Mvv 4,232,48
W 6
Z 63104
N 220,199,10240
Mvv 4,236,72
buffer_load_dwordx4 v226, s[16:19], s64 offen offset:384 nt lds
Mvvz 12,224,8
N 224,198,12288
Mvv 12,228,32
N 228,199,12288
Mvv 12,232,56
)"
R"(Z 98304
N 232,198,14336
Mvv 12,236,80
s_mov_b32 s64, 131072
buffer_load_dwordx4 v227, s[20:23], s64 offen nt lds
Mvv 4,240,96
W 6
N 236,199,14336
Mvv 4,244,120
Mvv 4,248,144
Z 106368
Mvv 4,252,168
buffer_load_dwordx4 v227, s[20:23], s64 offen offset:128 nt lds
Mvv 12,240,104
N 240,198,16384
Mvv 12,244,128
)"
R"(N 244,199,16384
Mvv 12,248,152
Z 114432
N 248,198,18432
Mvv 12,252,176
buffer_load_dwordx4 v227, s[20:23], s64 offen offset:256 nt lds
v_max_f32 v217, v4, v5
Mvvz 28,208,0
W 4
N 252,199,18432
v_max3_f32 v217, v217, v6, v7
Mvv 28,212,24
Mvv 28,216,48
Z 122496
Mvv 28,220,72
buffer_load_dwordx4 v227, s[20:23], s64 offen offset:384 nt lds
)"
R"(v_max_f32 v218, v12, v13
Mvvz 36,208,8
N 208,198,20480
v_max3_f32 v218, v218, v14, v15
Mvv 36,212,32
Z 100352
N 212,199,20480
Mvv 36,216,56
s_mov_b32 s64, 139264
buffer_load_dwordx4 v227, s[20:23], s64 offen nt lds
Mvv 36,220,80
N 216,198,22528
N 220,199,22528
Mvv 28,224,96
Mvv 28,228,120
Z 108416
)"
R"(Mvv 28,232,144
W 4
buffer_load_dwordx4 v227, s[20:23], s64 offen offset:128 nt lds
Mvv 28,236,168
Mvv 36,224,104
N 224,198,24576
Mvv 36,228,128
Z 116480
N 228,199,24576
Mvv 36,232,152
buffer_load_dwordx4 v227, s[20:23], s64 offen offset:256 nt lds
Mvv 36,236,176
N 232,198,26624
v_max3_f32 v217, v217, v28, v29
Mvvz 52,240,0
N 236,199,26624
)"
R"(v_max3_f32 v217, v217, v30, v31
Mvv 52,244,24
Z 124544
Mvv 52,248,48
buffer_load_dwordx4 v227, s[20:23], s64 offen offset:384 nt lds
Mvv 52,252,72
v_max3_f32 v218, v218, v36, v37
Mvvz 60,240,8
N 240,198,28672
v_max3_f32 v218, v218, v38, v39
Mvv 60,244,32
N 244,199,28672
Mvv 60,248,56
N 248,198,30720
Mvv 60,252,80
W 5
)"
R"(N 252,199,30720
Mvv 52,208,96
Mvv 52,212,120
Mvv 52,216,144
Mvv 52,220,168
Mvv 60,208,104
Mvv 60,212,128
Mvv 60,216,152
Mvv 60,220,176
s_nop 3
v_max3_f32 v217, v217, v52, v53
Mvvz 76,224,0
v_max3_f32 v217, v217, v54, v55
Mvv 76,228,24
W 0
Mvv 76,232,48
)"
R"(Mvv 76,236,72
s_waitcnt vmcnt(24) lgkmcnt(0)
s_barrier
T 208,194,0
v_max3_f32 v218, v218, v60, v61
Mvvz 84,224,8
Z 102400
s_mov_b32 s64, 147456
buffer_load_dwordx4 v227, s[20:23], s64 offen nt lds
v_max3_f32 v218, v218, v62, v63
Mvv 84,228,32
T 210,194,2048
T 212,194,4096
Mvv 84,232,56
T 214,194,6144
T 216,195,0
)"
R"(Mvv 84,236,80
Z 110464
T 218,195,2048
T 220,195,4096
Mvv 76,240,96
buffer_load_dwordx4 v227, s[20:23], s64 offen offset:128 nt lds
Mvv 76,244,120
T 222,195,6144
T 224,194,512
Mvv 76,248,144
T 226,194,2560
T 228,194,4608
Mvv 76,252,168
Z 118528
T 230,194,6656
T 232,195,512
)"
R"(Mvv 84,240,104
buffer_load_dwordx4 v227, s[20:23], s64 offen offset:256 nt lds
Mvv 84,244,128
T 234,195,2560
T 236,195,4608
Mvv 84,248,152
T 238,195,6656
T 240,194,8192
Mvv 84,252,176
W 12
Z 126592
T 242,194,10240
v_max3_f32 v217, v217, v76, v77
Mav 0,208,100
buffer_load_dwordx4 v227, s[20:23], s64 offen offset:384 nt lds
v_max3_f32 v217, v217, v78, v79
)"
R"(Mav 0,212,148
T 244,194,12288
v_cmp_gt_f32 vcc, v217, v211
Mav 4,208,108
T 246,194,14336
T 248,195,8192
Mav 4,212,156
W 10
Z 104448
T 250,195,10240
v_max3_f32 v218, v218, v84, v85
Mav 8,216,100
s_mov_b32 s64, 155648
buffer_load_dwordx4 v227, s[20:23], s64 offen nt lds
v_max3_f32 v218, v218, v86, v87
Mav 8,220,148
)"
R"(T 252,195,12288
v_cmp_gt_f32 s[68:69], v218, v212
Mav 12,216,108
T 254,195,14336
s_or_b64 vcc, vcc, s[68:69]
Mav 12,220,156
Z 112512
s_cbranch_vccnz .LrareA2_4
.LbackA2_4:
T 208,194,8704
F 4,214
F 5,214
Mav 16,224,100
W 7
buffer_load_dwordx4 v227, s[20:23], s64 offen offset:128 nt lds
F 6,214
)"
R"(Mav 16,228,148
T 210,194,10752
E 4
Mav 20,224,108
T 212,194,12800
E 5
Mav 20,228,156
Z 120576
T 214,194,14848
E 6
Mav 24,232,100
buffer_load_dwordx4 v227, s[20:23], s64 offen offset:256 nt lds
F 7,214
Mav 24,236,148
T 216,195,8704
E 7
)"
R"(Mav 28,232,108
T 218,195,10752
F 28,214
F 29,214
Mav 28,236,156
Z 128640
T 220,195,12800
E 28
Mav 32,240,100
W 6
buffer_load_dwordx4 v227, s[20:23], s64 offen offset:384 nt lds
F 30,214
Mav 32,244,148
T 222,195,14848
T 224,194,16384
F 31,214
)"
R"(Mav 36,240,108
T 226,194,18432
E 29
Mav 36,244,156
Z 131072
T 228,194,20480
E 30
Mav 40,248,100
s_mov_b32 s64, 196608
buffer_load_dwordx4 v226, s[16:19], s64 offen nt lds
C 4,4,5
Mav 40,252,148
T 230,194,22528
T 232,195,16384
C 5,6,7
Mav 44,248,108
)"
R"(T 234,195,18432
E 31
Mav 44,252,156
W 8
T 236,195,20480
C 6,28,29
C 7,30,31
Mav 48,208,100
Z 132992
T 238,195,22528
F 12,215
F 13,215
Mav 48,212,148
buffer_load_dwordx4 v226, s[16:19], s64 offen offset:128 nt lds
F 14,215
Mav 52,208,108
)"
R"(T 240,194,16896
T 242,194,18944
F 15,215
Mav 52,212,156
T 244,194,20992
E 12
Mav 56,216,100
W 8
Z 134912
T 246,194,23040
E 13
Mav 56,220,148
buffer_load_dwordx4 v226, s[16:19], s64 offen offset:256 nt lds
F 36,215
Mav 60,216,108
T 248,195,16896
)"
R"(T 250,195,18944
F 37,215
Mav 60,220,156
T 252,195,20992
E 14
Mav 64,224,100
W 8
Z 136832
T 254,195,23040
E 15
Mav 64,228,148
buffer_load_dwordx4 v226, s[16:19], s64 offen offset:384 nt lds
F 38,215
Mav 68,224,108
T 208,194,24576
T 210,194,26624
)"
R"(F 39,215
Mav 68,228,156
T 212,194,28672
E 36
Mav 72,232,100
W 8
Z 139264
T 214,194,30720
E 37
Mav 72,236,148
s_mov_b32 s64, 204800
buffer_load_dwordx4 v226, s[16:19], s64 offen nt lds
C 12,12,13
Mav 76,232,108
T 216,195,24576
T 218,195,26624
)"
R"(C 13,14,15
Mav 76,236,156
T 220,195,28672
E 38
Mav 192,204,100
T 222,195,30720
E 39
Mav 192,204,148
T 224,194,25088
C 14,36,37
C 15,38,39
Mav 196,204,108
T 226,194,27136
F 52,214
F 53,214
Mav 196,204,156
)"
R"(T 228,194,29184
E 52
Mav 80,240,100
W 8
T 230,194,31232
E 53
Mav 80,244,148
T 232,195,25088
F 54,214
F 55,214
Mav 84,240,108
T 234,195,27136
E 54
Mav 84,244,156
T 236,195,29184
E 55
)"
R"(Mav 88,248,100
T 238,195,31232
F 76,214
F 77,214
Mav 88,252,148
E 76
F 78,214
Mav 92,248,108
E 77
F 79,214
Mav 92,252,156
E 78
C 52,52,53
Mav 96,208,100
W 5
E 79
)"
R"(C 53,54,55
Mav 96,212,148
C 54,76,77
C 55,78,79
F 60,215
Mav 100,208,108
E 60
F 61,215
Mav 100,212,156
s_waitcnt vmcnt(21) lgkmcnt(0)
s_barrier
N 240,198,32768
F 62,215
Mav 104,216,100
Z 141184
s_nop 0
)"
R"(buffer_load_dwordx4 v226, s[16:19], s64 offen offset:128 nt lds
F 63,215
Mav 104,220,148
N 244,199,32768
E 61
Mav 108,216,108
Z 143104
N 248,198,34816
E 62
Mav 108,220,156
buffer_load_dwordx4 v226, s[16:19], s64 offen offset:256 nt lds
F 84,215
Mav 112,224,100
N 252,199,34816
E 63
Mav 112,228,148
)"
R"(N 208,198,36864
E 84
Mav 116,224,108
Z 145024
N 212,199,36864
F 85,215
F 86,215
Mav 116,228,156
buffer_load_dwordx4 v226, s[16:19], s64 offen offset:384 nt lds
F 87,215
Mav 120,232,100
N 216,198,38912
E 85
Mav 120,236,148
N 220,199,38912
E 86
)"
R"(Mav 124,232,108
Z 147456
N 224,198,40960
E 87
Mav 124,236,156
s_cbranch_vccnz .LrareB2_4
.LbackB2_4:
W 6
s_mov_b32 s64, 212992
buffer_load_dwordx4 v226, s[16:19], s64 offen nt lds
C 60,60,61
Mvvz 100,240,0
N 228,199,40960
C 61,62,63
Mvv 100,244,24
N 232,198,43008
)"
R"(C 62,84,85
C 63,86,87
Mvv 100,248,48
W 6
Z 149376
N 236,199,43008
Mvv 100,252,72
buffer_load_dwordx4 v226, s[16:19], s64 offen offset:128 nt lds
Mvvz 108,240,8
N 240,198,45056
Mvv 108,244,32
N 244,199,45056
Mvv 108,248,56
Z 151296
N 248,198,47104
Mvv 108,252,80
)"
R"(buffer_load_dwordx4 v226, s[16:19], s64 offen offset:256 nt lds
Mvv 100,208,96
W 6
N 252,199,47104
Mvv 100,212,120
Mvv 100,216,144
Z 153216
Mvv 100,220,168
buffer_load_dwordx4 v226, s[16:19], s64 offen offset:384 nt lds
Mvv 108,208,104
N 208,198,49152
Mvv 108,212,128
N 212,199,49152
Mvv 108,216,152
Z 155648
N 216,198,51200
)"
R"(Mvv 108,220,176
s_mov_b32 s64, 221184
buffer_load_dwordx4 v226, s[16:19], s64 offen nt lds
v_max_f32 v217, v100, v101
Mvvz 124,224,0
W 4
N 220,199,51200
v_max3_f32 v217, v217, v102, v103
Mvv 124,228,24
Mvv 124,232,48
Z 157568
Mvv 124,236,72
buffer_load_dwordx4 v226, s[16:19], s64 offen offset:128 nt lds
v_max_f32 v218, v108, v109
Mvvz 132,224,8
N 224,198,53248
)"
R"(v_max3_f32 v218, v218, v110, v111
Mvv 132,228,32
Z 159488
N 228,199,53248
Mvv 132,232,56
buffer_load_dwordx4 v226, s[16:19], s64 offen offset:256 nt lds
Mvv 132,236,80
N 232,198,55296
N 236,199,55296
Mvv 124,240,96
Mvv 124,244,120
Z 161408
Mvv 124,248,144
W 4
buffer_load_dwordx4 v226, s[16:19], s64 offen offset:384 nt lds
Mvv 124,252,168
)"
R"(Mvv 132,240,104
N 240,198,57344
Mvv 132,244,128
Z 0
N 244,199,57344
Mvv 132,248,152
s_mov_b32 s64, 163840
buffer_load_dwordx4 v227, s[20:23], s64 offen nt lds
Mvv 132,252,176
N 248,198,59392
v_max3_f32 v217, v217, v124, v125
Mvvz 148,208,0
N 252,199,59392
v_max3_f32 v217, v217, v126, v127
Mvv 148,212,24
Z 8064
)"
R"(Mvv 148,216,48
buffer_load_dwordx4 v227, s[20:23], s64 offen offset:128 nt lds
Mvv 148,220,72
v_max3_f32 v218, v218, v132, v133
Mvvz 156,208,8
N 208,198,61440
v_max3_f32 v218, v218, v134, v135
Mvv 156,212,32
N 212,199,61440
Mvv 156,216,56
N 216,198,63488
Mvv 156,220,80
W 5
N 220,199,63488
Mvv 148,224,96
Mvv 148,228,120
)"
R"(Mvv 148,232,144
Mvv 148,236,168
Mvv 156,224,104
Mvv 156,228,128
Mvv 156,232,152
Mvv 156,236,176
s_nop 3
v_max3_f32 v217, v217, v148, v149
Mvvz 172,240,0
v_max3_f32 v217, v217, v150, v151
Mvv 172,244,24
W 0
Mvv 172,248,48
Mvv 172,252,72
s_waitcnt vmcnt(18) lgkmcnt(0)
s_barrier
)"
R"(T 224,194,32768
v_max3_f32 v218, v218, v156, v157
Mvvz 180,240,8
Z 16128
s_nop 0
buffer_load_dwordx4 v227, s[20:23], s64 offen offset:256 nt lds
v_max3_f32 v218, v218, v158, v159
Mvv 180,244,32
T 226,194,34816
T 228,194,36864
Mvv 180,248,56
T 230,194,38912
T 232,195,32768
Mvv 180,252,80
Z 24192
T 234,195,34816
)"
R"(T 236,195,36864
Mvv 172,208,96
buffer_load_dwordx4 v227, s[20:23], s64 offen offset:384 nt lds
Mvv 172,212,120
T 238,195,38912
T 240,194,33280
Mvv 172,216,144
T 242,194,35328
T 244,194,37376
Mvv 172,220,168
Z 2048
T 246,194,39424
T 248,195,33280
Mvv 180,208,104
s_mov_b32 s64, 172032
buffer_load_dwordx4 v227, s[20:23], s64 offen nt lds
)"
R"(Mvv 180,212,128
T 250,195,35328
T 252,195,37376
Mvv 180,216,152
T 254,195,39424
T 208,194,40960
Mvv 180,220,176
W 12
Z 10112
T 210,194,43008
v_max3_f32 v217, v217, v172, v173
Mav 0,224,4
buffer_load_dwordx4 v227, s[20:23], s64 offen offset:128 nt lds
v_max3_f32 v217, v217, v174, v175
Mav 0,228,52
T 212,194,45056
)"
R"(v_cmp_gt_f32 vcc, v217, v211
Mav 4,224,12
T 214,194,47104
T 216,195,40960
Mav 4,228,60
W 10
Z 18176
T 218,195,43008
v_max3_f32 v218, v218, v180, v181
Mav 8,232,4
buffer_load_dwordx4 v227, s[20:23], s64 offen offset:256 nt lds
v_max3_f32 v218, v218, v182, v183
Mav 8,236,52
T 220,195,45056
v_cmp_gt_f32 s[68:69], v218, v212
Mav 12,232,12
)"
R"(T 222,195,47104
s_or_b64 vcc, vcc, s[68:69]
Mav 12,236,60
Z 26240
s_cbranch_vccnz .LrareA2_5
.LbackA2_5:
T 224,194,41472
F 100,214
F 101,214
Mav 16,240,4
W 7
buffer_load_dwordx4 v227, s[20:23], s64 offen offset:384 nt lds
F 102,214
Mav 16,244,52
T 226,194,43520
E 100
)"
R"(Mav 20,240,12
T 228,194,45568
E 101
Mav 20,244,60
Z 4096
T 230,194,47616
E 102
Mav 24,248,4
s_mov_b32 s64, 180224
buffer_load_dwordx4 v227, s[20:23], s64 offen nt lds
F 103,214
Mav 24,252,52
T 232,195,41472
E 103
Mav 28,248,12
T 234,195,43520
)"
R"(F 124,214
F 125,214
Mav 28,252,60
Z 12160
T 236,195,45568
E 124
Mav 32,208,4
W 6
buffer_load_dwordx4 v227, s[20:23], s64 offen offset:128 nt lds
F 126,214
Mav 32,212,52
T 238,195,47616
T 240,194,49152
F 127,214
Mav 36,208,12
T 242,194,51200
)"
R"(E 125
Mav 36,212,60
Z 20224
T 244,194,53248
E 126
Mav 40,216,4
buffer_load_dwordx4 v227, s[20:23], s64 offen offset:256 nt lds
C 100,100,101
Mav 40,220,52
T 246,194,55296
T 248,195,49152
C 101,102,103
Mav 44,216,12
T 250,195,51200
E 127
Mav 44,220,60
)"
R"(W 8
T 252,195,53248
C 102,124,125
C 103,126,127
Mav 48,224,4
Z 28288
T 254,195,55296
F 108,215
F 109,215
Mav 48,228,52
buffer_load_dwordx4 v227, s[20:23], s64 offen offset:384 nt lds
F 110,215
Mav 52,224,12
T 208,194,49664
T 210,194,51712
F 111,215
)"
R"(Mav 52,228,60
T 212,194,53760
E 108
Mav 56,232,4
W 8
Z 6144
T 214,194,55808
E 109
Mav 56,236,52
s_mov_b32 s64, 188416
buffer_load_dwordx4 v227, s[20:23], s64 offen nt lds
F 132,215
Mav 60,232,12
T 216,195,49664
T 218,195,51712
F 133,215
)"
R"(Mav 60,236,60
T 220,195,53760
E 110
Mav 64,240,4
W 8
Z 14208
T 222,195,55808
E 111
Mav 64,244,52
buffer_load_dwordx4 v227, s[20:23], s64 offen offset:128 nt lds
F 134,215
Mav 68,240,12
T 224,194,57344
T 226,194,59392
F 135,215
Mav 68,244,60
)"
R"(T 228,194,61440
E 132
Mav 72,248,4
W 8
Z 22272
T 230,194,63488
E 133
Mav 72,252,52
buffer_load_dwordx4 v227, s[20:23], s64 offen offset:256 nt lds
C 108,108,109
Mav 76,248,12
T 232,195,57344
T 234,195,59392
C 109,110,111
Mav 76,252,60
T 236,195,61440
)"
R"(E 134
Mav 192,204,4
T 238,195,63488
E 135
Mav 192,204,52
T 240,194,57856
C 110,132,133
C 111,134,135
Mav 196,204,12
T 242,194,59904
F 148,214
F 149,214
Mav 196,204,60
T 244,194,61952
E 148
Mav 80,208,4
)"
R"(W 8
T 246,194,64000
E 149
Mav 80,212,52
T 248,195,57856
F 150,214
F 151,214
Mav 84,208,12
T 250,195,59904
E 150
Mav 84,212,60
T 252,195,61952
E 151
Mav 88,216,4
T 254,195,64000
F 172,214
)"
R"(F 173,214
Mav 88,220,52
E 172
F 174,214
Mav 92,216,12
E 173
F 175,214
Mav 92,220,60
E 174
C 148,148,149
Mav 96,224,4
W 5
E 175
C 149,150,151
Mav 96,228,52
C 150,172,173
)"
R"(C 151,174,175
F 156,215
Mav 100,224,12
E 156
F 157,215
Mav 100,228,60
s_waitcnt vmcnt(15) lgkmcnt(0)
s_barrier
N 208,202,0
F 158,215
Mav 104,232,4
Z 30336
s_nop 0
buffer_load_dwordx4 v227, s[20:23], s64 offen offset:384 nt lds
F 159,215
Mav 104,236,52
)"
R"(N 212,203,0
E 157
Mav 108,232,12
Z 65536
N 216,202,2048
E 158
Mav 108,236,60
s_mov_b32 s64, 229376
buffer_load_dwordx4 v226, s[16:19], s64 offen nt lds
F 180,215
Mav 112,240,4
N 220,203,2048
E 159
Mav 112,244,52
N 224,202,4096
E 180
)"
R"(Mav 116,240,12
Z 67456
N 228,203,4096
F 181,215
F 182,215
Mav 116,244,60
buffer_load_dwordx4 v226, s[16:19], s64 offen offset:128 nt lds
F 183,215
Mav 120,248,4
N 232,202,6144
E 181
Mav 120,252,52
N 236,203,6144
E 182
Mav 124,248,12
Z 69376
)"
R"(N 240,202,8192
E 183
Mav 124,252,60
s_cbranch_vccnz .LrareB2_5
.LbackB2_5:
W 6
buffer_load_dwordx4 v226, s[16:19], s64 offen offset:256 nt lds
C 156,156,157
Mvvz 4,208,0
N 244,203,8192
C 157,158,159
Mvv 4,212,24
N 248,202,10240
C 158,180,181
C 159,182,183
Mvv 4,216,48
)"
R"(W 6
Z 71296
N 252,203,10240
Mvv 4,220,72
buffer_load_dwordx4 v226, s[16:19], s64 offen offset:384 nt lds
Mvvz 12,208,8
N 208,202,12288
Mvv 12,212,32
N 212,203,12288
Mvv 12,216,56
Z 73728
N 216,202,14336
Mvv 12,220,80
s_mov_b32 s64, 237568
buffer_load_dwordx4 v226, s[16:19], s64 offen nt lds
Mvv 4,224,96
)"
R"(W 6
N 220,203,14336
Mvv 4,228,120
Mvv 4,232,144
Z 75648
Mvv 4,236,168
buffer_load_dwordx4 v226, s[16:19], s64 offen offset:128 nt lds
Mvv 12,224,104
N 224,202,16384
Mvv 12,228,128
N 228,203,16384
Mvv 12,232,152
Z 77568
N 232,202,18432
Mvv 12,236,176
buffer_load_dwordx4 v226, s[16:19], s64 offen offset:256 nt lds
)"
R"(v_max_f32 v217, v4, v5
Mvvz 28,240,0
W 4
N 236,203,18432
v_max3_f32 v217, v217, v6, v7
Mvv 28,244,24
Mvv 28,248,48
Z 79488
Mvv 28,252,72
buffer_load_dwordx4 v226, s[16:19], s64 offen offset:384 nt lds
v_max_f32 v218, v12, v13
Mvvz 36,240,8
N 240,202,20480
v_max3_f32 v218, v218, v14, v15
Mvv 36,244,32
Z 81920
)"
R"(N 244,203,20480
Mvv 36,248,56
s_mov_b32 s64, 245760
buffer_load_dwordx4 v226, s[16:19], s64 offen nt lds
Mvv 36,252,80
N 248,202,22528
N 252,203,22528
Mvv 28,208,96
Mvv 28,212,120
Z 83840
Mvv 28,216,144
W 4
buffer_load_dwordx4 v226, s[16:19], s64 offen offset:128 nt lds
Mvv 28,220,168
Mvv 36,208,104
N 208,202,24576
)"
R"(Mvv 36,212,128
Z 85760
N 212,203,24576
Mvv 36,216,152
buffer_load_dwordx4 v226, s[16:19], s64 offen offset:256 nt lds
Mvv 36,220,176
N 216,202,26624
v_max3_f32 v217, v217, v28, v29
Mvvz 52,224,0
N 220,203,26624
v_max3_f32 v217, v217, v30, v31
Mvv 52,228,24
Z 87680
Mvv 52,232,48
buffer_load_dwordx4 v226, s[16:19], s64 offen offset:384 nt lds
Mvv 52,236,72
)"
R"(v_max3_f32 v218, v218, v36, v37
Mvvz 60,224,8
N 224,202,28672
v_max3_f32 v218, v218, v38, v39
Mvv 60,228,32
N 228,203,28672
Mvv 60,232,56
N 232,202,30720
Mvv 60,236,80
W 5
N 236,203,30720
Mvv 52,240,96
Mvv 52,244,120
Mvv 52,248,144
Mvv 52,252,168
Mvv 60,240,104
)"
R"(Mvv 60,244,128
Mvv 60,248,152
Mvv 60,252,176
s_nop 3
v_max3_f32 v217, v217, v52, v53
Mvvz 76,208,0
v_max3_f32 v217, v217, v54, v55
Mvv 76,212,24
W 0
Mvv 76,216,48
Mvv 76,220,72
s_waitcnt vmcnt(12) lgkmcnt(0)
s_barrier
T 240,192,0
v_max3_f32 v218, v218, v60, v61
Mvvz 84,208,8
)"
R"(Z 90112
s_mov_b32 s64, 253952
buffer_load_dwordx4 v226, s[16:19], s64 offen nt lds
v_max3_f32 v218, v218, v62, v63
Mvv 84,212,32
T 242,192,2048
T 244,192,4096
Mvv 84,216,56
T 246,192,6144
T 248,193,0
Mvv 84,220,80
Z 92032
T 250,193,2048
T 252,193,4096
Mvv 76,224,96
buffer_load_dwordx4 v226, s[16:19], s64 offen offset:128 nt lds
)"
R"(Mvv 76,228,120
T 254,193,6144
T 208,192,512
Mvv 76,232,144
T 210,192,2560
T 212,192,4608
Mvv 76,236,168
Z 93952
T 214,192,6656
T 216,193,512
Mvv 84,224,104
buffer_load_dwordx4 v226, s[16:19], s64 offen offset:256 nt lds
Mvv 84,228,128
T 218,193,2560
T 220,193,4608
Mvv 84,232,152
)"
R"(T 222,193,6656
T 224,192,8192
Mvv 84,236,176
W 12
Z 95872
T 226,192,10240
v_max3_f32 v217, v217, v76, v77
Mav 0,240,100
buffer_load_dwordx4 v226, s[16:19], s64 offen offset:384 nt lds
v_max3_f32 v217, v217, v78, v79
Mav 0,244,148
T 228,192,12288
v_cmp_gt_f32 vcc, v217, v211
Mav 4,240,108
T 230,192,14336
T 232,193,8192
)"
R"(Mav 4,244,156
W 10
Z 32768
T 234,193,10240
v_max3_f32 v218, v218, v84, v85
Mav 8,248,100
s_mov_b32 s64, 196608
buffer_load_dwordx4 v227, s[20:23], s64 offen nt lds
v_max3_f32 v218, v218, v86, v87
Mav 8,252,148
T 236,193,12288
v_cmp_gt_f32 s[68:69], v218, v212
Mav 12,248,108
T 238,193,14336
s_or_b64 vcc, vcc, s[68:69]
Mav 12,252,156
)"
R"(Z 40832
s_cbranch_vccnz .LrareA2_6
.LbackA2_6:
T 240,192,8704
F 4,214
F 5,214
Mav 16,208,100
W 7
buffer_load_dwordx4 v227, s[20:23], s64 offen offset:128 nt lds
F 6,214
Mav 16,212,148
T 242,192,10752
E 4
Mav 20,208,108
T 244,192,12800
E 5
)"
R"(Mav 20,212,156
Z 48896
T 246,192,14848
E 6
Mav 24,216,100
buffer_load_dwordx4 v227, s[20:23], s64 offen offset:256 nt lds
F 7,214
Mav 24,220,148
T 248,193,8704
E 7
Mav 28,216,108
T 250,193,10752
F 28,214
F 29,214
Mav 28,220,156
Z 56960
)"
R"(T 252,193,12800
E 28
Mav 32,224,100
W 6
buffer_load_dwordx4 v227, s[20:23], s64 offen offset:384 nt lds
F 30,214
Mav 32,228,148
T 254,193,14848
T 208,192,16384
F 31,214
Mav 36,224,108
T 210,192,18432
E 29
Mav 36,228,156
Z 34816
T 212,192,20480
)"
R"(E 30
Mav 40,232,100
s_mov_b32 s64, 204800
buffer_load_dwordx4 v227, s[20:23], s64 offen nt lds
C 4,4,5
Mav 40,236,148
T 214,192,22528
T 216,193,16384
C 5,6,7
Mav 44,232,108
T 218,193,18432
E 31
Mav 44,236,156
W 8
T 220,193,20480
C 6,28,29
)"
R"(C 7,30,31
Mav 48,240,100
Z 42880
T 222,193,22528
F 12,215
F 13,215
Mav 48,244,148
buffer_load_dwordx4 v227, s[20:23], s64 offen offset:128 nt lds
F 14,215
Mav 52,240,108
T 224,192,16896
T 226,192,18944
F 15,215
Mav 52,244,156
T 228,192,20992
E 12
)"
R"(Mav 56,248,100
W 8
Z 50944
T 230,192,23040
E 13
Mav 56,252,148
buffer_load_dwordx4 v227, s[20:23], s64 offen offset:256 nt lds
F 36,215
Mav 60,248,108
T 232,193,16896
T 234,193,18944
F 37,215
Mav 60,252,156
T 236,193,20992
E 14
Mav 64,208,100
)"
R"(W 8
Z 59008
T 238,193,23040
E 15
Mav 64,212,148
buffer_load_dwordx4 v227, s[20:23], s64 offen offset:384 nt lds
F 38,215
Mav 68,208,108
T 240,192,24576
T 242,192,26624
F 39,215
Mav 68,212,156
T 244,192,28672
E 36
Mav 72,216,100
W 8
)"
R"(Z 36864
T 246,192,30720
E 37
Mav 72,220,148
s_mov_b32 s64, 212992
buffer_load_dwordx4 v227, s[20:23], s64 offen nt lds
C 12,12,13
Mav 76,216,108
T 248,193,24576
T 250,193,26624
C 13,14,15
Mav 76,220,156
T 252,193,28672
E 38
Mav 192,204,100
T 254,193,30720
)"
R"(E 39
Mav 192,204,148
T 208,192,25088
C 14,36,37
C 15,38,39
Mav 196,204,108
T 210,192,27136
F 52,214
F 53,214
Mav 196,204,156
T 212,192,29184
E 52
Mav 80,224,100
W 8
T 214,192,31232
E 53
)"
R"(Mav 80,228,148
T 216,193,25088
F 54,214
F 55,214
Mav 84,224,108
T 218,193,27136
E 54
Mav 84,228,156
T 220,193,29184
E 55
Mav 88,232,100
T 222,193,31232
F 76,214
F 77,214
Mav 88,236,148
E 76
)"
R"(F 78,214
Mav 92,232,108
E 77
F 79,214
Mav 92,236,156
E 78
C 52,52,53
Mav 96,240,100
W 5
E 79
C 53,54,55
Mav 96,244,148
C 54,76,77
C 55,78,79
F 60,215
Mav 100,240,108
)"
R"(E 60
F 61,215
Mav 100,244,156
s_waitcnt vmcnt(9) lgkmcnt(0)
s_barrier
N 224,200,0
F 62,215
Mav 104,248,100
Z 44928
s_nop 0
buffer_load_dwordx4 v227, s[20:23], s64 offen offset:128 nt lds
F 63,215
Mav 104,252,148
N 228,201,0
E 61
Mav 108,248,108
)"
R"(Z 52992
N 232,200,2048
E 62
Mav 108,252,156
buffer_load_dwordx4 v227, s[20:23], s64 offen offset:256 nt lds
F 84,215
Mav 112,208,100
N 236,201,2048
E 63
Mav 112,212,148
Z 61056
N 240,200,4096
E 84
Mav 116,208,108
buffer_load_dwordx4 v227, s[20:23], s64 offen offset:384 nt lds
F 85,215
)"
R"(Mav 116,212,156
N 244,201,4096
E 85
Mav 120,216,100
Z 38912
N 248,200,6144
F 86,215
F 87,215
Mav 120,220,148
s_mov_b32 s64, 221184
buffer_load_dwordx4 v227, s[20:23], s64 offen nt lds
C 60,60,61
Mav 124,216,108
N 252,201,6144
E 86
Mav 124,220,156
)"
R"(Z 46976
s_cbranch_vccnz .LrareB2_6
.LbackB2_6:
W 5
N 208,200,8192
E 87
Mvvz 100,224,0
buffer_load_dwordx4 v227, s[20:23], s64 offen offset:128 nt lds
C 61,62,63
Mvv 100,228,24
N 212,201,8192
C 62,84,85
C 63,86,87
Mvv 100,232,48
W 5
Z 55040
)"
R"(N 216,200,10240
N 220,201,10240
Mvv 100,236,72
buffer_load_dwordx4 v227, s[20:23], s64 offen offset:256 nt lds
Mvvz 108,224,8
N 224,200,12288
Mvv 108,228,32
Z 63104
N 228,201,12288
Mvv 108,232,56
buffer_load_dwordx4 v227, s[20:23], s64 offen offset:384 nt lds
Mvv 108,236,80
N 232,200,14336
N 236,201,14336
Mvv 100,240,96
W 7
)"
R"(Mvv 100,244,120
Z 98304
Mvv 100,248,144
s_mov_b32 s64, 229376
buffer_load_dwordx4 v227, s[20:23], s64 offen nt lds
Mvv 100,252,168
Mvv 108,240,104
Z 106368
N 240,200,16384
Mvv 108,244,128
buffer_load_dwordx4 v227, s[20:23], s64 offen offset:128 nt lds
Mvv 108,248,152
N 244,201,16384
N 248,200,18432
Mvv 108,252,176
Z 114432
)"
R"(N 252,201,18432
v_max_f32 v217, v100, v101
Mvvz 124,208,0
W 4
buffer_load_dwordx4 v227, s[20:23], s64 offen offset:256 nt lds
v_max3_f32 v217, v217, v102, v103
Mvv 124,212,24
Mvv 124,216,48
Z 122496
Mvv 124,220,72
buffer_load_dwordx4 v227, s[20:23], s64 offen offset:384 nt lds
v_max_f32 v218, v108, v109
Mvvz 132,208,8
N 208,200,20480
v_max3_f32 v218, v218, v110, v111
Mvv 132,212,32
)"
R"(Z 100352
N 212,201,20480
Mvv 132,216,56
s_mov_b32 s64, 237568
buffer_load_dwordx4 v227, s[20:23], s64 offen nt lds
Mvv 132,220,80
N 216,200,22528
N 220,201,22528
Mvv 124,224,96
Z 108416
Mvv 124,228,120
buffer_load_dwordx4 v227, s[20:23], s64 offen offset:128 nt lds
Mvv 124,232,144
Mvv 124,236,168
Z 116480
Mvv 132,224,104
)"
R"(buffer_load_dwordx4 v227, s[20:23], s64 offen offset:256 nt lds
Mvv 132,228,128
N 224,200,24576
N 228,201,24576
Mvv 132,232,152
N 232,200,26624
Mvv 132,236,176
W 5
Z 124544
N 236,201,26624
v_max3_f32 v217, v217, v124, v125
Mvvz 148,240,0
buffer_load_dwordx4 v227, s[20:23], s64 offen offset:384 nt lds
v_max3_f32 v217, v217, v126, v127
Mvv 148,244,24
Mvv 148,248,48
)"
R"(Z 102400
Mvv 148,252,72
s_mov_b32 s64, 245760
buffer_load_dwordx4 v227, s[20:23], s64 offen nt lds
v_max3_f32 v218, v218, v132, v133
Mvvz 156,240,8
N 240,200,28672
v_max3_f32 v218, v218, v134, v135
Mvv 156,244,32
N 244,201,28672
Mvv 156,248,56
N 248,200,30720
Mvv 156,252,80
N 252,201,30720
Mvv 148,208,96
Mvv 148,212,120
)"
R"(W 4
Mvv 148,216,144
Mvv 148,220,168
Mvv 156,208,104
Mvv 156,212,128
Mvv 156,216,152
Mvv 156,220,176
s_nop 3
v_max3_f32 v217, v217, v148, v149
Mvvz 172,224,0
v_max3_f32 v217, v217, v150, v151
Mvv 172,228,24
Mvv 172,232,48
Mvv 172,236,72
s_waitcnt vmcnt(9) lgkmcnt(0)
s_barrier
)"
R"(T 208,192,32768
v_max3_f32 v218, v218, v156, v157
Mvvz 180,224,8
Z 110464
s_nop 0
buffer_load_dwordx4 v227, s[20:23], s64 offen offset:128 nt lds
v_max3_f32 v218, v218, v158, v159
Mvv 180,228,32
T 210,192,34816
T 212,192,36864
Mvv 180,232,56
Z 118528
T 214,192,38912
T 216,193,32768
Mvv 180,236,80
buffer_load_dwordx4 v227, s[20:23], s64 offen offset:256 nt lds
)"
R"(Mvv 172,240,96
T 218,193,34816
T 220,193,36864
Mvv 172,244,120
T 222,193,38912
T 224,192,33280
Mvv 172,248,144
Z 126592
T 226,192,35328
T 228,192,37376
Mvv 172,252,168
buffer_load_dwordx4 v227, s[20:23], s64 offen offset:384 nt lds
Mvv 180,240,104
T 230,192,39424
T 232,193,33280
Mvv 180,244,128
)"
R"(T 234,193,35328
T 236,193,37376
Mvv 180,248,152
Z 104448
T 238,193,39424
T 240,192,40960
Mvv 180,252,176
W 12
s_mov_b32 s64, 253952
buffer_load_dwordx4 v227, s[20:23], s64 offen nt lds
v_max3_f32 v217, v217, v172, v173
Mav 0,208,4
T 242,192,43008
v_max3_f32 v217, v217, v174, v175
Mav 0,212,52
T 244,192,45056
)"
R"(v_cmp_gt_f32 vcc, v217, v211
Mav 4,208,12
Z 112512
T 246,192,47104
T 248,193,40960
Mav 4,212,60
W 10
buffer_load_dwordx4 v227, s[20:23], s64 offen offset:128 nt lds
v_max3_f32 v218, v218, v180, v181
Mav 8,216,4
T 250,193,43008
v_max3_f32 v218, v218, v182, v183
Mav 8,220,52
T 252,193,45056
v_cmp_gt_f32 s[68:69], v218, v212
Mav 12,216,12
)"
R"(Z 120576
T 254,193,47104
s_or_b64 vcc, vcc, s[68:69]
Mav 12,220,60
s_cbranch_vccnz .LrareA2_7
.LbackA2_7:
buffer_load_dwordx4 v227, s[20:23], s64 offen offset:256 nt lds
F 100,214
Mav 16,224,4
W 7
T 208,192,41472
E 100
Mav 16,228,52
T 210,192,43520
F 101,214
F 102,214
)"
R"(Mav 20,224,12
Z 128640
T 212,192,45568
E 101
Mav 20,228,60
buffer_load_dwordx4 v227, s[20:23], s64 offen offset:384 nt lds
F 103,214
Mav 24,232,4
T 214,192,47616
E 102
Mav 24,236,52
T 216,193,41472
E 103
Mav 28,232,12
Z 131072
T 218,193,43520
)"
R"(F 124,214
F 125,214
Mav 28,236,60
W 6
buffer_load_dwordx4 v229, s[24:27], s48 offen lds
F 126,214
Mav 32,240,4
T 220,193,45568
E 124
Mav 32,244,52
T 222,193,47616
E 125
Mav 36,240,12
Z 133120
T 224,192,49152
E 126
)"
R"(Mav 36,244,60
s_add_u32 s64, s48, 14336
buffer_load_dwordx4 v229, s[24:27], s64 offen lds
F 127,214
Mav 40,248,4
T 226,192,51200
E 127
Mav 40,252,52
T 228,192,53248
C 100,100,101
F 108,215
Mav 44,248,12
Z 135168
T 230,192,55296
E 108
Mav 44,252,60
)"
R"(W 6
s_add_u32 s64, s48, 28672
buffer_load_dwordx4 v229, s[24:27], s64 offen lds
C 101,102,103
Mav 48,208,4
T 232,193,49152
T 234,193,51200
C 102,124,125
Mav 48,212,52
T 236,193,53248
C 103,126,127
F 109,215
Mav 52,208,12
Z 137216
T 238,193,55296
E 109
)"
R"(Mav 52,212,60
s_add_u32 s64, s48, 43008
buffer_load_dwordx4 v229, s[24:27], s64 offen lds
F 110,215
Mav 56,216,4
W 7
T 240,192,49664
T 242,192,51712
F 111,215
Mav 56,220,52
T 244,192,53760
E 110
Mav 60,216,12
Z 139264
T 246,192,55808
E 111
)"
R"(Mav 60,220,60
W 8
s_add_u32 s64, s48, 57344
buffer_load_dwordx4 v229, s[24:27], s64 offen lds
F 132,215
Mav 64,224,4
T 248,193,49664
T 250,193,51712
F 133,215
Mav 64,228,52
T 252,193,53760
E 132
Mav 68,224,12
Z 141312
T 254,193,55808
E 133
)"
R"(Mav 68,228,60
W 8
s_add_u32 s64, s48, 71680
buffer_load_dwordx4 v229, s[24:27], s64 offen lds
F 134,215
Mav 72,232,4
T 208,192,57344
T 210,192,59392
F 135,215
Mav 72,236,52
T 212,192,61440
E 134
Mav 76,232,12
Z 143360
T 214,192,63488
E 135
)"
R"(Mav 76,236,60
s_add_u32 s64, s48, 86016
buffer_load_dwordx4 v229, s[24:27], s64 offen lds
C 108,108,109
Mav 192,204,4
T 216,193,57344
T 218,193,59392
C 109,110,111
Mav 192,204,52
T 220,193,61440
C 110,132,133
C 111,134,135
Mav 196,204,12
Z 145408
T 222,193,63488
F 148,214
)"
R"(F 149,214
Mav 196,204,60
W 8
s_add_u32 s64, s48, 100352
buffer_load_dwordx4 v229, s[24:27], s64 offen lds
F 150,214
Mav 80,240,4
T 224,192,57856
T 226,192,59904
F 151,214
Mav 80,244,52
T 228,192,61952
E 148
Mav 84,240,12
Z 147456
T 230,192,64000
)"
R"(E 149
Mav 84,244,60
s_add_u32 s64, s48, 114688
buffer_load_dwordx4 v229, s[24:27], s64 offen lds
F 172,214
Mav 88,248,4
T 232,193,57856
T 234,193,59904
F 173,214
Mav 88,252,52
T 236,193,61952
E 150
Mav 92,248,12
T 238,193,64000
E 151
Mav 92,252,60
)"
R"(W 8
E 172
F 174,214
Mav 96,208,4
E 173
F 175,214
Mav 96,212,52
E 174
C 148,148,149
Mav 100,208,12
E 175
C 149,150,151
Mav 100,212,60
C 150,172,173
C 151,174,175
F 156,215
)"
R"(Mav 104,216,4
E 156
F 157,215
Mav 104,220,52
E 157
F 158,215
Mav 108,216,12
E 158
F 159,215
Mav 108,220,60
W 0
E 159
F 180,215
Mav 112,224,4
E 180
F 181,215
)"
R"(Mav 112,228,52
E 181
F 182,215
Mav 116,224,12
E 182
F 183,215
Mav 116,228,60
E 183
C 156,156,157
Mav 120,232,4
C 157,158,159
Mav 120,236,52
C 158,180,181
C 159,182,183
Mav 124,232,12
Mav 124,236,60
)"
R"(s_cbranch_vccnz .LrareB2_7
.LbackB2_7:
R 0,0
R 1,1
R 2,2
R 3,3
R 8,4
R 9,5
R 10,6
R 11,7
R 4,8
R 5,9
R 6,10
R 7,11
R 12,12
R 13,13
)"
R"(R 14,14
R 15,15
R 24,16
R 25,17
R 26,18
R 27,19
R 32,20
R 33,21
R 34,22
R 35,23
R 28,24
R 29,25
R 30,26
R 31,27
R 36,28
R 37,29
)"
R"(R 38,30
R 39,31
R 48,32
R 49,33
R 50,34
R 51,35
R 56,36
R 57,37
R 58,38
R 59,39
R 52,40
R 53,41
R 54,42
R 55,43
R 60,44
R 61,45
)"
R"(R 62,46
R 63,47
R 72,48
R 73,49
R 74,50
R 75,51
R 80,52
R 81,53
R 82,54
R 83,55
R 76,56
R 77,57
R 78,58
R 79,59
R 84,60
R 85,61
)"
R"(R 86,62
R 87,63
R 96,64
R 97,65
R 98,66
R 99,67
R 104,68
R 105,69
R 106,70
R 107,71
R 120,80
R 121,81
R 122,82
R 123,83
s_waitcnt vmcnt(9) lgkmcnt(0)
s_barrier
)"
R"(T 240,194,32768
T 242,194,34816
T 244,194,36864
T 246,194,38912
T 248,195,32768
T 250,195,34816
T 252,195,36864
T 254,195,38912
T 208,194,33280
R 128,84
Mav 192,204,100
T 210,194,35328
T 212,194,37376
R 129,85
Mav 192,204,148
Z 149504
)"
R"(T 214,194,39424
T 216,195,33280
R 130,86
Mav 196,204,108
s_add_u32 s64, s48, 129024
buffer_load_dwordx4 v229, s[24:27], s64 offen lds
R 131,87
Mav 196,204,156
W 11
Z 151552
T 218,195,35328
T 220,195,37376
R 144,96
Mvv 0,240,100
W 11
s_add_u32 s64, s48, 143360
)"
R"(buffer_load_dwordx4 v229, s[24:27], s64 offen lds
R 145,97
Mvv 0,244,148
Z 153600
T 222,195,39424
T 224,194,40960
R 146,98
Mvv 8,240,108
s_add_u32 s64, s48, 157696
buffer_load_dwordx4 v229, s[24:27], s64 offen lds
R 147,99
Mvv 8,244,156
W 11
Z 155648
T 226,194,43008
T 228,194,45056
)"
R"(R 152,100
Mvv 4,248,100
W 10
s_add_u32 s64, s48, 172032
buffer_load_dwordx4 v229, s[24:27], s64 offen lds
R 153,101
Mvv 4,252,148
Z 157696
T 230,194,47104
T 232,195,40960
R 154,102
Mvv 12,248,108
s_add_u32 s64, s48, 186368
buffer_load_dwordx4 v229, s[24:27], s64 offen lds
R 155,103
Mvv 12,252,156
)"
R"(W 8
Z 159744
T 234,195,43008
T 236,195,45056
R 168,112
Mvv 24,208,100
s_add_u32 s64, s48, 200704
buffer_load_dwordx4 v229, s[24:27], s64 offen lds
R 169,113
Mvv 24,212,148
Z 161792
T 238,195,47104
T 240,194,41472
R 170,114
Mvv 32,208,108
s_add_u32 s64, s48, 215040
)"
R"(buffer_load_dwordx4 v229, s[24:27], s64 offen lds
R 171,115
Mvv 32,212,156
W 8
Z 0
T 242,194,43520
T 244,194,45568
R 176,116
Mvv 28,216,100
buffer_load_dwordx4 v229, s[24:27], s49 offen lds
R 177,117
Mvv 28,220,148
Z 2048
T 246,194,47616
T 248,195,41472
R 178,118
)"
R"(Mvv 36,216,108
s_add_u32 s64, s49, 14336
buffer_load_dwordx4 v229, s[24:27], s64 offen lds
R 179,119
Mvv 36,220,156
W 8
Z 4096
T 250,195,43520
T 252,195,45568
R 220,192
Mvv 48,224,100
s_add_u32 s64, s49, 28672
buffer_load_dwordx4 v229, s[24:27], s64 offen lds
R 221,196
Mvv 48,228,148
Z 6144
)"
R"(T 254,195,47616
T 208,194,49152
C 0,0,1
Mvv 56,224,108
s_add_u32 s64, s49, 43008
buffer_load_dwordx4 v229, s[24:27], s64 offen lds
C 1,2,3
Mvv 56,228,156
W 8
Z 8192
T 210,194,51200
T 212,194,53248
C 2,4,5
Mvv 52,232,100
s_add_u32 s64, s49, 57344
buffer_load_dwordx4 v229, s[24:27], s64 offen lds
)"
R"(C 3,6,7
Mvv 52,236,148
Z 10240
T 214,194,55296
T 216,195,49152
C 8,8,9
Mvv 60,232,108
s_add_u32 s64, s49, 71680
buffer_load_dwordx4 v229, s[24:27], s64 offen lds
C 9,10,11
Mvv 60,236,156
W 8
Z 12288
T 218,195,51200
T 220,195,53248
C 10,12,13
)"
R"(Mvv 72,240,100
s_add_u32 s64, s49, 86016
buffer_load_dwordx4 v229, s[24:27], s64 offen lds
C 11,14,15
Mvv 72,244,148
Z 14336
T 222,195,55296
T 224,194,49664
C 24,24,25
Mvv 80,240,108
s_add_u32 s64, s49, 100352
buffer_load_dwordx4 v229, s[24:27], s64 offen lds
C 25,26,27
Mvv 80,244,156
W 8
Z 16384
)"
R"(T 226,194,51712
T 228,194,53760
C 26,28,29
Mvv 76,248,100
s_add_u32 s64, s49, 114688
buffer_load_dwordx4 v229, s[24:27], s64 offen lds
C 27,30,31
Mvv 76,252,148
Z 18432
T 230,194,55808
T 232,195,49664
C 32,32,33
Mvv 84,248,108
s_add_u32 s64, s49, 129024
buffer_load_dwordx4 v229, s[24:27], s64 offen lds
C 33,34,35
)"
R"(Mvv 84,252,156
W 8
Z 20480
T 234,195,51712
T 236,195,53760
C 34,36,37
Mvv 96,208,100
s_add_u32 s64, s49, 143360
buffer_load_dwordx4 v229, s[24:27], s64 offen lds
C 35,38,39
Mvv 96,212,148
Z 22528
T 238,195,55808
T 240,194,57344
C 48,48,49
Mvv 104,208,108
)"
R"(s_add_u32 s64, s49, 157696
buffer_load_dwordx4 v229, s[24:27], s64 offen lds
C 49,50,51
Mvv 104,212,156
W 8
Z 24576
T 242,194,59392
T 244,194,61440
C 50,52,53
Mav 72,216,100
s_add_u32 s64, s49, 172032
buffer_load_dwordx4 v229, s[24:27], s64 offen lds
C 51,54,55
Mav 72,220,148
Z 26624
T 246,194,63488
)"
R"(T 248,195,57344
C 56,56,57
Mav 76,216,108
s_add_u32 s64, s49, 186368
buffer_load_dwordx4 v229, s[24:27], s64 offen lds
C 57,58,59
Mav 76,220,156
W 8
Z 28672
T 250,195,59392
T 252,195,61440
C 58,60,61
Mvv 120,224,100
s_add_u32 s64, s49, 200704
buffer_load_dwordx4 v229, s[24:27], s64 offen lds
C 59,62,63
)"
R"(Mvv 120,228,148
Z 30720
T 254,195,63488
T 208,194,57856
C 72,72,73
Mvv 128,224,108
s_add_u32 s64, s49, 215040
buffer_load_dwordx4 v229, s[24:27], s64 offen lds
C 73,74,75
Mvv 128,228,156
W 8
Z 65536
T 210,194,59904
T 212,194,61952
C 74,76,77
Mav 88,232,100
)"
R"(buffer_load_dwordx4 v229, s[24:27], s50 offen lds
C 75,78,79
Mav 88,236,148
Z 67584
T 214,194,64000
T 216,195,57856
C 80,80,81
Mav 92,232,108
s_add_u32 s64, s50, 14336
buffer_load_dwordx4 v229, s[24:27], s64 offen lds
C 81,82,83
Mav 92,236,156
W 8
Z 69632
T 218,195,59904
T 220,195,61952
)"
R"(C 82,84,85
Mvv 144,240,100
s_add_u32 s64, s50, 28672
buffer_load_dwordx4 v229, s[24:27], s64 offen lds
C 83,86,87
Mvv 144,244,148
Z 71680
T 222,195,64000
v_rcp_f32 v220, v220
Mvv 152,240,108
s_add_u32 s64, s50, 43008
buffer_load_dwordx4 v229, s[24:27], s64 offen lds
C 96,96,97
Mvv 152,244,156
Z 73728
s_add_u32 s64, s50, 57344
)"
R"(buffer_load_dwordx4 v229, s[24:27], s64 offen lds
Z 75776
s_add_u32 s64, s50, 71680
buffer_load_dwordx4 v229, s[24:27], s64 offen lds
Z 77824
s_add_u32 s64, s50, 86016
buffer_load_dwordx4 v229, s[24:27], s64 offen lds
Z 79872
s_add_u32 s64, s50, 100352
buffer_load_dwordx4 v229, s[24:27], s64 offen lds
Z 81920
s_add_u32 s64, s50, 114688
buffer_load_dwordx4 v229, s[24:27], s64 offen lds
s_waitcnt vmcnt(25) lgkmcnt(0)
s_barrier
T 224,196,0
)"
R"(C 97,98,99
Mav 104,248,100
T 226,196,2048
T 228,196,4096
R 242,72
Mav 104,252,148
Z 83968
T 230,196,6144
T 232,196,8192
R 243,73
Mav 108,248,108
s_add_u32 s64, s50, 129024
buffer_load_dwordx4 v229, s[24:27], s64 offen lds
C 98,242,243
Mav 108,252,156
Z 86016
)"
R"(T 234,196,10240
T 236,196,12288
R 244,74
Mvv 168,208,100
s_add_u32 s64, s50, 143360
buffer_load_dwordx4 v229, s[24:27], s64 offen lds
R 245,75
Mvv 168,212,148
Z 88064
T 238,196,14336
T 240,197,0
C 99,244,245
Mvv 176,208,108
s_add_u32 s64, s50, 157696
buffer_load_dwordx4 v229, s[24:27], s64 offen lds
C 104,104,105
)"
R"(Mvv 176,212,156
Z 90112
T 242,197,2048
T 244,197,4096
C 105,106,107
Mav 120,216,100
s_add_u32 s64, s50, 172032
buffer_load_dwordx4 v229, s[24:27], s64 offen lds
R 246,76
Mav 120,220,148
Z 92160
T 246,197,6144
T 248,197,8192
R 247,77
Mav 124,216,108
s_add_u32 s64, s50, 186368
)"
R"(buffer_load_dwordx4 v229, s[24:27], s64 offen lds
C 106,246,247
Mav 124,220,156
W 8
Z 94208
T 250,197,10240
T 252,197,12288
R 248,78
Mvvz 4,224,0
s_add_u32 s64, s50, 200704
buffer_load_dwordx4 v229, s[24:27], s64 offen lds
R 249,79
Mvv 4,228,24
W 8
T 254,197,14336
T 208,196,512
)"
R"(C 107,248,249
Mvv 4,232,48
W 8
Z 96256
T 210,196,2560
T 212,196,4608
C 120,120,121
Mvv 4,236,72
s_add_u32 s64, s50, 215040
buffer_load_dwordx4 v229, s[24:27], s64 offen lds
C 121,122,123
Mvvz 12,224,8
Z 32768
T 214,196,6656
T 216,196,8704
R 250,88
)"
R"(Mvv 12,228,32
buffer_load_dwordx4 v229, s[24:27], s51 offen lds
R 251,89
Mvv 12,232,56
Z 34816
T 218,196,10752
T 220,196,12800
C 122,250,251
Mvv 12,236,80
W 10
s_add_u32 s64, s51, 14336
buffer_load_dwordx4 v229, s[24:27], s64 offen lds
R 242,90
Mvvz 28,240,0
Z 36864
T 222,196,14848
)"
R"(T 224,197,512
R 243,91
Mvv 28,244,24
W 10
s_add_u32 s64, s51, 28672
buffer_load_dwordx4 v229, s[24:27], s64 offen lds
C 123,242,243
Mvv 28,248,48
W 8
Z 38912
T 226,197,2560
T 228,197,4608
C 128,128,129
Mvv 28,252,72
s_add_u32 s64, s51, 43008
buffer_load_dwordx4 v229, s[24:27], s64 offen lds
)"
R"(C 129,130,131
Mvvz 36,240,8
T 230,197,6656
T 232,197,8704
R 244,92
Mvv 36,244,32
Z 40960
T 234,197,10752
T 236,197,12800
R 245,93
Mvv 36,248,56
s_add_u32 s64, s51, 57344
buffer_load_dwordx4 v229, s[24:27], s64 offen lds
C 130,244,245
Mvv 36,252,80
W 8
)"
R"(Z 43008
T 238,197,14848
T 240,196,16384
R 246,94
Mvvz 52,208,0
s_add_u32 s64, s51, 71680
buffer_load_dwordx4 v229, s[24:27], s64 offen lds
R 247,95
Mvv 52,212,24
Z 45056
T 242,196,18432
T 244,196,20480
C 131,246,247
Mvv 52,216,48
W 10
s_add_u32 s64, s51, 86016
)"
R"(buffer_load_dwordx4 v229, s[24:27], s64 offen lds
C 144,144,145
Mvv 52,220,72
Z 47104
T 246,196,22528
T 248,196,24576
C 145,146,147
Mvvz 60,208,8
s_add_u32 s64, s51, 100352
buffer_load_dwordx4 v229, s[24:27], s64 offen lds
R 248,104
Mvv 60,212,32
Z 49152
T 250,196,26624
T 252,196,28672
R 249,105
)"
R"(Mvv 60,216,56
s_add_u32 s64, s51, 114688
buffer_load_dwordx4 v229, s[24:27], s64 offen lds
C 146,248,249
Mvv 60,220,80
W 8
T 254,196,30720
T 208,197,16384
R 250,106
Mvvz 76,224,0
Z 51200
T 210,197,18432
T 212,197,20480
R 251,107
Mvv 76,228,24
s_add_u32 s64, s51, 129024
)"
R"(buffer_load_dwordx4 v229, s[24:27], s64 offen lds
C 147,250,251
Mvv 76,232,48
W 8
Z 53248
T 214,197,22528
T 216,197,24576
C 152,152,153
Mvv 76,236,72
s_add_u32 s64, s51, 143360
buffer_load_dwordx4 v229, s[24:27], s64 offen lds
C 153,154,155
Mvvz 84,224,8
Z 55296
T 218,197,26624
T 220,197,28672
)"
R"(R 242,108
Mvv 84,228,32
s_add_u32 s64, s51, 157696
buffer_load_dwordx4 v229, s[24:27], s64 offen lds
R 243,109
Mvv 84,232,56
Z 57344
T 222,197,30720
T 224,196,16896
C 154,242,243
Mvv 84,236,80
s_add_u32 s64, s51, 172032
buffer_load_dwordx4 v229, s[24:27], s64 offen lds
R 244,110
Mvv 4,240,96
W 8
)"
R"(Z 59392
T 226,196,18944
T 228,196,20992
R 245,111
Mvv 4,244,120
s_add_u32 s64, s51, 186368
buffer_load_dwordx4 v229, s[24:27], s64 offen lds
C 155,244,245
Mvv 4,248,144
T 230,196,23040
T 232,196,25088
C 168,168,169
C 169,170,171
R 246,120
R 247,121
C 170,246,247
)"
R"(R 248,122
R 249,123
C 171,248,249
s_nop 1
Mvv 4,252,168
Z 61440
T 234,196,27136
T 236,196,29184
C 176,176,177
Mvv 12,240,104
s_add_u32 s64, s51, 200704
buffer_load_dwordx4 v229, s[24:27], s64 offen lds
C 177,178,179
Mvv 12,244,128
Z 63488
T 238,196,31232
)"
R"(T 240,197,16896
R 250,124
Mvv 12,248,152
s_add_u32 s64, s51, 215040
buffer_load_dwordx4 v229, s[24:27], s64 offen lds
R 251,125
C 178,250,251
R 242,126
R 243,127
C 179,242,243
s_nop 1
Mvv 12,252,176
W 8
T 242,197,18944
T 244,197,20992
C 4,4,5
)"
R"(Mvv 28,208,96
buffer_store_dword v220, v206, s[72:75], 0 offen offset:0 sc1
C 5,6,7
Mvv 28,212,120
T 246,197,23040
T 248,197,25088
Mvv 28,216,144
T 250,197,27136
T 252,197,29184
Mvv 28,220,168
T 254,197,31232
v_rcp_f32 v221, v221
Mvv 36,208,104
buffer_store_dword v221, v206, s[72:75], 0 offen offset:64 sc1
C 12,12,13
Mvv 36,212,128
)"
R"(C 13,14,15
Mvv 36,216,152
Mvv 36,220,176
W 7
C 6,28,29
C 7,30,31
Mvv 52,224,96
buffer_store_dwordx4 v[4:7], v223, s[28:31], s55 offen offset:0 sc1
Mvv 52,228,120
Mvv 52,232,144
Mvv 52,236,168
s_waitcnt vmcnt(35) lgkmcnt(0)
s_barrier
T 208,192,0
C 14,36,37
Mvv 60,224,104
)"
R"(T 210,192,2048
T 212,192,4096
Mvv 60,228,128
Z 131072
T 214,192,6144
T 216,192,8192
Mvv 60,232,152
buffer_load_dwordx4 v229, s[24:27], s52 offen lds
Mvv 60,236,176
T 218,192,10240
T 220,192,12288
Mvv 76,240,96
Z 133120
T 222,192,14336
ds_read_b128 v[16:19], v232 offset:0
Mvv 76,244,120
)"
R"(s_add_u32 s64, s52, 14336
buffer_load_dwordx4 v229, s[24:27], s64 offen lds
Mvv 76,248,144
T 224,192,16384
T 226,192,18432
Mvv 76,252,168
ds_read_b128 v[40:43], v232 offset:1024
T 228,192,20480
Mvv 84,240,104
Z 135168
T 230,192,22528
ds_read_b128 v[64:67], v232 offset:2048
Mvv 84,244,128
s_add_u32 s64, s52, 28672
buffer_load_dwordx4 v229, s[24:27], s64 offen lds
Mvv 84,248,152
)"
R"(T 232,192,24576
T 234,192,26624
Mvv 84,252,176
W 12
Z 137216
ds_read_b128 v[88:91], v232 offset:3072
T 236,192,28672
Mvvz 100,208,0
s_add_u32 s64, s52, 43008
buffer_load_dwordx4 v229, s[24:27], s64 offen lds
Mvv 100,212,24
W 10
T 238,192,30720
ds_read_b128 v[112:115], v232 offset:4096
Mvv 100,216,48
Z 139264
)"
R"(T 240,193,0
T 242,193,2048
Mvv 100,220,72
s_add_u32 s64, s52, 57344
buffer_load_dwordx4 v229, s[24:27], s64 offen lds
Mvvz 108,208,8
ds_read_b128 v[136:139], v232 offset:5120
T 244,193,4096
Mvv 108,212,32
T 246,193,6144
ds_read_b128 v[160:163], v232 offset:6144
Mvv 108,216,56
Z 141312
T 248,193,8192
C 15,38,39
Mvv 108,220,80
)"
R"(W 11
s_add_u32 s64, s52, 71680
buffer_load_dwordx4 v229, s[24:27], s64 offen lds
Mvv 100,224,96
buffer_store_dwordx4 v[12:15], v224, s[28:31], s55 offen offset:0 sc1
Mvv 100,228,120
T 250,193,10240
ds_read_b128 v[184:187], v232 offset:7168
Mvv 100,232,144
W 9
Z 143360
T 252,193,12288
T 254,193,14336
Mvv 100,236,168
s_add_u32 s64, s52, 86016
buffer_load_dwordx4 v229, s[24:27], s64 offen lds
)"
R"(Mvv 108,224,104
T 208,193,16384
T 210,193,18432
Mvv 108,228,128
T 212,193,20480
C 52,52,53
Mvv 108,232,152
Z 145408
T 214,193,22528
C 53,54,55
Mvv 108,236,176
W 8
s_add_u32 s64, s52, 100352
buffer_load_dwordx4 v229, s[24:27], s64 offen lds
Mvvz 124,240,0
T 216,193,24576
)"
R"(T 218,193,26624
Mvv 124,244,24
W 9
T 220,193,28672
C 54,76,77
Mvv 124,248,48
W 7
Z 147456
T 222,193,30720
C 55,78,79
Mvv 124,252,72
s_add_u32 s64, s52, 114688
buffer_load_dwordx4 v229, s[24:27], s64 offen lds
Mvvz 132,240,8
buffer_store_dwordx4 v[52:55], v223, s[28:31], s55 offen offset:1024 sc1
Mvv 132,244,32
)"
R"(T 224,192,512
T 226,192,2560
Mvv 132,248,56
Z 149504
T 228,192,4608
T 230,192,6656
Mvv 132,252,80
W 8
s_add_u32 s64, s52, 129024
buffer_load_dwordx4 v229, s[24:27], s64 offen lds
Mvv 124,208,96
T 232,192,8704
T 234,192,10752
Mvv 124,212,120
W 8
T 236,192,12800
)"
R"(C 60,60,61
Mvv 124,216,144
W 7
Z 151552
T 238,192,14848
C 61,62,63
Mvv 124,220,168
s_add_u32 s64, s52, 143360
buffer_load_dwordx4 v229, s[24:27], s64 offen lds
Mvv 132,208,104
T 240,192,16896
C 62,84,85
Mvv 132,212,128
T 242,192,18944
C 63,86,87
Mvv 132,216,152
)"
R"(Z 153600
buffer_store_dwordx4 v[60:63], v224, s[28:31], s55 offen offset:1024 sc1
Mvv 132,220,176
W 6
s_add_u32 s64, s52, 157696
buffer_load_dwordx4 v229, s[24:27], s64 offen lds
Mvvz 148,224,0
T 244,192,20992
C 100,100,101
Mvv 148,228,24
W 5
T 246,192,23040
C 101,102,103
Mvv 148,232,48
W 4
Z 155648
)"
R"(T 248,192,25088
C 102,124,125
Mvv 148,236,72
s_add_u32 s64, s52, 172032
buffer_load_dwordx4 v229, s[24:27], s64 offen lds
Mvvz 156,224,8
T 250,192,27136
C 103,126,127
Mvv 156,228,32
buffer_store_dwordx4 v[100:103], v223, s[28:31], s56 offen offset:0 sc1
Mvv 156,232,56
Z 157696
T 252,192,29184
T 254,192,31232
Mvv 156,236,80
s_add_u32 s64, s52, 186368
)"
R"(buffer_load_dwordx4 v229, s[24:27], s64 offen lds
Mvvz 164,224,16
T 208,193,512
C 108,108,109
Mvv 164,228,40
T 210,193,2560
C 109,110,111
Mvv 164,232,64
Z 159744
T 212,193,4608
C 110,132,133
Mvv 164,236,88
W 6
s_add_u32 s64, s52, 200704
buffer_load_dwordx4 v229, s[24:27], s64 offen lds
Mvv 148,240,96
)"
R"(T 214,193,6656
C 111,134,135
Mvv 148,244,120
W 6
buffer_store_dwordx4 v[108:111], v224, s[28:31], s56 offen offset:0 sc1
Mvv 148,248,144
W 4
Z 161792
T 216,193,8704
T 218,193,10752
Mvv 148,252,168
s_add_u32 s64, s52, 215040
buffer_load_dwordx4 v229, s[24:27], s64 offen lds
Mvv 156,240,104
T 220,193,12800
T 222,193,14848
)"
R"(Mvv 156,244,128
T 224,193,16896
T 226,193,18944
Mvv 156,248,152
T 228,193,20992
T 230,193,23040
Mvv 156,252,176
T 232,193,25088
C 148,148,149
Mvv 164,240,112
T 234,193,27136
C 149,150,151
Mvv 164,244,136
T 236,193,29184
T 238,193,31232
Mvv 164,248,160
)"
R"(Mvv 164,252,184
W 10
C 156,156,157
Mvvz 172,208,0
C 157,158,159
Mvv 172,212,24
Mvv 172,216,48
W 6
Mvv 172,220,72
C 164,164,165
Mvvz 180,208,8
C 165,166,167
Mvv 180,212,32
Mvv 180,216,56
Mvv 180,220,80
Mvvz 188,208,16
)"
R"(Mvv 188,212,40
Mvv 188,216,64
Mvv 188,220,88
s_waitcnt vmcnt(24) lgkmcnt(0)
s_barrier
T 240,194,0
Mvv 172,224,96
Z 0
s_nop 0
buffer_load_dwordx4 v229, s[24:27], s53 offen lds
Mvv 172,228,120
T 242,194,2048
T 244,194,4096
Mvv 172,232,144
Z 2048
T 246,194,6144
)"
R"(T 248,194,8192
Mvv 172,236,168
s_add_u32 s64, s53, 14336
buffer_load_dwordx4 v229, s[24:27], s64 offen lds
Mvv 180,224,104
T 250,194,10240
T 252,194,12288
Mvv 180,228,128
T 254,194,14336
T 208,194,16384
Mvv 180,232,152
Z 4096
T 210,194,18432
T 212,194,20480
Mvv 180,236,176
s_add_u32 s64, s53, 28672
)"
R"(buffer_load_dwordx4 v229, s[24:27], s64 offen lds
Mvv 188,224,112
T 214,194,22528
C 150,172,173
Mvv 188,228,136
T 216,194,24576
C 151,174,175
Mvv 188,232,160
Z 6144
buffer_store_dwordx4 v[148:151], v223, s[28:31], s56 offen offset:1024 sc1
Mvv 188,236,184
W 8
s_add_u32 s64, s53, 43008
buffer_load_dwordx4 v229, s[24:27], s64 offen lds
Mvvz 4,240,0
T 218,194,26624
)"
R"(C 158,180,181
Mvv 4,244,24
W 7
T 220,194,28672
C 159,182,183
Mvv 4,248,48
W 6
Z 8192
buffer_store_dwordx4 v[156:159], v224, s[28:31], s56 offen offset:1024 sc1
Mvv 4,252,72
s_add_u32 s64, s53, 57344
buffer_load_dwordx4 v229, s[24:27], s64 offen lds
Mvvz 12,240,8
T 222,194,30720
T 224,195,0
Mvv 12,244,32
)"
R"(T 226,195,2048
T 228,195,4096
Mvv 12,248,56
Z 10240
T 230,195,6144
C 166,188,189
Mvv 12,252,80
W 7
s_add_u32 s64, s53, 71680
buffer_load_dwordx4 v229, s[24:27], s64 offen lds
Mvv 4,208,96
T 232,195,8192
T 234,195,10240
Mvv 4,212,120
W 8
T 236,195,12288
)"
R"(C 167,190,191
Mvv 4,216,144
W 7
Z 12288
buffer_store_dwordx4 v[164:167], v225, s[28:31], s56 offen offset:1024 sc1
Mvv 4,220,168
s_add_u32 s64, s53, 86016
buffer_load_dwordx4 v229, s[24:27], s64 offen lds
Mvv 12,208,104
T 238,195,14336
T 240,195,16384
Mvv 12,212,128
T 242,195,18432
T 244,195,20480
Mvv 12,216,152
Z 14336
)"
R"(T 246,195,22528
T 248,195,24576
Mvv 12,220,176
W 9
s_add_u32 s64, s53, 100352
buffer_load_dwordx4 v229, s[24:27], s64 offen lds
Mvvz 28,224,0
T 250,195,26624
C 4,4,5
Mvv 28,228,24
W 8
T 252,195,28672
C 5,6,7
Mvv 28,232,48
W 7
Z 16384
)"
R"(T 254,195,30720
T 208,194,512
Mvv 28,236,72
s_add_u32 s64, s53, 114688
buffer_load_dwordx4 v229, s[24:27], s64 offen lds
Mvvz 36,224,8
T 210,194,2560
C 12,12,13
Mvv 36,228,32
T 212,194,4608
C 13,14,15
Mvv 36,232,56
Z 18432
T 214,194,6656
T 216,194,8704
Mvv 36,236,80
)"
R"(W 8
s_add_u32 s64, s53, 129024
buffer_load_dwordx4 v229, s[24:27], s64 offen lds
Mvv 28,240,96
T 218,194,10752
T 220,194,12800
Mvv 28,244,120
W 9
T 222,194,14848
T 224,194,16896
Mvv 28,248,144
W 9
Z 20480
T 226,194,18944
T 228,194,20992
Mvv 28,252,168
)"
R"(s_add_u32 s64, s53, 143360
buffer_load_dwordx4 v229, s[24:27], s64 offen lds
Mvv 36,240,104
T 230,194,23040
T 232,194,25088
Mvv 36,244,128
T 234,194,27136
T 236,194,29184
Mvv 36,248,152
Z 22528
T 238,194,31232
T 240,195,512
Mvv 36,252,176
W 12
s_add_u32 s64, s53, 157696
buffer_load_dwordx4 v229, s[24:27], s64 offen lds
)"
R"(Mvvz 52,208,0
T 242,195,2560
C 6,28,29
Mvv 52,212,24
W 11
T 244,195,4608
C 7,30,31
Mvv 52,216,48
W 10
Z 24576
buffer_store_dwordx4 v[4:7], v223, s[28:31], s57 offen offset:0 sc1
Mvv 52,220,72
s_add_u32 s64, s53, 172032
buffer_load_dwordx4 v229, s[24:27], s64 offen lds
Mvvz 60,208,8
T 246,195,6656
)"
R"(C 14,36,37
Mvv 60,212,32
T 248,195,8704
C 15,38,39
Mvv 60,216,56
Z 26624
buffer_store_dwordx4 v[12:15], v224, s[28:31], s57 offen offset:0 sc1
Mvv 60,220,80
s_add_u32 s64, s53, 186368
buffer_load_dwordx4 v229, s[24:27], s64 offen lds
Mvvz 68,208,16
T 250,195,10752
T 252,195,12800
Mvv 68,212,40
T 254,195,14848
T 208,195,16896
)"
R"(Mvv 68,216,64
Z 28672
T 210,195,18944
T 212,195,20992
Mvv 68,220,88
W 8
s_add_u32 s64, s53, 200704
buffer_load_dwordx4 v229, s[24:27], s64 offen lds
Mvv 52,224,96
T 214,195,23040
T 216,195,25088
Mvv 52,228,120
T 218,195,27136
T 220,195,29184
Mvv 52,232,144
Z 30720
)"
R"(T 222,195,31232
Mvv 52,236,168
s_add_u32 s64, s53, 215040
buffer_load_dwordx4 v229, s[24:27], s64 offen lds
Mvv 60,224,104
Mvv 60,228,128
Mvv 60,232,152
Mvv 60,236,176
s_nop 1
C 52,52,53
Mvv 68,224,112
T 224,192,32768
C 53,54,55
Mvv 68,228,136
T 226,192,34816
T 228,192,36864
)"
R"(Mvv 68,232,160
T 230,192,38912
T 232,192,40960
Mvv 68,236,184
T 234,192,43008
C 60,60,61
Mvvz 76,240,0
W 6
T 236,192,45056
C 61,62,63
Mvv 76,244,24
T 238,192,47104
Mvv 76,248,48
Mvv 76,252,72
C 68,68,69
Mvvz 84,240,8
)"
R"(C 69,70,71
Mvv 84,244,32
Mvv 84,248,56
Mvv 84,252,80
Mvvz 92,240,16
T 240,192,49152
T 242,192,51200
Mvv 92,244,40
T 244,192,53248
T 246,192,55296
Mvv 92,248,64
T 248,192,57344
T 250,192,59392
Mvv 92,252,88
Z 98304

)"
R"(T 252,192,61440
T 254,192,63488
Mvv 76,208,96
buffer_load_dwordx4 v229, s[24:27], s54 offen lds
Mvv 76,212,120
Mvv 76,216,144
Z 100352
Mvv 76,220,168
s_add_u32 s64, s54, 14336
buffer_load_dwordx4 v229, s[24:27], s64 offen lds
Mvv 84,208,104
Mvv 84,212,128
Mvv 84,216,152
Z 102400
Mvv 84,220,176
s_add_u32 s64, s54, 28672
)"
R"(buffer_load_dwordx4 v229, s[24:27], s64 offen lds
Mvv 92,208,112
T 208,193,32768
C 54,76,77
Mvv 92,212,136
T 210,193,34816
C 55,78,79
Mvv 92,216,160
Z 104448
buffer_store_dwordx4 v[52:55], v223, s[28:31], s57 offen offset:1024 sc1
Mvv 92,220,184
W 2
s_add_u32 s64, s54, 43008
buffer_load_dwordx4 v229, s[24:27], s64 offen lds
Mvvz 100,224,0
T 212,193,36864
)"
R"(C 62,84,85
Mvv 100,228,24
T 214,193,38912
C 63,86,87
Mvv 100,232,48
Z 106496
buffer_store_dwordx4 v[60:63], v224, s[28:31], s57 offen offset:1024 sc1
Mvv 100,236,72
s_add_u32 s64, s54, 57344
buffer_load_dwordx4 v229, s[24:27], s64 offen lds
Mvvz 108,224,8
T 216,193,40960
C 70,92,93
Mvv 108,228,32
T 218,193,43008
C 71,94,95
)"
R"(Mvv 108,232,56
Z 108544
buffer_store_dwordx4 v[68:71], v225, s[28:31], s57 offen offset:1024 sc1
Mvv 108,236,80
s_add_u32 s64, s54, 71680
buffer_load_dwordx4 v229, s[24:27], s64 offen lds
Mvv 100,240,96
T 220,193,45056
T 222,193,47104
Mvv 100,244,120
T 224,193,49152
T 226,193,51200
Mvv 100,248,144
Z 110592
T 228,193,53248
T 230,193,55296
)"
R"(Mvv 100,252,168
s_add_u32 s64, s54, 86016
buffer_load_dwordx4 v229, s[24:27], s64 offen lds
Mvv 108,240,104
T 232,193,57344
T 234,193,59392
Mvv 108,244,128
T 236,193,61440
T 238,193,63488
Mvv 108,248,152
Z 112640
T 240,192,33280
T 242,192,35328
Mvv 108,252,176
W 12
s_add_u32 s64, s54, 100352
)"
R"(buffer_load_dwordx4 v229, s[24:27], s64 offen lds
Mvvz 124,208,0
T 244,192,37376
C 100,100,101
Mvv 124,212,24
T 246,192,39424
C 101,102,103
Mvv 124,216,48
W 10
Z 114688
T 248,192,41472
T 250,192,43520
Mvv 124,220,72
s_add_u32 s64, s54, 114688
buffer_load_dwordx4 v229, s[24:27], s64 offen lds
Mvvz 132,208,8
)"
R"(T 252,192,45568
C 108,108,109
Mvv 132,212,32
T 254,192,47616
C 109,110,111
Mvv 132,216,56
Z 116736
T 208,192,49664
T 210,192,51712
Mvv 132,220,80
s_add_u32 s64, s54, 129024
buffer_load_dwordx4 v229, s[24:27], s64 offen lds
Mvv 124,224,96
W 8
T 212,192,53760
T 214,192,55808
)"
R"(Mvv 124,228,120
T 216,192,57856
T 218,192,59904
Mvv 124,232,144
Z 118784
T 220,192,61952
T 222,192,64000
Mvv 124,236,168
s_add_u32 s64, s54, 143360
buffer_load_dwordx4 v229, s[24:27], s64 offen lds
Mvv 132,224,104
T 224,193,33280
T 226,193,35328
Mvv 132,228,128
T 228,193,37376
T 230,193,39424
)"
R"(Mvv 132,232,152
Z 120832
T 232,193,41472
T 234,193,43520
Mvv 132,236,176
s_add_u32 s64, s54, 157696
buffer_load_dwordx4 v229, s[24:27], s64 offen lds
Mvvz 148,240,0
W 12
T 236,193,45568
C 102,124,125
Mvv 148,244,24
T 238,193,47616
C 103,126,127
Mvv 148,248,48
Z 122880
)"
R"(buffer_store_dwordx4 v[100:103], v223, s[28:31], s58 offen offset:0 sc1
Mvv 148,252,72
s_add_u32 s64, s54, 172032
buffer_load_dwordx4 v229, s[24:27], s64 offen lds
Mvvz 156,240,8
C 110,132,133
C 111,134,135
Mvv 156,244,32
buffer_store_dwordx4 v[108:111], v224, s[28:31], s58 offen offset:0 sc1
Mvv 156,248,56
Z 124928
Mvv 156,252,80
s_add_u32 s64, s54, 186368
buffer_load_dwordx4 v229, s[24:27], s64 offen lds
Mvvz 164,240,16
T 240,193,49664
)"
R"(T 242,193,51712
Mvv 164,244,40
T 244,193,53760
T 246,193,55808
Mvv 164,248,64
Z 126976
T 248,193,57856
T 250,193,59904
Mvv 164,252,88
s_add_u32 s64, s54, 200704
buffer_load_dwordx4 v229, s[24:27], s64 offen lds
Mvv 148,208,96
W 6
T 252,193,61952
T 254,193,64000
Mvv 148,212,120
)"
R"(Mvv 148,216,144
Z 129024
Mvv 148,220,168
s_add_u32 s64, s54, 215040
buffer_load_dwordx4 v229, s[24:27], s64 offen lds
Mvv 156,208,104
Mvv 156,212,128
Mvv 156,216,152
Mvv 156,220,176
s_nop 1
C 148,148,149
Mvv 164,208,112
C 149,150,151
Mvv 164,212,136
Mvv 164,216,160
Mvv 164,220,184
)"
R"(C 156,156,157
Mvvz 172,224,0
C 157,158,159
Mvv 172,228,24
Mvv 172,232,48
Mvv 172,236,72
s_nop 1
C 164,164,165
Mvvz 180,224,8
C 165,166,167
Mvv 180,228,32
Mvv 180,232,56
Mvv 180,236,80
Mvvz 188,224,16
Mvv 188,228,40
Mvv 188,232,64
)"
R"(Mvv 188,236,88
s_waitcnt vmcnt(42) lgkmcnt(0)
s_barrier
T 208,196,0
Mvv 172,240,96
T 210,196,2048
T 212,196,4096
Mvv 172,244,120
T 214,196,6144
T 216,196,8192
Mvv 172,248,144
T 218,196,10240
T 220,196,12288
Mvv 172,252,168
T 222,196,14336
T 224,196,16384
)"
R"(Mvv 180,240,104
T 226,196,18432
T 228,196,20480
Mvv 180,244,128
T 230,196,22528
T 232,196,24576
Mvv 180,248,152
T 234,196,26624
T 236,196,28672
Mvv 180,252,176
T 238,196,30720
C 150,172,173
Mvv 188,240,112
T 240,197,0
C 151,174,175
Mvv 188,244,136
)"
R"(buffer_store_dwordx4 v[148:151], v223, s[28:31], s58 offen offset:1024 sc1
Mvv 188,248,160
T 242,197,2048
T 244,197,4096
Mvv 188,252,184
W 12
T 246,197,6144
C 158,180,181
Mvvz 4,208,0
T 248,197,8192
C 159,182,183
Mvv 4,212,24
buffer_store_dwordx4 v[156:159], v224, s[28:31], s58 offen offset:1024 sc1
Mvv 4,216,48
W 8
T 250,197,10240
)"
R"(T 252,197,12288
Mvv 4,220,72
T 254,197,14336
C 166,188,189
Mvvz 12,208,8
T 208,197,16384
C 167,190,191
Mvv 12,212,32
buffer_store_dwordx4 v[164:167], v225, s[28:31], s58 offen offset:1024 sc1
Mvv 12,216,56
T 210,197,18432
T 212,197,20480
Mvv 12,220,80
T 214,197,22528
T 216,197,24576
Mvv 4,224,96
)"
R"(T 218,197,26624
T 220,197,28672
Mvv 4,228,120
W 10
T 222,197,30720
Mvv 4,232,144
Mvv 4,236,168
Mvv 12,224,104
T 224,196,512
T 226,196,2560
Mvv 12,228,128
T 228,196,4608
T 230,196,6656
Mvv 12,232,152
T 232,196,8704
T 234,196,10752
)"
R"(Mvv 12,236,176
T 236,196,12800
C 4,4,5
Mvvz 28,240,0
T 238,196,14848
C 5,6,7
Mvv 28,244,24
W 9
Mvv 28,248,48
Mvv 28,252,72
C 12,12,13
Mvvz 36,240,8
T 240,196,16896
C 13,14,15
Mvv 36,244,32
T 242,196,18944
)"
R"(T 244,196,20992
Mvv 36,248,56
T 246,196,23040
T 248,196,25088
Mvv 36,252,80
T 250,196,27136
T 252,196,29184
Mvv 28,208,96
T 254,196,31232
Mvv 28,212,120
Mvv 28,216,144
W 8
Mvv 28,220,168
Mvv 36,208,104
T 208,197,512
T 210,197,2560
)"
R"(Mvv 36,212,128
T 212,197,4608
T 214,197,6656
Mvv 36,216,152
T 216,197,8704
T 218,197,10752
Mvv 36,220,176
T 220,197,12800
C 6,28,29
Mvvz 52,224,0
T 222,197,14848
C 7,30,31
Mvv 52,228,24
buffer_store_dwordx4 v[4:7], v223, s[28:31], s59 offen offset:0 sc1
Mvv 52,232,48
Mvv 52,236,72
)"
R"(C 14,36,37
C 15,38,39
Mvvz 60,224,8
buffer_store_dwordx4 v[12:15], v224, s[28:31], s59 offen offset:0 sc1
Mvv 60,228,32
Mvv 60,232,56
Mvv 60,236,80
Mvvz 68,224,16
T 224,197,16896
T 226,197,18944
Mvv 68,228,40
T 228,197,20992
T 230,197,23040
Mvv 68,232,64
T 232,197,25088
T 234,197,27136
)"
R"(Mvv 68,236,88
W 6
T 236,197,29184
T 238,197,31232
Mvv 52,240,96
Mvv 52,244,120
Mvv 52,248,144
Mvv 52,252,168
Mvv 60,240,104
Mvv 60,244,128
Mvv 60,248,152
Mvv 60,252,176
s_nop 3
C 52,52,53
Mvv 68,240,112
C 53,54,55
)"
R"(Mvv 68,244,136
Mvv 68,248,160
Mvv 68,252,184
C 60,60,61
Mvvz 76,208,0
C 61,62,63
Mvv 76,212,24
Mvv 76,216,48
Mvv 76,220,72
s_nop 1
C 68,68,69
Mvvz 84,208,8
C 69,70,71
Mvv 84,212,32
Mvv 84,216,56
Mvv 84,220,80
)"
R"(Mvvz 92,208,16
Mvv 92,212,40
Mvv 92,216,64
Mvv 92,220,88
s_waitcnt vmcnt(2) lgkmcnt(0)
s_barrier
v_add_u32 v238, 1, v233
buffer_store_dword v238, v252, s[72:75], 0 offen sc1
T 240,192,0
Mvv 76,224,96
T 242,192,2048
T 244,192,4096
Mvv 76,228,120
T 246,192,6144
T 248,192,8192
Mvv 76,232,144
)"
R"(T 250,192,10240
T 252,192,12288
Mvv 76,236,168
T 254,192,14336
T 208,192,16384
Mvv 84,224,104
T 210,192,18432
T 212,192,20480
Mvv 84,228,128
T 214,192,22528
T 216,192,24576
Mvv 84,232,152
T 218,192,26624
T 220,192,28672
Mvv 84,236,176
T 222,192,30720
)"
R"(C 54,76,77
Mvv 92,224,112
T 224,193,0
C 55,78,79
Mvv 92,228,136
buffer_store_dwordx4 v[52:55], v223, s[28:31], s59 offen offset:1024 sc1
Mvv 92,232,160
T 226,193,2048
T 228,193,4096
Mvv 92,236,184
W 12
T 230,193,6144
C 62,84,85
Mvvz 100,240,0
T 232,193,8192
C 63,86,87
)"
R"(Mvv 100,244,24
buffer_store_dwordx4 v[60:63], v224, s[28:31], s59 offen offset:1024 sc1
Mvv 100,248,48
W 8
T 234,193,10240
T 236,193,12288
Mvv 100,252,72
T 238,193,14336
C 70,92,93
Mvvz 108,240,8
T 240,193,16384
C 71,94,95
Mvv 108,244,32
buffer_store_dwordx4 v[68:71], v225, s[28:31], s59 offen offset:1024 sc1
Mvv 108,248,56
T 242,193,18432
)"
R"(T 244,193,20480
Mvv 108,252,80
T 246,193,22528
T 248,193,24576
Mvv 100,208,96
T 250,193,26624
T 252,193,28672
Mvv 100,212,120
W 10
T 254,193,30720
Mvv 100,216,144
Mvv 100,220,168
Mvv 108,208,104
T 208,192,512
T 210,192,2560
Mvv 108,212,128
)"
R"(T 212,192,4608
T 214,192,6656
Mvv 108,216,152
T 216,192,8704
T 218,192,10752
Mvv 108,220,176
T 220,192,12800
C 100,100,101
Mvvz 124,224,0
T 222,192,14848
C 101,102,103
Mvv 124,228,24
W 9
Mvv 124,232,48
Mvv 124,236,72
C 108,108,109
)"
R"(Mvvz 132,224,8
T 224,192,16896
C 109,110,111
Mvv 132,228,32
T 226,192,18944
T 228,192,20992
Mvv 132,232,56
T 230,192,23040
T 232,192,25088
Mvv 132,236,80
T 234,192,27136
T 236,192,29184
Mvv 124,240,96
T 238,192,31232
Mvv 124,244,120
Mvv 124,248,144
)"
R"(W 8
Mvv 124,252,168
Mvv 132,240,104
T 240,193,512
T 242,193,2560
Mvv 132,244,128
T 244,193,4608
T 246,193,6656
Mvv 132,248,152
T 248,193,8704
T 250,193,10752
Mvv 132,252,176
T 252,193,12800
C 102,124,125
Mvvz 148,208,0
T 254,193,14848
)"
R"(C 103,126,127
Mvv 148,212,24
buffer_store_dwordx4 v[100:103], v223, s[28:31], s60 offen offset:0 sc1
Mvv 148,216,48
Mvv 148,220,72
C 110,132,133
C 111,134,135
Mvvz 156,208,8
buffer_store_dwordx4 v[108:111], v224, s[28:31], s60 offen offset:0 sc1
Mvv 156,212,32
Mvv 156,216,56
Mvv 156,220,80
Mvvz 164,208,16
T 208,193,16896
T 210,193,18944
Mvv 164,212,40
)"
R"(T 212,193,20992
T 214,193,23040
Mvv 164,216,64
T 216,193,25088
T 218,193,27136
Mvv 164,220,88
W 6
T 220,193,29184
T 222,193,31232
Mvv 148,224,96
Mvv 148,228,120
Mvv 148,232,144
Mvv 148,236,168
Mvv 156,224,104
Mvv 156,228,128
Mvv 156,232,152
)"
R"(Mvv 156,236,176
s_nop 3
C 148,148,149
Mvv 164,224,112
T 224,194,32768
C 149,150,151
Mvv 164,228,136
T 226,194,34816
T 228,194,36864
Mvv 164,232,160
T 230,194,38912
T 232,194,40960
Mvv 164,236,184
T 234,194,43008
C 156,156,157
Mvvz 172,240,0
)"
R"(T 236,194,45056
C 157,158,159
Mvv 172,244,24
T 238,194,47104
Mvv 172,248,48
Mvv 172,252,72
C 164,164,165
Mvvz 180,240,8
C 165,166,167
Mvv 180,244,32
Mvv 180,248,56
Mvv 180,252,80
Mvvz 188,240,16
T 240,194,49152
T 242,194,51200
Mvv 188,244,40
)"
R"(T 244,194,53248
T 246,194,55296
Mvv 188,248,64
T 248,194,57344
T 250,194,59392
Mvv 188,252,88

W 6
T 252,194,61440
T 254,194,63488
Mvv 172,208,96
Mvv 172,212,120
Mvv 172,216,144
Mvv 172,220,168
Mvv 180,208,104
Mvv 180,212,128
)"
R"(Mvv 180,216,152
Mvv 180,220,176
s_nop 3
C 150,172,173
C 151,174,175
Mvv 188,208,112
buffer_store_dwordx4 v[148:151], v223, s[28:31], s60 offen offset:1024 sc1
Mvv 188,212,136
T 208,195,32768
T 210,195,34816
Mvv 188,216,160
T 212,195,36864
T 214,195,38912
Mvv 188,220,184
T 216,195,40960
C 158,180,181
)"
R"(Mvvz 4,224,0
T 218,195,43008
C 159,182,183
Mvv 4,228,24
buffer_store_dwordx4 v[156:159], v224, s[28:31], s60 offen offset:1024 sc1
Mvv 4,232,48
T 220,195,45056
T 222,195,47104
Mvv 4,236,72
C 166,188,189
C 167,190,191
Mvvz 12,224,8
buffer_store_dwordx4 v[164:167], v225, s[28:31], s60 offen offset:1024 sc1
Mvv 12,228,32
T 224,195,49152
T 226,195,51200
)"
R"(Mvv 12,232,56
T 228,195,53248
T 230,195,55296
Mvv 12,236,80
W 8
T 232,195,57344
T 234,195,59392
Mvv 4,240,96
T 236,195,61440
T 238,195,63488
Mvv 4,244,120
Mvv 4,248,144
Mvv 4,252,168
Mvv 12,240,104
T 240,194,33280
T 242,194,35328
)"
R"(Mvv 12,244,128
T 244,194,37376
T 246,194,39424
Mvv 12,248,152
T 248,194,41472
T 250,194,43520
Mvv 12,252,176
T 252,194,45568
C 4,4,5
Mvvz 28,208,0
T 254,194,47616
C 5,6,7
Mvv 28,212,24
W 8
Mvv 28,216,48
Mvv 28,220,72
)"
R"(C 12,12,13
Mvvz 36,208,8
T 208,194,49664
C 13,14,15
Mvv 36,212,32
T 210,194,51712
T 212,194,53760
Mvv 36,216,56
T 214,194,55808
T 216,194,57856
Mvv 36,220,80
T 218,194,59904
T 220,194,61952
Mvv 28,224,96
T 222,194,64000
Mvv 28,228,120
)"
R"(Mvv 28,232,144
Mvv 28,236,168
Mvv 36,224,104
T 224,195,33280
T 226,195,35328
Mvv 36,228,128
T 228,195,37376
T 230,195,39424
Mvv 36,232,152
T 232,195,41472
T 234,195,43520
Mvv 36,236,176
W 9
T 236,195,45568
C 6,28,29
Mvvz 52,240,0
)"
R"(T 238,195,47616
C 7,30,31
Mvv 52,244,24
buffer_store_dwordx4 v[4:7], v223, s[28:31], s61 offen offset:0 sc1
Mvv 52,248,48
Mvv 52,252,72
C 14,36,37
C 15,38,39
Mvvz 60,240,8
buffer_store_dwordx4 v[12:15], v224, s[28:31], s61 offen offset:0 sc1
Mvv 60,244,32
Mvv 60,248,56
Mvv 60,252,80
Mvvz 68,240,16
T 240,195,49664
T 242,195,51712
)"
R"(Mvv 68,244,40
T 244,195,53760
T 246,195,55808
Mvv 68,248,64
T 248,195,57856
T 250,195,59904
Mvv 68,252,88
T 252,195,61952
T 254,195,64000
Mvv 52,208,96
buffer_load_dword v237, v207, s[72:75], 0 offen sc1
Mvv 52,212,120
W 8
Mvv 52,216,144
Mvv 52,220,168
Mvv 60,208,104
)"
R"(Mvv 60,212,128
Mvv 60,216,152
Mvv 60,220,176
s_nop 3
C 52,52,53
Mvv 68,208,112
C 53,54,55
Mvv 68,212,136
Mvv 68,216,160
Mvv 68,220,184
C 60,60,61
Mvvz 76,224,0
C 61,62,63
Mvv 76,228,24
Mvv 76,232,48
Mvv 76,236,72
)"
R"(s_nop 1
C 68,68,69
Mvvz 84,224,8
C 69,70,71
Mvv 84,228,32
Mvv 84,232,56
Mvv 84,236,80
Mvvz 92,224,16
Mvv 92,228,40
Mvv 92,232,64
Mvv 92,236,88
W 0
Mvv 76,240,96
Mvv 76,244,120
Mvv 76,248,144
Mvv 76,252,168
)"
R"(Mvv 84,240,104
Mvv 84,244,128
Mvv 84,248,152
Mvv 84,252,176
s_nop 3
C 54,76,77
C 55,78,79
Mvv 92,240,112
buffer_store_dwordx4 v[52:55], v223, s[28:31], s61 offen offset:1024 sc1
Mvv 92,244,136
Mvv 92,248,160
Mvv 92,252,184
C 62,84,85
C 63,86,87
buffer_store_dwordx4 v[60:63], v224, s[28:31], s61 offen offset:1024 sc1
s_nop 4
)"
R"(C 70,92,93
C 71,94,95
buffer_store_dwordx4 v[68:71], v225, s[28:31], s61 offen offset:1024 sc1
s_waitcnt vmcnt(3)
s_branch .Ltail
.Lheavy:
s_getpc_b64 s[80:81]
.Lpc:
s_sub_u32 s80, s80, .Lpc-.Lbegin
s_subb_u32 s81, s81, 0
s_lshr_b32 s82, %1, 3
s_lshl1_add_u32 s82, s82, s76
s_lshl_b32 s82, s82, 11
v_lshl_add_u32 v6, v0, 5, s82
v_min_u32 v6, .Lend-.Lbegin-4, v6
global_load_dword v7, v6, s[80:81]
)"
R"(W 0
s_cmp_ge_u32 s84, 7
s_cselect_b32 s79, 0, s84
s_lshl_b32 s48, s79, 7
s_add_u32 s49, s48, 128
s_cmp_eq_u32 s49, 896
s_cselect_b32 s49, 0, s49
s_add_u32 s50, s49, 128
s_cmp_eq_u32 s50, 896
s_cselect_b32 s50, 0, s50
s_add_u32 s51, s50, 128
s_cmp_eq_u32 s51, 896
s_cselect_b32 s51, 0, s51
s_add_u32 s52, s51, 128
s_cmp_eq_u32 s52, 896
s_cselect_b32 s52, 0, s52
)"
R"(s_add_u32 s53, s52, 128
s_cmp_eq_u32 s53, 896
s_cselect_b32 s53, 0, s53
s_add_u32 s54, s53, 128
s_cmp_eq_u32 s54, 896
s_cselect_b32 s54, 0, s54
s_mul_i32 s80, s86, 143360
s_add_u32 s28, s4, s80
s_addc_u32 s29, s5, 0
s_and_b32 s29, s29, 0xffff
s_mov_b32 s30, 143360
s_mov_b32 s31, 0x20000
s_mov_b32 s55, 0
s_mov_b32 s56, 20480
s_mov_b32 s57, 40960
s_mov_b32 s58, 61440
)"
R"(s_mov_b32 s59, 81920
s_mov_b32 s60, 102400
s_mov_b32 s61, 122880
s_mov_b32 s63, 0x3db8aa3b
s_mov_b32 s65, 0x42b17218
s_and_b32 s81, s76, 1
s_mul_i32 s82, s81, 3
s_mul_i32 s83, s81, 3
s_add_u32 s83, s83, 2
v_lshrrev_b32 v2, 5, v0
v_lshlrev_b32 v2, 10, v2
v_bfe_u32 v3, v0, 2, 3
v_lshl_add_u32 v2, v3, 6, v2
v_and_b32 v3, 3, v0
v_lshl_add_u32 v2, v3, 3, v2
v_bfe_u32 v3, v0, 4, 1
)"
R"(v_lshl_add_u32 v192, v3, 5, v2
v_xor_b32 v193, 32, v192
v_and_b32 v2, 15, v0
v_bfe_u32 v4, v2, 1, 1
v_bfe_u32 v5, v2, 3, 1
v_lshl_or_b32 v4, v5, 1, v4
v_bfe_u32 v6, v2, 2, 1
v_xor_b32 v5, v5, v6
v_lshl_or_b32 v4, v5, 2, v4
v_lshrrev_b32 v5, 5, v0
v_lshl_or_b32 v5, v3, 1, v5
v_xor_b32 v4, v4, v5
v_lshlrev_b32 v4, 4, v4
v_lshl_add_u32 v198, v2, 7, v4
v_xor_b32 v199, 64, v198
v_add_u32 v194, 65536, v192
)"
R"(v_add_u32 v200, 65536, v198
v_add_u32 v195, 65536, v193
v_add_u32 v201, 65536, v199
v_add_u32 v196, 131072, v192
v_add_u32 v202, 131072, v198
v_add_u32 v197, 131072, v193
v_add_u32 v203, 131072, v199
v_add_u32 v2, 32768, v198
v_lshl_add_u32 v204, s82, 11, v2
v_lshl_add_u32 v206, s83, 11, v2
v_add_u32 v2, 32768, v199
v_lshl_add_u32 v205, s82, 11, v2
v_lshl_add_u32 v207, s83, 11, v2
v_lshlrev_b32 v2, 4, v0
s_mul_i32 s80, s82, 2048
v_add_u32 v223, s80, v2
)"
R"(v_add_u32 v224, 2048, v223
s_mul_i32 s80, s83, 2048
v_add_u32 v225, s80, v2
v_lshl_add_u32 v231, s81, 12, v2
v_add_u32 v231, 57344, v231
v_lshl_add_u32 v232, s81, 14, v2
v_add_u32 v232, 98304, v232
s_lshl4_add_u32 s94, s78, s82
s_lshl_b32 s94, s94, 6
s_add_u32 s94, s94, 1024
s_mov_b32 s80, 0x3f803f80
v_accvgpr_write_b32 a204, s80
v_accvgpr_write_b32 a205, s80
v_accvgpr_write_b32 a206, s80
v_accvgpr_write_b32 a207, s80
s_mul_i32 s80, s77, 32768
)"
R"(s_add_u32 s80, s80, 50331648
s_add_u32 s72, s4, s80
s_addc_u32 s73, s5, 0
s_and_b32 s73, s73, 0xffff
s_mov_b32 s74, 17408
s_mov_b32 s75, 0x20000
v_lshlrev_b32 v238, 2, v0
s_cmp_eq_u32 s76, 3
s_cselect_b32 s80, s78, 64
v_cmp_eq_u32 vcc, s80, v0
v_mov_b32 v2, 0x100000
s_nop 0
v_cndmask_b32 v252, v2, v238, vcc
buffer_load_dword v233, v238, s[72:75], 0 offen sc1
s_waitcnt vmcnt(0)
W 0
)"
R"(s_barrier
N 0,204,0
N 4,204,2048
N 8,206,0
N 12,205,0
N 16,205,2048
N 20,207,0
T 208,192,0
T 210,192,2048
W 0
T 212,192,4096
T 214,192,6144
T 216,193,0
T 218,193,2048
T 220,193,4096
T 222,193,6144
)"
R"(Mvaz 0,208,0
W 4
T 224,192,512
T 226,192,2560
Mva 0,212,12
T 228,192,4608
T 230,192,6656
Mvaz 8,208,4
T 232,193,512
T 234,193,2560
Mva 8,212,16
T 236,193,4608
Mvaz 16,208,8
T 238,193,6656
Mva 16,212,20
W 10
)"
R"(T 240,192,8192
Mvaz 4,216,0
W 9
T 242,192,10240
Mva 4,220,12
T 244,192,12288
Mvaz 12,216,4
T 246,192,14336
Mva 12,220,16
T 248,193,8192
Mvaz 20,216,8
T 250,193,10240
Mva 20,220,20
W 8
T 252,193,12288
Mvaz 24,224,0
)"
R"(T 254,193,14336
Mva 24,228,12
T 208,192,8704
Mvaz 32,224,4
T 210,192,10752
Mva 32,228,16
T 212,192,12800
Mvaz 40,224,8
T 214,192,14848
Mva 40,228,20
T 216,193,8704
Mvaz 28,232,0
W 8
T 218,193,10752
Mva 28,236,12
T 220,193,12800
)"
R"(Mvaz 36,232,4
T 222,193,14848
Mva 36,236,16
T 224,192,16384
Mvaz 44,232,8
T 226,192,18432
Mva 44,236,20
T 228,192,20480
Mvaz 48,240,0
T 230,192,22528
Mva 48,244,12
T 232,193,16384
Mvaz 56,240,4
T 234,193,18432
Mva 56,244,16
T 236,193,20480
)"
R"(Mvaz 64,240,8
T 238,193,22528
Mva 64,244,20
W 8
T 240,192,16896
Mvaz 52,248,0
T 242,192,18944
Mva 52,252,12
T 244,192,20992
Mvaz 60,248,4
T 246,192,23040
Mva 60,252,16
Mvaz 68,248,8
T 248,193,16896
Mva 68,252,20
T 250,193,18944
)"
R"(Mvaz 72,208,0
T 252,193,20992
Mva 72,212,12
T 254,193,23040
Mvaz 80,208,4
Mva 80,212,16
Mvaz 88,208,8
T 208,192,24576
Mva 88,212,20
T 210,192,26624
Mvaz 76,216,0
T 212,192,28672
Mva 76,220,12
T 214,192,30720
Mvaz 84,216,4
Mva 84,220,16
)"
R"(Mvaz 92,216,8
T 216,193,24576
Mva 92,220,20
W 5
T 218,193,26624
Mvaz 96,224,0
T 220,193,28672
Mva 96,228,12
T 222,193,30720
Mvaz 104,224,4
Mva 104,228,16
Mvaz 112,224,8
T 224,192,25088
Mva 112,228,20
T 226,192,27136
Mvaz 100,232,0
)"
R"(T 228,192,29184
Mva 100,236,12
T 230,192,31232
Mvaz 108,232,4
Mva 108,236,16
Mvaz 116,232,8
T 232,193,25088
Mva 116,236,20
T 234,193,27136
Mvaz 120,240,0
T 236,193,29184
Mva 120,244,12
T 238,193,31232
Mvaz 128,240,4
Mva 128,244,16
Mvaz 136,240,8
)"
R"(Mva 136,244,20
Mvaz 124,248,0
Mva 124,252,12
Mvaz 132,248,4
Mva 132,252,16
Mvaz 140,248,8
Mva 140,252,20
W 0
Mvaz 144,208,0
Mva 144,212,12
Mvaz 152,208,4
Mva 152,212,16
Mvaz 160,208,8
Mva 160,212,20
Mvaz 148,216,0
Mva 148,220,12
)"
R"(Mvaz 156,216,4
Mva 156,220,16
Mvaz 164,216,8
Mva 164,220,20
s_waitcnt vmcnt(0) lgkmcnt(0)
s_barrier
N 24,204,20480
Mvaz 168,224,0
N 28,204,22528
N 32,206,20480
Mva 168,228,12
N 36,205,20480
N 40,205,22528
Mvaz 176,224,4
N 44,207,20480
T 240,194,32768
)"
R"(Mva 176,228,16
T 242,194,34816
T 244,194,36864
Mvaz 184,224,8
T 246,194,38912
T 248,195,32768
Mva 184,228,20
T 250,195,34816
T 252,195,36864
Mvaz 172,232,0
T 254,195,38912
Mva 172,236,12
T 208,194,33280
Mvaz 180,232,4
T 210,194,35328
Mva 180,236,16
)"
R"(T 212,194,37376
Mvaz 188,232,8
T 214,194,39424
Mva 188,236,20
W 10
T 216,195,33280
Mva 0,240,24
W 9
T 218,195,35328
Mva 0,244,36
T 220,195,37376
Mva 8,240,28
T 222,195,39424
Mva 8,244,40
T 224,194,40960
Mva 16,240,32
)"
R"(T 226,194,43008
Mva 16,244,44
W 8
T 228,194,45056
Mva 4,248,24
T 230,194,47104
Mva 4,252,36
T 232,195,40960
Mva 12,248,28
T 234,195,43008
Mva 12,252,40
T 236,195,45056
Mva 20,248,32
T 238,195,47104
Mva 20,252,44
T 240,194,41472
)"
R"(Mva 24,208,24
W 8
T 242,194,43520
Mva 24,212,36
T 244,194,45568
Mva 32,208,28
T 246,194,47616
Mva 32,212,40
T 248,195,41472
Mva 40,208,32
T 250,195,43520
Mva 40,212,44
T 252,195,45568
Mva 28,216,24
T 254,195,47616
Mva 28,220,36
)"
R"(T 208,194,49152
Mva 36,216,28
T 210,194,51200
Mva 36,220,40
T 212,194,53248
Mva 44,216,32
T 214,194,55296
Mva 44,220,44
W 8
T 216,195,49152
Mva 48,224,24
T 218,195,51200
Mva 48,228,36
T 220,195,53248
Mva 56,224,28
T 222,195,55296
)"
R"(Mva 56,228,40
Mva 64,224,32
T 224,194,49664
Mva 64,228,44
T 226,194,51712
Mva 52,232,24
T 228,194,53760
Mva 52,236,36
T 230,194,55808
Mva 60,232,28
Mva 60,236,40
Mva 68,232,32
T 232,195,49664
Mva 68,236,44
T 234,195,51712
Mva 72,240,24
)"
R"(T 236,195,53760
Mva 72,244,36
T 238,195,55808
Mva 80,240,28
Mva 80,244,40
Mva 88,240,32
T 240,194,57344
Mva 88,244,44
W 5
T 242,194,59392
Mva 76,248,24
T 244,194,61440
Mva 76,252,36
T 246,194,63488
Mva 84,248,28
Mva 84,252,40
)"
R"(Mva 92,248,32
T 248,195,57344
Mva 92,252,44
T 250,195,59392
Mva 96,208,24
T 252,195,61440
Mva 96,212,36
T 254,195,63488
Mva 104,208,28
Mva 104,212,40
Mva 112,208,32
T 208,194,57856
Mva 112,212,44
T 210,194,59904
Mva 100,216,24
T 212,194,61952
)"
R"(Mva 100,220,36
T 214,194,64000
Mva 108,216,28
Mva 108,220,40
Mva 116,216,32
T 216,195,57856
Mva 116,220,44
T 218,195,59904
Mva 120,224,24
T 220,195,61952
Mva 120,228,36
T 222,195,64000
Mva 128,224,28
Mva 128,228,40
Mva 136,224,32
Mva 136,228,44
)"
R"(W 4
Mva 124,232,24
Mva 124,236,36
Mva 132,232,28
Mva 132,236,40
Mva 140,232,32
Mva 140,236,44
Mva 144,240,24
Mva 144,244,36
Mva 152,240,28
Mva 152,244,40
Mva 160,240,32
Mva 160,244,44
Mva 148,248,24
Mva 148,252,36
Mva 156,248,28
)"
R"(Mva 156,252,40
Mva 164,248,32
Mva 164,252,44
W 0
s_barrier
N 0,204,40960
Mva 168,208,24
N 4,204,43008
N 8,206,40960
Mva 168,212,36
N 12,205,40960
N 16,205,43008
Mva 176,208,28
N 20,207,40960
T 224,196,0
Mva 176,212,40
)"
R"(T 226,196,2048
T 228,196,4096
Mva 184,208,32
T 230,196,6144
T 232,197,0
Mva 184,212,44
T 234,197,2048
T 236,197,4096
Mva 172,216,24
T 238,197,6144
Mva 172,220,36
T 240,196,512
Mva 180,216,28
T 242,196,2560
Mva 180,220,40
T 244,196,4608
)"
R"(Mva 188,216,32
T 246,196,6656
Mva 188,220,44
W 10
T 248,197,512
Mva 0,224,0
W 9
T 250,197,2560
Mva 0,228,12
T 252,197,4608
Mva 8,224,4
T 254,197,6656
Mva 8,228,16
T 208,196,8192
Mva 16,224,8
T 210,196,10240
)"
R"(Mva 16,228,20
W 8
T 212,196,12288
Mva 4,232,0
T 214,196,14336
Mva 4,236,12
T 216,197,8192
Mva 12,232,4
T 218,197,10240
Mva 12,236,16
T 220,197,12288
Mva 20,232,8
T 222,197,14336
Mva 20,236,20
T 224,196,8704
Mva 24,240,0
)"
R"(W 8
T 226,196,10752
Mva 24,244,12
T 228,196,12800
Mva 32,240,4
T 230,196,14848
Mva 32,244,16
T 232,197,8704
Mva 40,240,8
T 234,197,10752
Mva 40,244,20
T 236,197,12800
Mva 28,248,0
T 238,197,14848
Mva 28,252,12
T 240,196,16384
)"
R"(Mva 36,248,4
T 242,196,18432
Mva 36,252,16
T 244,196,20480
Mva 44,248,8
T 246,196,22528
Mva 44,252,20
W 8
T 248,197,16384
Mva 48,208,0
T 250,197,18432
Mva 48,212,12
T 252,197,20480
Mva 56,208,4
T 254,197,22528
Mva 56,212,16
)"
R"(Mva 64,208,8
T 208,196,16896
Mva 64,212,20
T 210,196,18944
Mva 52,216,0
T 212,196,20992
Mva 52,220,12
T 214,196,23040
Mva 60,216,4
Mva 60,220,16
Mva 68,216,8
T 216,197,16896
Mva 68,220,20
T 218,197,18944
Mva 72,224,0
T 220,197,20992
)"
R"(Mva 72,228,12
T 222,197,23040
Mva 80,224,4
Mva 80,228,16
Mva 88,224,8
T 224,196,24576
Mva 88,228,20
W 5
T 226,196,26624
Mva 76,232,0
T 228,196,28672
Mva 76,236,12
T 230,196,30720
Mva 84,232,4
Mva 84,236,16
Mva 92,232,8
)"
R"(T 232,197,24576
Mva 92,236,20
T 234,197,26624
Mva 96,240,0
T 236,197,28672
Mva 96,244,12
T 238,197,30720
Mva 104,240,4
Mva 104,244,16
Mva 112,240,8
T 240,196,25088
Mva 112,244,20
T 242,196,27136
Mva 100,248,0
T 244,196,29184
Mva 100,252,12
)"
R"(T 246,196,31232
Mva 108,248,4
Mva 108,252,16
Mva 116,248,8
T 248,197,25088
Mva 116,252,20
T 250,197,27136
Mva 120,208,0
T 252,197,29184
Mva 120,212,12
T 254,197,31232
Mva 128,208,4
Mva 128,212,16
Mva 136,208,8
Mva 136,212,20
W 4
)"
R"(Mva 124,216,0
Mva 124,220,12
Mva 132,216,4
Mva 132,220,16
Mva 140,216,8
Mva 140,220,20
Mva 144,224,0
Mva 144,228,12
Mva 152,224,4
Mva 152,228,16
Mva 160,224,8
Mva 160,228,20
Mva 148,232,0
Mva 148,236,12
Mva 156,232,4
Mva 156,236,16
)"
R"(Mva 164,232,8
Mva 164,236,20
W 0
s_barrier
N 24,204,0
Mva 168,240,0
N 28,204,2048
N 32,206,0
Mva 168,244,12
N 36,205,0
N 40,205,2048
Mva 176,240,4
N 44,207,0
T 208,192,0
Mva 176,244,16
T 210,192,2048
)"
R"(T 212,192,4096
Mva 184,240,8
T 214,192,6144
T 216,193,0
Mva 184,244,20
T 218,193,2048
T 220,193,4096
Mva 172,248,0
T 222,193,6144
Mva 172,252,12
T 224,192,512
Mva 180,248,4
T 226,192,2560
Mva 180,252,16
T 228,192,4608
Mva 188,248,8
)"
R"(T 230,192,6656
Mva 188,252,20
W 10
T 232,193,512
Mva 0,208,24
W 9
T 234,193,2560
Mva 0,212,36
T 236,193,4608
Mva 8,208,28
T 238,193,6656
Mva 8,212,40
T 240,192,8192
Mva 16,208,32
T 242,192,10240
Mva 16,212,44
)"
R"(W 8
T 244,192,12288
Mva 4,216,24
T 246,192,14336
Mva 4,220,36
T 248,193,8192
Mva 12,216,28
T 250,193,10240
Mva 12,220,40
T 252,193,12288
Mva 20,216,32
T 254,193,14336
Mva 20,220,44
T 208,192,8704
Mva 24,224,24
W 8
)"
R"(T 210,192,10752
Mva 24,228,36
T 212,192,12800
Mva 32,224,28
T 214,192,14848
Mva 32,228,40
T 216,193,8704
Mva 40,224,32
T 218,193,10752
Mva 40,228,44
T 220,193,12800
Mva 28,232,24
T 222,193,14848
Mva 28,236,36
T 224,192,16384
Mva 36,232,28
)"
R"(T 226,192,18432
Mva 36,236,40
T 228,192,20480
Mva 44,232,32
T 230,192,22528
Mva 44,236,44
W 8
T 232,193,16384
Mva 48,240,24
T 234,193,18432
Mva 48,244,36
T 236,193,20480
Mva 56,240,28
T 238,193,22528
Mva 56,244,40
Mva 64,240,32
)"
R"(T 240,192,16896
Mva 64,244,44
T 242,192,18944
Mva 52,248,24
T 244,192,20992
Mva 52,252,36
T 246,192,23040
Mva 60,248,28
Mva 60,252,40
Mva 68,248,32
T 248,193,16896
Mva 68,252,44
T 250,193,18944
Mva 72,208,24
T 252,193,20992
Mva 72,212,36
)"
R"(T 254,193,23040
Mva 80,208,28
Mva 80,212,40
Mva 88,208,32
T 208,192,24576
Mva 88,212,44
W 5
T 210,192,26624
Mva 76,216,24
T 212,192,28672
Mva 76,220,36
T 214,192,30720
Mva 84,216,28
Mva 84,220,40
Mva 92,216,32
T 216,193,24576
)"
R"(Mva 92,220,44
T 218,193,26624
Mva 96,224,24
T 220,193,28672
Mva 96,228,36
T 222,193,30720
Mva 104,224,28
Mva 104,228,40
Mva 112,224,32
T 224,192,25088
Mva 112,228,44
T 226,192,27136
Mva 100,232,24
T 228,192,29184
Mva 100,236,36
T 230,192,31232
)"
R"(Mva 108,232,28
Mva 108,236,40
Mva 116,232,32
T 232,193,25088
Mva 116,236,44
T 234,193,27136
Mva 120,240,24
T 236,193,29184
Mva 120,244,36
T 238,193,31232
Mva 128,240,28
Mva 128,244,40
Mva 136,240,32
Mva 136,244,44
W 4
Mva 124,248,24
)"
R"(Mva 124,252,36
Mva 132,248,28
Mva 132,252,40
Mva 140,248,32
Mva 140,252,44
Mva 144,208,24
Mva 144,212,36
Mva 152,208,28
Mva 152,212,40
Mva 160,208,32
Mva 160,212,44
Mva 148,216,24
Mva 148,220,36
Mva 156,216,28
Mva 156,220,40
Mva 164,216,32
)"
R"(Mva 164,220,44
W 0
s_barrier
N 0,204,20480
Mva 168,224,24
N 4,204,22528
N 8,206,20480
Mva 168,228,36
N 12,205,20480
N 16,205,22528
Mva 176,224,28
N 20,207,20480
T 240,194,32768
Mva 176,228,40
T 242,194,34816
T 244,194,36864
)"
R"(Mva 184,224,32
T 246,194,38912
T 248,195,32768
Mva 184,228,44
T 250,195,34816
T 252,195,36864
Mva 172,232,24
T 254,195,38912
Mva 172,236,36
T 208,194,33280
Mva 180,232,28
T 210,194,35328
Mva 180,236,40
T 212,194,37376
Mva 188,232,32
T 214,194,39424
)"
R"(Mva 188,236,44
W 10
T 216,195,33280
Mva 0,240,0
W 9
T 218,195,35328
Mva 0,244,12
T 220,195,37376
Mva 8,240,4
T 222,195,39424
Mva 8,244,16
T 224,194,40960
Mva 16,240,8
T 226,194,43008
Mva 16,244,20
W 8
)"
R"(T 228,194,45056
Mva 4,248,0
T 230,194,47104
Mva 4,252,12
T 232,195,40960
Mva 12,248,4
T 234,195,43008
Mva 12,252,16
T 236,195,45056
Mva 20,248,8
T 238,195,47104
Mva 20,252,20
T 240,194,41472
Mva 24,208,0
W 8
T 242,194,43520
)"
R"(Mva 24,212,12
T 244,194,45568
Mva 32,208,4
T 246,194,47616
Mva 32,212,16
T 248,195,41472
Mva 40,208,8
T 250,195,43520
Mva 40,212,20
T 252,195,45568
Mva 28,216,0
T 254,195,47616
Mva 28,220,12
T 208,194,49152
Mva 36,216,4
T 210,194,51200
)"
R"(Mva 36,220,16
T 212,194,53248
Mva 44,216,8
T 214,194,55296
Mva 44,220,20
W 8
T 216,195,49152
Mva 48,224,0
T 218,195,51200
Mva 48,228,12
T 220,195,53248
Mva 56,224,4
T 222,195,55296
Mva 56,228,16
Mva 64,224,8
T 224,194,49664
)"
R"(Mva 64,228,20
T 226,194,51712
Mva 52,232,0
T 228,194,53760
Mva 52,236,12
T 230,194,55808
Mva 60,232,4
Mva 60,236,16
Mva 68,232,8
T 232,195,49664
Mva 68,236,20
T 234,195,51712
Mva 72,240,0
T 236,195,53760
Mva 72,244,12
T 238,195,55808
)"
R"(Mva 80,240,4
Mva 80,244,16
Mva 88,240,8
T 240,194,57344
Mva 88,244,20
W 5
T 242,194,59392
Mva 76,248,0
T 244,194,61440
Mva 76,252,12
T 246,194,63488
Mva 84,248,4
Mva 84,252,16
Mva 92,248,8
T 248,195,57344
Mva 92,252,20
)"
R"(T 250,195,59392
Mva 96,208,0
T 252,195,61440
Mva 96,212,12
T 254,195,63488
Mva 104,208,4
Mva 104,212,16
Mva 112,208,8
T 208,194,57856
Mva 112,212,20
T 210,194,59904
Mva 100,216,0
T 212,194,61952
Mva 100,220,12
T 214,194,64000
Mva 108,216,4
)"
R"(Mva 108,220,16
Mva 116,216,8
T 216,195,57856
Mva 116,220,20
T 218,195,59904
Mva 120,224,0
T 220,195,61952
Mva 120,228,12
T 222,195,64000
Mva 128,224,4
Mva 128,228,16
Mva 136,224,8
Mva 136,228,20
W 4
Mva 124,232,0
Mva 124,236,12
)"
R"(Mva 132,232,4
Mva 132,236,16
Mva 140,232,8
Mva 140,236,20
Mva 144,240,0
Mva 144,244,12
Mva 152,240,4
Mva 152,244,16
Mva 160,240,8
Mva 160,244,20
Mva 148,248,0
Mva 148,252,12
Mva 156,248,4
Mva 156,252,16
Mva 164,248,8
Mva 164,252,20
)"
R"(W 0
s_barrier
N 24,204,40960
Mva 168,208,0
N 28,204,43008
N 32,206,40960
Mva 168,212,12
N 36,205,40960
N 40,205,43008
Mva 176,208,4
N 44,207,40960
T 224,196,0
Mva 176,212,16
T 226,196,2048
T 228,196,4096
Mva 184,208,8
)"
R"(T 230,196,6144
T 232,197,0
Mva 184,212,20
T 234,197,2048
T 236,197,4096
Mva 172,216,0
T 238,197,6144
Mva 172,220,12
T 240,196,512
Mva 180,216,4
T 242,196,2560
Mva 180,220,16
T 244,196,4608
Mva 188,216,8
T 246,196,6656
Mva 188,220,20
)"
R"(W 10
T 248,197,512
Mva 0,224,24
W 9
T 250,197,2560
Mva 0,228,36
T 252,197,4608
Mva 8,224,28
T 254,197,6656
Mva 8,228,40
T 208,196,8192
Mva 16,224,32
T 210,196,10240
Mva 16,228,44
W 8
T 212,196,12288
)"
R"(Mva 4,232,24
T 214,196,14336
Mva 4,236,36
T 216,197,8192
Mva 12,232,28
T 218,197,10240
Mva 12,236,40
T 220,197,12288
Mva 20,232,32
T 222,197,14336
Mva 20,236,44
T 224,196,8704
Mva 24,240,24
W 8
T 226,196,10752
Mva 24,244,36
)"
R"(T 228,196,12800
Mva 32,240,28
T 230,196,14848
Mva 32,244,40
T 232,197,8704
Mva 40,240,32
T 234,197,10752
Mva 40,244,44
T 236,197,12800
Mva 28,248,24
T 238,197,14848
Mva 28,252,36
T 240,196,16384
Mva 36,248,28
T 242,196,18432
Mva 36,252,40
)"
R"(T 244,196,20480
Mva 44,248,32
T 246,196,22528
Mva 44,252,44
W 8
T 248,197,16384
Mva 48,208,24
T 250,197,18432
Mva 48,212,36
T 252,197,20480
Mva 56,208,28
T 254,197,22528
Mva 56,212,40
Mva 64,208,32
T 208,196,16896
Mva 64,212,44
)"
R"(T 210,196,18944
Mva 52,216,24
T 212,196,20992
Mva 52,220,36
T 214,196,23040
Mva 60,216,28
Mva 60,220,40
Mva 68,216,32
T 216,197,16896
Mva 68,220,44
T 218,197,18944
Mva 72,224,24
T 220,197,20992
Mva 72,228,36
T 222,197,23040
Mva 80,224,28
)"
R"(Mva 80,228,40
Mva 88,224,32
T 224,196,24576
Mva 88,228,44
W 5
T 226,196,26624
Mva 76,232,24
T 228,196,28672
Mva 76,236,36
T 230,196,30720
Mva 84,232,28
Mva 84,236,40
Mva 92,232,32
T 232,197,24576
Mva 92,236,44
T 234,197,26624
)"
R"(Mva 96,240,24
T 236,197,28672
Mva 96,244,36
T 238,197,30720
Mva 104,240,28
Mva 104,244,40
Mva 112,240,32
T 240,196,25088
Mva 112,244,44
T 242,196,27136
Mva 100,248,24
T 244,196,29184
Mva 100,252,36
T 246,196,31232
Mva 108,248,28
Mva 108,252,40
)"
R"(Mva 116,248,32
T 248,197,25088
Mva 116,252,44
T 250,197,27136
Mva 120,208,24
T 252,197,29184
Mva 120,212,36
T 254,197,31232
Mva 128,208,28
Mva 128,212,40
Mva 136,208,32
Mva 136,212,44
W 4
Mva 124,216,24
Mva 124,220,36
Mva 132,216,28
)"
R"(Mva 132,220,40
Mva 140,216,32
Mva 140,220,44
Mva 144,224,24
Mva 144,228,36
Mva 152,224,28
Mva 152,228,40
Mva 160,224,32
Mva 160,228,44
Mva 148,232,24
Mva 148,236,36
Mva 156,232,28
Mva 156,236,40
Mva 164,232,32
Mva 164,236,44
W 0
)"
R"(s_barrier
N 0,204,0
Mva 168,240,24
N 4,204,2048
N 8,206,0
Mva 168,244,36
N 12,205,0
N 16,205,2048
Mva 176,240,28
N 20,207,0
T 208,192,0
Mva 176,244,40
T 210,192,2048
T 212,192,4096
Mva 184,240,32
T 214,192,6144
)"
R"(T 216,193,0
Mva 184,244,44
T 218,193,2048
T 220,193,4096
Mva 172,248,24
T 222,193,6144
Mva 172,252,36
T 224,192,512
Mva 180,248,28
T 226,192,2560
Mva 180,252,40
T 228,192,4608
Mva 188,248,32
T 230,192,6656
Mva 188,252,44
W 10
)"
R"(T 232,193,512
Mva 0,208,0
W 9
T 234,193,2560
Mva 0,212,12
T 236,193,4608
Mva 8,208,4
T 238,193,6656
Mva 8,212,16
T 240,192,8192
Mva 16,208,8
T 242,192,10240
Mva 16,212,20
W 8
T 244,192,12288
C 0,0,1
)"
R"(Mva 4,216,0
T 246,192,14336
C 1,2,3
Mva 4,220,12
T 248,193,8192
C 8,8,9
Mva 12,216,4
T 250,193,10240
C 9,10,11
Mva 12,220,16
T 252,193,12288
C 16,16,17
Mva 20,216,8
T 254,193,14336
C 17,18,19
Mva 20,220,20
)"
R"(T 208,192,8704
C 2,4,5
Mva 24,224,0
W 8
T 210,192,10752
C 3,6,7
Mva 24,228,12
T 212,192,12800
C 10,12,13
Mva 32,224,4
T 214,192,14848
C 11,14,15
Mva 32,228,16
T 216,193,8704
C 18,20,21
Mva 40,224,8
)"
R"(T 218,193,10752
C 19,22,23
Mva 40,228,20
T 220,193,12800
C 24,24,25
Mva 28,232,0
T 222,193,14848
C 25,26,27
Mva 28,236,12
T 224,192,16384
C 32,32,33
Mva 36,232,4
T 226,192,18432
C 33,34,35
Mva 36,236,16
T 228,192,20480
)"
R"(C 40,40,41
Mva 44,232,8
T 230,192,22528
C 41,42,43
Mva 44,236,20
W 8
T 232,193,16384
C 26,28,29
Mva 48,240,0
T 234,193,18432
C 27,30,31
Mva 48,244,12
T 236,193,20480
C 34,36,37
Mva 56,240,4
T 238,193,22528
)"
R"(C 35,38,39
Mva 56,244,16
v_permlane16_swap_b32 v0, v2
Mva 64,240,8
T 240,192,16896
C 42,44,45
Mva 64,244,20
T 242,192,18944
C 43,46,47
Mva 52,248,0
T 244,192,20992
C 48,48,49
Mva 52,252,12
T 246,192,23040
C 49,50,51
Mva 60,248,4
)"
R"(v_permlane16_swap_b32 v1, v3
Mva 60,252,16
v_permlane16_swap_b32 v8, v10
Mva 68,248,8
T 248,193,16896
C 56,56,57
Mva 68,252,20
T 250,193,18944
C 50,52,53
Mva 72,208,0
T 252,193,20992
C 51,54,55
Mva 72,212,12
T 254,193,23040
C 57,58,59
Mva 80,208,4
)"
R"(v_permlane16_swap_b32 v9, v11
Mva 80,212,16
v_permlane16_swap_b32 v16, v18
Mva 88,208,8
T 208,192,24576
C 58,60,61
Mva 88,212,20
W 5
T 210,192,26624
C 59,62,63
Mva 76,216,0
T 212,192,28672
C 64,64,65
Mva 76,220,12
T 214,192,30720
C 65,66,67
)"
R"(Mva 84,216,4
v_permlane16_swap_b32 v17, v19
Mva 84,220,16
v_permlane16_swap_b32 v24, v26
Mva 92,216,8
T 216,193,24576
C 66,68,69
Mva 92,220,20
T 218,193,26624
C 67,70,71
Mva 96,224,0
T 220,193,28672
C 72,72,73
Mva 96,228,12
T 222,193,30720
C 73,74,75
)"
R"(Mva 104,224,4
v_permlane16_swap_b32 v25, v27
Mva 104,228,16
v_permlane16_swap_b32 v32, v34
Mva 112,224,8
T 224,192,25088
C 74,76,77
Mva 112,228,20
T 226,192,27136
C 75,78,79
Mva 100,232,0
T 228,192,29184
C 80,80,81
Mva 100,236,12
T 230,192,31232
C 81,82,83
)"
R"(Mva 108,232,4
v_permlane16_swap_b32 v33, v35
Mva 108,236,16
v_permlane16_swap_b32 v40, v42
Mva 116,232,8
T 232,193,25088
C 82,84,85
Mva 116,236,20
T 234,193,27136
C 83,86,87
Mva 120,240,0
T 236,193,29184
C 88,88,89
Mva 120,244,12
T 238,193,31232
C 89,90,91
)"
R"(Mva 128,240,4
v_permlane16_swap_b32 v41, v43
Mva 128,244,16
v_permlane16_swap_b32 v48, v50
Mva 136,240,8
v_permlane16_swap_b32 v49, v51
Mva 136,244,20
W 4
v_permlane16_swap_b32 v56, v58
Mva 124,248,0
v_permlane16_swap_b32 v57, v59
Mva 124,252,12
v_permlane16_swap_b32 v64, v66
Mva 132,248,4
v_permlane16_swap_b32 v65, v67
Mva 132,252,16
)"
R"(v_permlane16_swap_b32 v72, v74
Mva 140,248,8
v_permlane16_swap_b32 v73, v75
Mva 140,252,20
v_permlane16_swap_b32 v80, v82
Mva 144,208,0
v_permlane16_swap_b32 v81, v83
Mva 144,212,12
C 90,92,93
C 91,94,95
Mva 152,208,4
v_permlane16_swap_b32 v88, v90
Mva 152,212,16
v_permlane16_swap_b32 v89, v91
Mva 160,208,8
C 96,96,97
)"
R"(C 104,104,105
Mva 160,212,20
C 97,98,99
C 105,106,107
Mva 148,216,0
C 98,100,101
C 99,102,103
Mva 148,220,12
v_permlane16_swap_b32 v96, v98
Mva 156,216,4
v_permlane16_swap_b32 v97, v99
Mva 156,220,16
C 106,108,109
C 107,110,111
Mva 164,216,8
v_permlane16_swap_b32 v104, v106
)"
R"(Mva 164,220,20
W 0
s_barrier
N 240,200,32768
Mva 168,224,0
N 244,201,32768
C 112,112,113
Mva 168,228,12
N 248,200,34816
C 113,114,115
Mva 176,224,4
N 252,201,34816
C 114,116,117
Mva 176,228,16
N 208,200,40960
C 115,118,119
)"
R"(Mva 184,224,8
N 212,201,40960
C 120,120,121
Mva 184,228,20
N 216,200,43008
C 121,122,123
Mva 172,232,0
N 220,201,43008
C 122,124,125
Mva 172,236,12
N 224,200,49152
C 123,126,127
Mva 180,232,4
N 228,201,49152
C 128,128,129
Mva 180,236,16
)"
R"(v_permlane16_swap_b32 v105, v107
Mva 188,232,8
N 232,200,51200
C 129,130,131
Mva 188,236,20
W 7
N 236,201,51200
C 130,132,133
Mvvz 4,240,0
v_permlane16_swap_b32 v112, v114
Mvv 4,244,24
v_permlane16_swap_b32 v113, v115
Mvv 4,248,48
v_permlane16_swap_b32 v120, v122
Mvv 4,252,72
v_permlane16_swap_b32 v121, v123
)"
R"(Mvvz 12,240,8
v_permlane16_swap_b32 v128, v130
Mvv 12,244,32
C 131,134,135
C 136,136,137
Mvv 12,248,56
v_permlane16_swap_b32 v129, v131
Mvv 12,252,80
C 137,138,139
C 144,144,145
Mvvz 20,240,16
N 240,200,57344
C 138,140,141
Mvv 20,244,40
N 244,201,57344
C 139,142,143
)"
R"(Mvv 20,248,64
N 248,200,59392
C 145,146,147
Mvv 20,252,88
W 3
N 252,201,59392
C 146,148,149
Mvvz 28,208,0
v_permlane16_swap_b32 v136, v138
Mvv 28,212,24
v_permlane16_swap_b32 v137, v139
Mvv 28,216,48
v_permlane16_swap_b32 v144, v146
Mvv 28,220,72
C 147,150,151
C 152,152,153
)"
R"(Mvvz 36,208,8
v_permlane16_swap_b32 v145, v147
Mvv 36,212,32
C 153,154,155
C 160,160,161
Mvv 36,216,56
C 154,156,157
C 155,158,159
Mvv 36,220,80
v_permlane16_swap_b32 v152, v154
Mvvz 44,208,16
N 208,200,36864
C 161,162,163
Mvv 44,212,40
N 212,201,36864
C 162,164,165
)"
R"(Mvv 44,216,64
N 216,200,38912
C 163,166,167
Mvv 44,220,88
N 220,201,38912
C 168,168,169
Mvvz 52,224,0
v_permlane16_swap_b32 v153, v155
Mvv 52,228,24
v_permlane16_swap_b32 v160, v162
Mvv 52,232,48
v_permlane16_swap_b32 v161, v163
Mvv 52,236,72
C 169,170,171
C 176,176,177
Mvvz 60,224,8
)"
R"(C 170,172,173
C 171,174,175
Mvv 60,228,32
v_permlane16_swap_b32 v168, v170
Mvv 60,232,56
v_permlane16_swap_b32 v169, v171
Mvv 60,236,80
C 177,178,179
C 184,184,185
Mvvz 68,224,16
N 224,200,45056
C 178,180,181
Mvv 68,228,40
N 228,201,45056
C 179,182,183
Mvv 68,232,64
)"
R"(N 232,200,47104
C 185,186,187
Mvv 68,236,88
W 3
N 236,201,47104
C 186,188,189
Mvvz 76,240,0
v_permlane16_swap_b32 v176, v178
Mvv 76,244,24
v_permlane16_swap_b32 v177, v179
Mvv 76,248,48
v_permlane16_swap_b32 v184, v186
Mvv 76,252,72
C 187,190,191
Mvvz 84,240,8
Mvv 84,244,32
)"
R"(v_permlane16_swap_b32 v185, v187
Mvv 84,248,56
Mvv 84,252,80
Mvvz 92,240,16
N 240,200,53248
Mvv 92,244,40
N 244,201,53248
Mvv 92,248,64
N 248,200,55296
Mvv 92,252,88
N 252,201,55296
Mvv 4,208,96
Mvv 4,212,120
Mvv 4,216,144
Mvv 4,220,168
Mvv 12,208,104
)"
R"(Mvv 12,212,128
Mvv 12,216,152
Mvv 12,220,176
s_nop 3
v_max_f32 v217, v4, v5
Mvv 20,208,112
N 208,200,61440
v_max3_f32 v217, v217, v6, v7
Mvv 20,212,136
N 212,201,61440
Mvv 20,216,160
N 216,200,63488
Mvv 20,220,184
W 3
N 220,201,63488
v_max_f32 v218, v12, v13
)"
R"(Mvv 28,224,96
v_max3_f32 v218, v218, v14, v15
Mvv 28,228,120
Mvv 28,232,144
Mvv 28,236,168
v_max_f32 v219, v20, v21
Mvv 36,224,104
v_max3_f32 v219, v219, v22, v23
Mvv 36,228,128
Mvv 36,232,152
Mvv 36,236,176
s_nop 1
v_max3_f32 v217, v217, v28, v29
Mvv 44,224,112
v_max3_f32 v217, v217, v30, v31
Mvv 44,228,136
)"
R"(Mvv 44,232,160
Mvv 44,236,184
v_max3_f32 v218, v218, v36, v37
Mvv 52,240,96
v_max3_f32 v218, v218, v38, v39
Mvv 52,244,120
Mvv 52,248,144
Mvv 52,252,168
s_nop 1
v_max3_f32 v219, v219, v44, v45
Mvv 60,240,104
v_max3_f32 v219, v219, v46, v47
Mvv 60,244,128
Mvv 60,248,152
Mvv 60,252,176
v_max3_f32 v217, v217, v52, v53
)"
R"(Mvv 68,240,112
v_max3_f32 v217, v217, v54, v55
Mvv 68,244,136
Mvv 68,248,160
Mvv 68,252,184
W 0
s_barrier
N 224,202,0
Mvv 76,208,96
N 228,203,0
v_max3_f32 v218, v218, v60, v61
Mvv 76,212,120
N 232,202,2048
v_max3_f32 v218, v218, v62, v63
Mvv 76,216,144
N 236,203,2048
)"
R"(Mvv 76,220,168
N 240,202,4096
v_max3_f32 v219, v219, v68, v69
Mvv 84,208,104
N 244,203,4096
v_max3_f32 v219, v219, v70, v71
Mvv 84,212,128
N 248,202,6144
Mvv 84,216,152
N 252,203,6144
Mvv 84,220,176
v_max3_f32 v217, v217, v76, v77
Mvv 92,208,112
N 208,202,8192
v_max3_f32 v217, v217, v78, v79
Mvv 92,212,136
)"
R"(N 212,203,8192
v_mov_b32 v238, v217
Mvv 92,216,160
N 216,202,10240
Mvv 92,220,184
W 7
N 220,203,10240
v_max3_f32 v218, v218, v84, v85
Mvvz 100,224,0
v_permlane32_swap_b32 v217, v238
Mvv 100,228,24
v_max3_f32 v218, v218, v86, v87
v_max_f32 v217, v217, v238
Mvv 100,232,48
v_mov_b32 v239, v218
v_mov_b32 v238, v217
)"
R"(Mvv 100,236,72
v_permlane32_swap_b32 v218, v239
Mvvz 108,224,8
v_permlane16_swap_b32 v217, v238
Mvv 108,228,32
v_max3_f32 v219, v219, v92, v93
v_max_f32 v218, v218, v239
Mvv 108,232,56
v_max3_f32 v219, v219, v94, v95
v_mov_b32 v239, v218
Mvv 108,236,80
v_mov_b32 v240, v219
v_max_f32 v217, v217, v238
Mvvz 116,224,16
N 224,202,12288
v_mov_b32 v208, v217
)"
R"(Mvv 116,228,40
N 228,203,12288
v_mul_f32 v214, -s63, v217
Mvv 116,232,64
N 232,202,14336
v_add_f32 v211, s65, v217
Mvv 116,236,88
W 3
N 236,203,14336
F 4,214
Mvv 100,240,96
v_permlane32_swap_b32 v219, v240
Mvv 100,244,120
v_permlane16_swap_b32 v218, v239
Mvv 100,248,144
E 4
)"
R"(Mvv 100,252,168
v_max_f32 v219, v219, v240
v_max_f32 v218, v218, v239
Mvv 108,240,104
v_mov_b32 v240, v219
v_mov_b32 v209, v218
Mvv 108,244,128
v_permlane16_swap_b32 v219, v240
Mvv 108,248,152
v_max_f32 v219, v219, v240
v_mul_f32 v215, -s63, v218
Mvv 108,252,176
v_mov_b32 v210, v219
v_mul_f32 v216, -s63, v219
Mvv 116,240,112
N 240,202,16384
)"
R"(v_add_f32 v212, s65, v218
Mvv 116,244,136
N 244,203,16384
v_add_f32 v213, s65, v219
Mvv 116,248,160
N 248,202,18432
F 5,214
Mvv 116,252,184
N 252,203,18432
F 6,214
Mvvz 124,208,0
E 5
Mvv 124,212,24
E 6
Mvv 124,216,48
F 7,214
)"
R"(F 28,214
Mvv 124,220,72
E 7
Mvvz 132,208,8
E 28
Mvv 132,212,32
F 29,214
F 30,214
Mvv 132,216,56
E 29
Mvv 132,220,80
E 30
Mvvz 140,208,16
N 208,202,20480
F 31,214
Mvv 140,212,40
)"
R"(N 212,203,20480
C 4,4,5
Mvv 140,216,64
N 216,202,22528
C 5,6,7
Mvv 140,220,88
W 3
N 220,203,22528
C 6,28,29
Mvv 124,224,96
E 31
Mvv 124,228,120
C 7,30,31
F 12,215
Mvv 124,232,144
E 12
)"
R"(Mvv 124,236,168
F 13,215
F 14,215
Mvv 132,224,104
E 13
Mvv 132,228,128
E 14
Mvv 132,232,152
F 15,215
F 36,215
Mvv 132,236,176
E 15
Mvv 140,224,112
N 224,202,24576
F 37,215
Mvv 140,228,136
)"
R"(N 228,203,24576
F 38,215
Mvv 140,232,160
N 232,202,26624
F 39,215
Mvv 140,236,184
N 236,203,26624
C 12,12,13
Mvvz 148,240,0
E 36
Mvv 148,244,24
E 37
Mvv 148,248,48
E 38
Mvv 148,252,72
E 39
)"
R"(Mvvz 156,240,8
C 13,14,15
F 20,216
Mvv 156,244,32
E 20
Mvv 156,248,56
C 14,36,37
C 15,38,39
Mvv 156,252,80
F 21,216
F 22,216
Mvvz 164,240,16
N 240,202,28672
F 23,216
Mvv 164,244,40
N 244,203,28672
)"
R"(F 44,216
Mvv 164,248,64
N 248,202,30720
F 45,216
Mvv 164,252,88
W 3
N 252,203,30720
F 46,216
Mvv 148,208,96
E 21
Mvv 148,212,120
E 22
Mvv 148,216,144
E 23
Mvv 148,220,168
E 44
)"
R"(Mvv 156,208,104
E 45
Mvv 156,212,128
E 46
Mvv 156,216,152
F 47,216
C 20,20,21
Mvv 156,220,176
E 47
Mvv 164,208,112
C 21,22,23
F 52,214
Mvv 164,212,136
E 52
Mvv 164,216,160
C 22,44,45
)"
R"(C 23,46,47
Mvv 164,220,184
F 53,214
F 54,214
Mvvz 172,224,0
E 53
Mvv 172,228,24
E 54
Mvv 172,232,48
F 55,214
F 76,214
Mvv 172,236,72
E 55
Mvvz 180,224,8
E 76
Mvv 180,228,32
)"
R"(F 77,214
F 78,214
Mvv 180,232,56
E 77
Mvv 180,236,80
E 78
Mvvz 188,224,16
F 79,214
C 52,52,53
Mvv 188,228,40
E 79
Mvv 188,232,64
C 53,54,55
F 60,215
Mvv 188,236,88
W 0
)"
R"(s_barrier
T 208,192,0
Mvv 172,240,96
T 210,192,2048
C 54,76,77
Mvv 172,244,120
T 212,193,0
C 55,78,79
Mvv 172,248,144
T 214,193,2048
F 61,215
Mvv 172,252,168
T 216,192,512
F 62,215
Mvv 180,240,104
T 218,192,2560
)"
R"(F 63,215
Mvv 180,244,128
T 220,193,512
F 84,215
Mvv 180,248,152
T 222,193,2560
F 85,215
Mvv 180,252,176
T 224,192,8192
F 86,215
Mvv 188,240,112
T 226,192,10240
F 87,215
Mvv 188,244,136
T 228,193,8192
F 68,216
)"
R"(Mvv 188,248,160
T 230,193,10240
F 69,216
Mvv 188,252,184
W 8
T 232,192,8704
F 70,216
Mavz 0,208,4
T 234,192,10752
F 71,216
Mavz 4,208,12
T 236,193,8704
F 92,216
Mavz 8,208,20
T 238,193,10752
F 93,216
)"
R"(Mavz 12,212,4
T 240,192,4096
F 94,216
Mavz 16,212,12
T 242,192,6144
F 95,216
Mavz 20,212,20
W 8
E 60
Mavz 24,216,4
E 61
Mavz 28,216,12
T 244,193,4096
C 60,60,61
Mavz 32,216,20
E 62
)"
R"(Mavz 36,220,4
E 63
Mavz 40,220,12
T 246,193,6144
C 61,62,63
Mavz 44,220,20
E 84
Mavz 48,224,4
T 248,192,4608
v_max_f32 v217, v100, v101
Mavz 52,224,12
T 250,192,6656
v_max_f32 v218, v108, v109
Mavz 56,224,20
W 4
T 252,193,4608
)"
R"(v_max_f32 v219, v116, v117
Mavz 60,228,4
T 254,193,6656
v_max3_f32 v217, v217, v102, v103
Mavz 64,228,12
T 208,192,12288
v_max3_f32 v218, v218, v110, v111
Mavz 68,228,20
T 210,192,14336
v_max3_f32 v219, v219, v118, v119
Mavz 72,232,4
T 212,193,12288
v_max3_f32 v217, v217, v124, v125
Mavz 76,232,12
T 214,193,14336
v_max3_f32 v218, v218, v132, v133
)"
R"(Mavz 80,232,20
T 216,192,12800
v_max3_f32 v219, v219, v140, v141
Mavz 84,236,4
T 218,192,14848
v_max3_f32 v217, v217, v126, v127
Mavz 88,236,12
T 220,193,12800
v_max3_f32 v218, v218, v134, v135
Mavz 92,236,20
T 222,193,14848
v_max3_f32 v219, v219, v142, v143
Mav 0,240,52
T 224,192,16384
v_max3_f32 v217, v217, v148, v149
E 85
)"
R"(s_nop 0
C 62,84,85
E 86
E 87
s_nop 0
C 63,86,87
s_nop 1
Mav 4,240,60
T 226,192,18432
v_max3_f32 v218, v218, v156, v157
E 68
E 69
s_nop 0
C 68,68,69
E 70
E 71
)"
R"(s_nop 0
C 69,70,71
E 92
E 93
s_nop 0
C 70,92,93
E 94
E 95
s_nop 0
C 71,94,95
s_nop 1
Mav 8,240,68
W 8
T 228,192,20480
v_max3_f32 v219, v219, v164, v165
Mav 12,244,52
)"
R"(T 230,192,22528
v_max3_f32 v217, v217, v150, v151
Mav 16,244,60
T 232,193,16384
v_max3_f32 v218, v218, v158, v159
Mav 20,244,68
T 234,193,18432
v_max3_f32 v219, v219, v166, v167
Mav 24,248,52
T 236,193,20480
v_max3_f32 v217, v217, v172, v173
Mav 28,248,60
T 238,193,22528
v_max3_f32 v218, v218, v180, v181
Mav 32,248,68
T 240,192,16896
)"
R"(v_max3_f32 v219, v219, v188, v189
Mav 36,252,52
T 242,192,18944
v_max3_f32 v217, v217, v174, v175
Mav 40,252,60
T 244,192,20992
v_max3_f32 v218, v218, v182, v183
Mav 44,252,68
T 246,192,23040
v_max3_f32 v219, v219, v190, v191
Mav 48,208,52
T 248,193,16896
v_cmp_gt_f32 vcc, v217, v211
Mav 52,208,60
T 250,193,18944
v_cmp_gt_f32 s[68:69], v218, v212
)"
R"(Mav 56,208,68
W 8
T 252,193,20992
v_cmp_gt_f32 s[70:71], v219, v213
Mav 60,212,52
T 254,193,23040
s_or_b64 vcc, vcc, s[68:69]
Mav 64,212,60
T 208,192,24576
s_or_b64 vcc, vcc, s[70:71]
Mav 68,212,68
s_cbranch_vccnz .LrareA3_1
.LbackA3_1:
T 210,192,26624
F 100,214
Mav 72,216,52
)"
R"(T 212,192,28672
F 101,214
Mav 76,216,60
T 214,192,30720
F 102,214
Mav 80,216,68
T 216,193,24576
F 103,214
Mav 84,220,52
T 218,193,26624
F 124,214
Mav 88,220,60
E 100
Mav 92,220,68
T 220,193,28672
F 125,214
)"
R"(Mavz 96,224,4
T 222,193,30720
F 126,214
Mav 96,228,52
E 101
Mavz 100,224,12
E 102
Mav 100,228,60
E 103
Mavz 104,224,20
T 224,192,25088
F 127,214
Mav 104,228,68
T 226,192,27136
C 100,100,101
Mavz 108,232,4
)"
R"(W 4
T 228,192,29184
C 101,102,103
Mav 108,236,52
T 230,192,31232
F 108,215
Mavz 112,232,12
E 124
Mav 112,236,60
E 125
Mavz 116,232,20
T 232,193,25088
C 102,124,125
Mav 116,236,68
T 234,193,27136
F 109,215
)"
R"(Mavz 192,204,4
T 236,193,29184
F 110,215
Mav 192,204,52
T 238,193,31232
F 111,215
Mavz 196,204,12
E 126
Mav 196,204,60
E 127
Mavz 200,204,20
E 108
Mav 200,204,68
E 109
Mavz 120,240,4
E 110
)"
R"(Mav 120,244,52
E 111
Mavz 124,240,12
C 103,126,127
F 132,215
Mav 124,244,60
E 132
Mavz 128,240,20
F 133,215
F 134,215
Mav 128,244,68
E 133
Mavz 132,248,4
E 134
Mav 132,252,52
F 135,215
)"
R"(C 108,108,109
Mavz 136,248,12
E 135
Mav 136,252,60
C 109,110,111
F 116,216
Mavz 140,248,20
E 116
Mav 140,252,68
C 110,132,133
C 111,134,135
Mavz 144,208,4
F 117,216
F 118,216
Mav 144,212,52
E 117
)"
R"(Mavz 148,208,12
E 118
Mav 148,212,60
F 119,216
F 140,216
Mavz 152,208,20
E 119
Mav 152,212,68
E 140
Mavz 156,216,4
W 0
F 141,216
F 142,216
Mav 156,220,52
E 141
Mavz 160,216,12
)"
R"(E 142
Mav 160,220,60
F 143,216
C 116,116,117
Mavz 164,216,20
E 143
Mav 164,220,68
W 0
s_barrier
v_and_b32 v207, 63, v255
v_lshrrev_b32 v206, 4, v207
v_lshlrev_b32 v206, 20, v206
v_lshl_add_u32 v206, v207, 2, v206
v_add_u32 v206, s94, v206
v_lshlrev_b32 v207, 2, v207
N 240,200,0
)"
R"(Mavz 168,224,4
N 244,201,0
C 117,118,119
Mav 168,228,52
N 248,200,2048
C 118,140,141
Mavz 172,224,12
N 252,201,2048
C 119,142,143
Mav 172,228,60
N 208,200,4096
F 148,214
Mavz 176,224,20
N 212,201,4096
F 149,214
Mav 176,228,68
)"
R"(N 216,200,6144
F 150,214
Mavz 180,232,4
N 220,201,6144
F 151,214
Mav 180,236,52
N 224,200,8192
F 172,214
Mavz 184,232,12
N 228,201,8192
F 173,214
Mav 184,236,60
E 148
Mavz 188,232,20
N 232,200,10240
F 174,214
)"
R"(Mav 188,236,68
s_cbranch_vccnz .LrareB3_1
.LbackB3_1:
W 7
N 236,201,10240
F 175,214
Mvvz 4,240,0
E 149
Mvv 4,244,24
E 150
Mvv 4,248,48
E 151
Mvv 4,252,72
E 172
Mvvz 12,240,8
E 173
)"
R"(Mvv 12,244,32
E 174
Mvv 12,248,56
E 175
Mvv 12,252,80
C 148,148,149
F 156,215
Mvvz 20,240,16
N 240,200,12288
C 149,150,151
Mvv 20,244,40
N 244,201,12288
C 150,172,173
Mvv 20,248,64
N 248,200,14336
C 151,174,175
)"
R"(Mvv 20,252,88
W 3
N 252,201,14336
F 157,215
Mvv 4,208,96
E 156
Mvv 4,212,120
E 157
Mvv 4,216,144
F 158,215
F 159,215
Mvv 4,220,168
E 158
Mvv 12,208,104
E 159
Mvv 12,212,128
)"
R"(F 180,215
F 181,215
Mvv 12,216,152
E 180
Mvv 12,220,176
E 181
Mvv 20,208,112
N 208,200,16384
F 182,215
Mvv 20,212,136
N 212,201,16384
F 183,215
Mvv 20,216,160
N 216,200,18432
C 156,156,157
Mvv 20,220,184
)"
R"(N 220,201,18432
C 157,158,159
Mvvz 28,224,0
E 182
Mvv 28,228,24
E 183
Mvv 28,232,48
C 158,180,181
C 159,182,183
Mvv 28,236,72
F 164,216
F 165,216
Mvvz 36,224,8
E 164
Mvv 36,228,32
E 165
)"
R"(Mvv 36,232,56
F 166,216
F 167,216
Mvv 36,236,80
E 166
Mvvz 44,224,16
N 224,200,20480
F 188,216
Mvv 44,228,40
N 228,201,20480
F 189,216
Mvv 44,232,64
N 232,200,22528
F 190,216
Mvv 44,236,88
W 3
)"
R"(N 236,201,22528
F 191,216
Mvv 28,240,96
E 167
Mvv 28,244,120
E 188
Mvv 28,248,144
E 189
Mvv 28,252,168
E 190
Mvv 36,240,104
E 191
Mvv 36,244,128
C 164,164,165
v_max_f32 v217, v4, v5
Mvv 36,248,152
)"
R"(C 165,166,167
v_max_f32 v218, v12, v13
Mvv 36,252,176
C 166,188,189
C 167,190,191
Mvv 44,240,112
N 240,200,24576
v_max_f32 v219, v20, v21
Mvv 44,244,136
N 244,201,24576
v_max3_f32 v217, v217, v6, v7
Mvv 44,248,160
N 248,200,26624
v_max3_f32 v218, v218, v14, v15
Mvv 44,252,184
N 252,201,26624
)"
R"(v_max3_f32 v219, v219, v22, v23
Mvvz 52,208,0
v_max3_f32 v217, v217, v28, v29
v_max3_f32 v218, v218, v36, v37
Mvv 52,212,24
v_max3_f32 v217, v217, v30, v31
v_max3_f32 v218, v218, v38, v39
Mvv 52,216,48
Mvv 52,220,72
v_max3_f32 v219, v219, v44, v45
Mvvz 60,208,8
v_max3_f32 v219, v219, v46, v47
Mvv 60,212,32
Mvv 60,216,56
Mvv 60,220,80
Mvvz 68,208,16
)"
R"(N 208,200,28672
Mvv 68,212,40
N 212,201,28672
Mvv 68,216,64
N 216,200,30720
Mvv 68,220,88
W 3
N 220,201,30720
Mvv 52,224,96
Mvv 52,228,120
Mvv 52,232,144
Mvv 52,236,168
Mvv 60,224,104
Mvv 60,228,128
Mvv 60,232,152
Mvv 60,236,176
)"
R"(s_nop 3
v_max3_f32 v217, v217, v52, v53
Mvv 68,224,112
v_max3_f32 v217, v217, v54, v55
Mvv 68,228,136
Mvv 68,232,160
Mvv 68,236,184
v_max3_f32 v218, v218, v60, v61
Mvvz 76,240,0
v_max3_f32 v218, v218, v62, v63
Mvv 76,244,24
Mvv 76,248,48
Mvv 76,252,72
s_nop 1
v_max3_f32 v219, v219, v68, v69
Mvvz 84,240,8
)"
R"(v_max3_f32 v219, v219, v70, v71
Mvv 84,244,32
Mvv 84,248,56
Mvv 84,252,80
Mvvz 92,240,16
Mvv 92,244,40
Mvv 92,248,64
Mvv 92,252,88
W 0
s_barrier
T 224,192,32768
Mvv 76,208,96
T 226,192,34816
T 228,192,36864
Mvv 76,212,120
T 230,192,38912
)"
R"(T 232,193,32768
Mvv 76,216,144
T 234,193,34816
Mvv 76,220,168
T 236,193,36864
Mvv 84,208,104
T 238,193,38912
Mvv 84,212,128
T 240,192,33280
Mvv 84,216,152
T 242,192,35328
Mvv 84,220,176
T 244,192,37376
v_max3_f32 v217, v217, v76, v77
Mvv 92,208,112
T 246,192,39424
)"
R"(v_max3_f32 v217, v217, v78, v79
Mvv 92,212,136
T 248,193,33280
v_cmp_gt_f32 vcc, v217, v211
Mvv 92,216,160
T 250,193,35328
Mvv 92,220,184
W 8
T 252,193,37376
v_max3_f32 v218, v218, v84, v85
Mav 0,224,100
T 254,193,39424
v_max3_f32 v218, v218, v86, v87
Mav 0,228,148
T 208,192,40960
v_cmp_gt_f32 s[68:69], v218, v212
)"
R"(Mav 4,224,108
T 210,192,43008
s_or_b64 vcc, vcc, s[68:69]
Mav 4,228,156
T 212,192,45056
v_max3_f32 v219, v219, v92, v93
Mav 8,224,116
T 214,192,47104
v_max3_f32 v219, v219, v94, v95
Mav 8,228,164
T 216,193,40960
v_cmp_gt_f32 s[70:71], v219, v213
Mav 12,232,100
W 8
T 218,193,43008
s_or_b64 vcc, vcc, s[70:71]
)"
R"(Mav 12,236,148
s_cbranch_vccnz .LrareA3_2
.LbackA3_2:
T 220,193,45056
F 4,214
Mav 16,232,108
T 222,193,47104
F 5,214
Mav 16,236,156
T 224,192,41472
F 6,214
Mav 20,232,116
T 226,192,43520
F 7,214
Mav 20,236,164
T 228,192,45568
)"
R"(F 28,214
Mav 24,240,100
T 230,192,47616
F 29,214
Mav 24,244,148
T 232,193,41472
F 30,214
Mav 28,240,108
T 234,193,43520
F 31,214
Mav 28,244,156
T 236,193,45568
F 12,215
Mav 32,240,116
T 238,193,47616
F 13,215
)"
R"(Mav 32,244,164
W 8
T 240,192,49152
F 14,215
Mav 36,248,100
T 242,192,51200
F 15,215
Mav 36,252,148
T 244,192,53248
F 36,215
Mav 40,248,108
T 246,192,55296
F 37,215
Mav 40,252,156
E 4
Mav 44,248,116
)"
R"(T 248,193,49152
F 38,215
Mav 44,252,164
T 250,193,51200
F 39,215
Mav 48,208,100
E 5
Mav 48,212,148
T 252,193,53248
C 4,4,5
Mav 52,208,108
E 6
Mav 52,212,156
E 7
Mav 56,208,116
T 254,193,55296
)"
R"(C 5,6,7
Mav 56,212,164
E 28
Mav 60,216,100
T 208,192,49664
F 20,216
Mav 60,220,148
T 210,192,51712
F 21,216
Mav 64,216,108
T 212,192,53760
F 22,216
Mav 64,220,156
T 214,192,55808
F 23,216
Mav 68,216,116
)"
R"(T 216,193,49664
F 44,216
Mav 68,220,164
W 6
T 218,193,51712
F 45,216
Mav 72,224,100
T 220,193,53760
F 46,216
Mav 72,228,148
T 222,193,55808
F 47,216
Mav 76,224,108
E 29
Mav 76,228,156
E 30
)"
R"(Mav 80,224,116
T 224,192,57344
C 6,28,29
Mav 80,228,164
E 31
Mav 84,232,100
T 226,192,59392
C 7,30,31
Mav 84,236,148
E 12
Mav 88,232,108
E 13
Mav 88,236,156
T 228,192,61440
C 12,12,13
Mav 92,232,116
)"
R"(E 14
Mav 92,236,164
E 15
Mav 96,240,100
T 230,192,63488
C 13,14,15
Mav 96,244,148
E 36
Mav 100,240,108
T 232,193,57344
F 52,214
Mav 100,244,156
T 234,193,59392
F 53,214
Mav 104,240,116
T 236,193,61440
)"
R"(F 54,214
Mav 104,244,164
T 238,193,63488
F 55,214
Mav 108,248,100
W 5
T 240,192,57856
F 76,214
Mav 108,252,148
T 242,192,59904
F 77,214
Mav 112,248,108
T 244,192,61952
F 78,214
Mav 112,252,156
T 246,192,64000
)"
R"(F 79,214
Mav 116,248,116
E 37
Mav 116,252,164
T 248,193,57856
C 14,36,37
Mav 192,204,100
E 38
Mav 192,204,148
E 39
Mav 196,204,108
T 250,193,59904
C 15,38,39
Mav 196,204,156
E 20
Mav 200,204,116
)"
R"(E 21
Mav 200,204,164
T 252,193,61952
C 20,20,21
Mav 120,208,100
E 22
Mav 120,212,148
E 23
Mav 124,208,108
T 254,193,64000
C 21,22,23
Mav 124,212,156
E 44
Mav 128,208,116
E 45
Mav 128,212,164
)"
R"(E 46
Mav 132,216,100
E 47
Mav 132,220,148
E 52
Mav 136,216,108
E 53
Mav 136,220,156
E 54
Mav 140,216,116
E 55
Mav 140,220,164
E 76
Mav 144,224,100
W 0
E 77
)"
R"(Mav 144,228,148
E 78
Mav 148,224,108
E 79
Mav 148,228,156
C 22,44,45
C 23,46,47
Mav 152,224,116
C 52,52,53
F 60,215
Mav 152,228,164
E 60
Mav 156,232,100
C 53,54,55
F 61,215
Mav 156,236,148
)"
R"(E 61
Mav 160,232,108
C 54,76,77
C 55,78,79
Mav 160,236,156
F 62,215
F 63,215
Mav 164,232,116
E 62
Mav 164,236,164
W 0
s_barrier
N 208,200,32768
Mav 168,240,100
s_mul_i32 s80, s77, 2293760
N 212,201,32768
)"
R"(F 84,215
Mav 168,244,148
s_add_u32 s8, s4, s80
s_addc_u32 s9, s5, 0
N 216,200,34816
F 85,215
Mav 172,240,108
s_and_b32 s9, s9, 0xffff
N 220,201,34816
F 86,215
Mav 172,244,156
s_mov_b32 s10, 2293760
N 224,200,36864
F 87,215
Mav 176,240,116
s_mov_b32 s11, 0x20000
)"
R"(N 228,201,36864
C 60,60,61
Mav 176,244,164
s_mul_i32 s80, s77, 143360
N 232,200,38912
F 68,216
Mav 180,248,100
s_add_u32 s12, s6, s80
s_addc_u32 s13, s7, 0
N 236,201,38912
F 69,216
Mav 180,252,148
s_and_b32 s13, s13, 0xffff
N 240,200,40960
F 70,216
Mav 184,248,108
)"
R"(s_mov_b32 s14, 143360
N 244,201,40960
F 71,216
Mav 184,252,156
s_mov_b32 s15, 0x20000
E 63
Mav 188,248,116
s_lshl2_add_u32 s34, s78, s76
N 248,200,43008
C 61,62,63
Mav 188,252,164
s_add_u32 s37, s34, 76
s_cbranch_vccnz .LrareB3_2
.LbackB3_2:
W 7
N 252,201,43008
)"
R"(F 92,216
Mvvz 100,208,0
s_add_u32 s40, s78, 64
E 84
Mvv 100,212,24
s_mul_i32 s80, s34, 6554
E 85
Mvv 100,216,48
s_lshr_b32 s80, s80, 16
E 86
Mvv 100,220,72
s_mul_i32 s81, s80, 10
E 87
Mvvz 108,208,8
s_sub_u32 s81, s34, s81
E 68
)"
R"(Mvv 108,212,32
s_and_b32 s82, s80, 1
E 69
Mvv 108,216,56
s_lshr_b32 s80, s80, 1
E 70
Mvv 108,220,80
s_mul_i32 s34, s80, 20
E 71
Mvvz 116,208,16
s_add_u32 s34, s34, s82
N 208,200,45056
C 62,84,85
Mvv 116,212,40
s_lshl1_add_u32 s34, s81, s34
N 212,201,45056
)"
R"(C 63,86,87
Mvv 116,216,64
s_lshl_b32 s34, s34, 10
N 216,200,47104
F 93,216
Mvv 116,220,88
W 3
s_add_u32 s80, s80, s79
N 220,201,47104
F 94,216
Mvv 100,224,96
s_cmp_ge_u32 s80, 7
s_cselect_b32 s35, 7, 0
E 92
Mvv 100,228,120
s_sub_u32 s80, s80, s35
)"
R"(E 93
Mvv 100,232,144
s_lshl1_add_u32 s80, s80, s82
E 94
Mvv 100,236,168
s_lshl_b32 s35, s80, 6
F 95,216
C 68,68,69
Mvv 108,224,104
s_mul_i32 s80, s81, 14336
E 95
Mvv 108,228,128
s_add_u32 s35, s35, s80
C 69,70,71
Mvv 108,232,152
s_lshl_b32 s36, s81, 6
)"
R"(C 70,92,93
C 71,94,95
Mvv 108,236,176
s_mul_i32 s80, s37, 6554
v_max_f32 v217, v100, v101
Mvv 116,224,112
s_lshr_b32 s80, s80, 16
N 224,200,49152
v_max3_f32 v217, v217, v102, v103
Mvv 116,228,136
s_mul_i32 s81, s80, 10
N 228,201,49152
Mvv 116,232,160
s_sub_u32 s81, s37, s81
N 232,200,51200
Mvv 116,236,184
)"
R"(s_and_b32 s82, s80, 1
N 236,201,51200
v_max_f32 v218, v108, v109
Mvvz 124,240,0
s_lshr_b32 s80, s80, 1
v_max3_f32 v218, v218, v110, v111
Mvv 124,244,24
s_mul_i32 s37, s80, 20
Mvv 124,248,48
s_add_u32 s37, s37, s82
Mvv 124,252,72
s_lshl1_add_u32 s37, s81, s37
v_max_f32 v219, v116, v117
Mvvz 132,240,8
s_lshl_b32 s37, s37, 10
v_max3_f32 v219, v219, v118, v119
)"
R"(Mvv 132,244,32
s_add_u32 s80, s80, s79
Mvv 132,248,56
s_cmp_ge_u32 s80, 7
s_cselect_b32 s38, 7, 0
Mvv 132,252,80
s_sub_u32 s80, s80, s38
Mvvz 140,240,16
s_lshl1_add_u32 s80, s80, s82
N 240,200,53248
Mvv 140,244,40
s_lshl_b32 s38, s80, 6
N 244,201,53248
Mvv 140,248,64
s_mul_i32 s80, s81, 14336
N 248,200,55296
)"
R"(Mvv 140,252,88
W 3
s_add_u32 s38, s38, s80
N 252,201,55296
Mvv 124,208,96
s_lshl_b32 s39, s81, 6
Mvv 124,212,120
s_mul_i32 s80, s40, 6554
Mvv 124,216,144
s_lshr_b32 s80, s80, 16
Mvv 124,220,168
s_mul_i32 s81, s80, 10
Mvv 132,208,104
s_sub_u32 s81, s40, s81
Mvv 132,212,128
s_and_b32 s82, s80, 1
)"
R"(Mvv 132,216,152
s_lshr_b32 s80, s80, 1
Mvv 132,220,176
s_mul_i32 s40, s80, 20
v_max3_f32 v217, v217, v124, v125
Mvv 140,208,112
s_add_u32 s40, s40, s82
N 208,200,57344
v_max3_f32 v217, v217, v126, v127
Mvv 140,212,136
s_lshl1_add_u32 s40, s81, s40
N 212,201,57344
Mvv 140,216,160
s_lshl_b32 s40, s40, 10
N 216,200,59392
Mvv 140,220,184
)"
R"(s_add_u32 s80, s80, s79
N 220,201,59392
v_max3_f32 v218, v218, v132, v133
Mvvz 148,224,0
s_cmp_ge_u32 s80, 7
s_cselect_b32 s41, 7, 0
v_max3_f32 v218, v218, v134, v135
Mvv 148,228,24
s_sub_u32 s80, s80, s41
Mvv 148,232,48
s_lshl1_add_u32 s80, s80, s82
Mvv 148,236,72
s_lshl_b32 s41, s80, 6
v_max3_f32 v219, v219, v140, v141
Mvvz 156,224,8
s_mul_i32 s80, s81, 14336
)"
R"(v_max3_f32 v219, v219, v142, v143
Mvv 156,228,32
s_add_u32 s41, s41, s80
Mvv 156,232,56
s_lshl_b32 s42, s81, 6
Mvv 156,236,80
Mvvz 164,224,16
N 224,200,61440
Mvv 164,228,40
N 228,201,61440
Mvv 164,232,64
N 232,200,63488
Mvv 164,236,88
W 3
N 236,201,63488
Mvv 148,240,96
)"
R"(Mvv 148,244,120
Mvv 148,248,144
Mvv 148,252,168
Mvv 156,240,104
Mvv 156,244,128
Mvv 156,248,152
Mvv 156,252,176
s_nop 3
v_max3_f32 v217, v217, v148, v149
Mvv 164,240,112
v_max3_f32 v217, v217, v150, v151
Mvv 164,244,136
Mvv 164,248,160
Mvv 164,252,184
v_max3_f32 v218, v218, v156, v157
Mvvz 172,208,0
)"
R"(v_max3_f32 v218, v218, v158, v159
Mvv 172,212,24
Mvv 172,216,48
Mvv 172,220,72
s_nop 1
v_max3_f32 v219, v219, v164, v165
Mvvz 180,208,8
v_max3_f32 v219, v219, v166, v167
Mvv 180,212,32
Mvv 180,216,56
Mvv 180,220,80
Mvvz 188,208,16
Mvv 188,212,40
Mvv 188,216,64
Mvv 188,220,88
W 0
)"
R"(s_barrier
T 240,196,0
Mvv 172,224,96
T 242,196,2048
T 244,196,4096
Mvv 172,228,120
T 246,196,6144
T 248,197,0
Mvv 172,232,144
T 250,197,2048
Mvv 172,236,168
T 252,197,4096
Mvv 180,224,104
T 254,197,6144
Mvv 180,228,128
T 208,196,512
)"
R"(Mvv 180,232,152
T 210,196,2560
Mvv 180,236,176
T 212,196,4608
v_max3_f32 v217, v217, v172, v173
Mvv 188,224,112
T 214,196,6656
v_max3_f32 v217, v217, v174, v175
Mvv 188,228,136
T 216,197,512
v_cmp_gt_f32 vcc, v217, v211
Mvv 188,232,160
T 218,197,2560
Mvv 188,236,184
W 8
T 220,197,4608
)"
R"(v_max3_f32 v218, v218, v180, v181
Mav 0,240,4
T 222,197,6656
v_max3_f32 v218, v218, v182, v183
Mav 0,244,52
T 224,196,8192
v_cmp_gt_f32 s[68:69], v218, v212
Mav 4,240,12
T 226,196,10240
s_or_b64 vcc, vcc, s[68:69]
Mav 4,244,60
T 228,196,12288
v_max3_f32 v219, v219, v188, v189
Mav 8,240,20
T 230,196,14336
v_max3_f32 v219, v219, v190, v191
)"
R"(Mav 8,244,68
T 232,197,8192
v_cmp_gt_f32 s[70:71], v219, v213
Mav 12,248,4
W 8
T 234,197,10240
s_or_b64 vcc, vcc, s[70:71]
Mav 12,252,52
s_cbranch_vccnz .LrareA3_3
.LbackA3_3:
T 236,197,12288
F 100,214
Mav 16,248,12
T 238,197,14336
F 101,214
Mav 16,252,60
)"
R"(T 240,196,8704
F 102,214
Mav 20,248,20
T 242,196,10752
F 103,214
Mav 20,252,68
T 244,196,12800
F 124,214
Mav 24,208,4
T 246,196,14848
F 125,214
Mav 24,212,52
T 248,197,8704
F 126,214
Mav 28,208,12
T 250,197,10752
)"
R"(F 127,214
Mav 28,212,60
T 252,197,12800
F 108,215
Mav 32,208,20
T 254,197,14848
F 109,215
Mav 32,212,68
W 8
T 208,196,16384
F 110,215
Mav 36,216,4
T 210,196,18432
F 111,215
Mav 36,220,52
T 212,196,20480
)"
R"(F 132,215
Mav 40,216,12
T 214,196,22528
F 133,215
Mav 40,220,60
E 100
Mav 44,216,20
T 216,197,16384
F 134,215
Mav 44,220,68
T 218,197,18432
F 135,215
Mav 48,224,4
E 101
Mav 48,228,52
T 220,197,20480
)"
R"(C 100,100,101
Mav 52,224,12
E 102
Mav 52,228,60
E 103
Mav 56,224,20
T 222,197,22528
C 101,102,103
Mav 56,228,68
E 124
Mav 60,232,4
T 224,196,16896
F 116,216
Mav 60,236,52
T 226,196,18944
F 117,216
)"
R"(Mav 64,232,12
T 228,196,20992
F 118,216
Mav 64,236,60
T 230,196,23040
F 119,216
Mav 68,232,20
T 232,197,16896
F 140,216
Mav 68,236,68
W 6
T 234,197,18944
F 141,216
Mav 72,240,4
T 236,197,20992
F 142,216
)"
R"(Mav 72,244,52
T 238,197,23040
F 143,216
Mav 76,240,12
E 125
Mav 76,244,60
E 126
Mav 80,240,20
T 240,196,24576
C 102,124,125
Mav 80,244,68
E 127
Mav 84,248,4
T 242,196,26624
C 103,126,127
Mav 84,252,52
)"
R"(E 108
Mav 88,248,12
E 109
Mav 88,252,60
T 244,196,28672
C 108,108,109
Mav 92,248,20
E 110
Mav 92,252,68
E 111
Mav 96,208,4
T 246,196,30720
C 109,110,111
Mav 96,212,52
E 132
Mav 100,208,12
)"
R"(T 248,197,24576
F 148,214
Mav 100,212,60
T 250,197,26624
F 149,214
Mav 104,208,20
T 252,197,28672
F 150,214
Mav 104,212,68
T 254,197,30720
F 151,214
Mav 108,216,4
W 5
T 208,196,25088
F 172,214
Mav 108,220,52
)"
R"(T 210,196,27136
F 173,214
Mav 112,216,12
T 212,196,29184
F 174,214
Mav 112,220,60
T 214,196,31232
F 175,214
Mav 116,216,20
E 133
Mav 116,220,68
T 216,197,25088
C 110,132,133
Mav 192,204,4
E 134
Mav 192,204,52
)"
R"(E 135
Mav 196,204,12
T 218,197,27136
C 111,134,135
Mav 196,204,60
E 116
Mav 200,204,20
E 117
Mav 200,204,68
T 220,197,29184
C 116,116,117
Mav 120,224,4
E 118
Mav 120,228,52
E 119
Mav 124,224,12
)"
R"(T 222,197,31232
C 117,118,119
Mav 124,228,60
E 140
Mav 128,224,20
E 141
Mav 128,228,68
E 142
Mav 132,232,4
E 143
Mav 132,236,52
E 148
Mav 136,232,12
E 149
Mav 136,236,60
E 150
)"
R"(Mav 140,232,20
E 151
Mav 140,236,68
E 172
Mav 144,240,4
W 0
E 173
Mav 144,244,52
E 174
Mav 148,240,12
E 175
Mav 148,244,60
C 118,140,141
C 119,142,143
Mav 152,240,20
C 148,148,149
)"
R"(F 156,215
Mav 152,244,68
E 156
Mav 156,248,4
C 149,150,151
F 157,215
Mav 156,252,52
E 157
Mav 160,248,12
C 150,172,173
C 151,174,175
Mav 160,252,60
F 158,215
F 159,215
Mav 164,248,20
E 158
)"
R"(Mav 164,252,68
W 0
s_barrier
N 224,198,0
Mav 168,208,4
N 228,199,0
F 180,215
Mav 168,212,52
N 232,198,2048
F 181,215
Mav 172,208,12
N 236,199,2048
F 182,215
Mav 172,212,60
N 240,198,4096
F 183,215
)"
R"(Mav 176,208,20
N 244,199,4096
C 156,156,157
Mav 176,212,68
N 248,198,6144
F 164,216
Mav 180,216,4
N 252,199,6144
F 165,216
Mav 180,220,52
N 208,198,8192
F 166,216
Mav 184,216,12
N 212,199,8192
F 167,216
Mav 184,220,60
)"
R"(E 159
Mav 188,216,20
N 216,198,10240
C 157,158,159
Mav 188,220,68
s_cbranch_vccnz .LrareB3_3
.LbackB3_3:
W 7
N 220,199,10240
F 188,216
Mvvz 4,224,0
E 180
Mvv 4,228,24
E 181
Mvv 4,232,48
E 182
)"
R"(Mvv 4,236,72
E 183
Mvvz 12,224,8
E 164
Mvv 12,228,32
E 165
Mvv 12,232,56
E 166
Mvv 12,236,80
E 167
Mvvz 20,224,16
N 224,198,12288
C 158,180,181
Mvv 20,228,40
N 228,199,12288
C 159,182,183
)"
R"(Mvv 20,232,64
N 232,198,14336
F 189,216
Mvv 20,236,88
W 3
N 236,199,14336
F 190,216
Mvv 4,240,96
E 188
Mvv 4,244,120
E 189
Mvv 4,248,144
E 190
Mvv 4,252,168
F 191,216
C 164,164,165
)"
R"(Mvv 12,240,104
E 191
Mvv 12,244,128
C 165,166,167
Mvv 12,248,152
C 166,188,189
C 167,190,191
Mvv 12,252,176
v_max_f32 v217, v4, v5
Mvv 20,240,112
N 240,198,16384
v_max3_f32 v217, v217, v6, v7
Mvv 20,244,136
N 244,199,16384
Mvv 20,248,160
N 248,198,18432
)"
R"(Mvv 20,252,184
N 252,199,18432
v_max_f32 v218, v12, v13
Mvvz 28,208,0
v_max3_f32 v218, v218, v14, v15
Mvv 28,212,24
Mvv 28,216,48
Mvv 28,220,72
s_nop 0
v_max_f32 v219, v20, v21
Mvvz 36,208,8
v_max3_f32 v219, v219, v22, v23
Mvv 36,212,32
Mvv 36,216,56
Mvv 36,220,80
Mvvz 44,208,16
)"
R"(N 208,198,20480
Mvv 44,212,40
N 212,199,20480
Mvv 44,216,64
N 216,198,22528
Mvv 44,220,88
W 3
N 220,199,22528
Mvv 28,224,96
Mvv 28,228,120
Mvv 28,232,144
Mvv 28,236,168
Mvv 36,224,104
Mvv 36,228,128
Mvv 36,232,152
Mvv 36,236,176
)"
R"(s_nop 3
v_max3_f32 v217, v217, v28, v29
Mvv 44,224,112
N 224,198,24576
v_max3_f32 v217, v217, v30, v31
Mvv 44,228,136
N 228,199,24576
Mvv 44,232,160
N 232,198,26624
Mvv 44,236,184
N 236,199,26624
v_max3_f32 v218, v218, v36, v37
Mvvz 52,240,0
v_max3_f32 v218, v218, v38, v39
Mvv 52,244,24
Mvv 52,248,48
)"
R"(Mvv 52,252,72
s_nop 0
v_max3_f32 v219, v219, v44, v45
Mvvz 60,240,8
v_max3_f32 v219, v219, v46, v47
Mvv 60,244,32
Mvv 60,248,56
Mvv 60,252,80
Mvvz 68,240,16
N 240,198,28672
Mvv 68,244,40
N 244,199,28672
Mvv 68,248,64
N 248,198,30720
Mvv 68,252,88
W 3
)"
R"(N 252,199,30720
Mvv 52,208,96
Mvv 52,212,120
Mvv 52,216,144
Mvv 52,220,168
Mvv 60,208,104
Mvv 60,212,128
Mvv 60,216,152
Mvv 60,220,176
s_nop 3
v_max3_f32 v217, v217, v52, v53
Mvv 68,208,112
v_max3_f32 v217, v217, v54, v55
Mvv 68,212,136
Mvv 68,216,160
Mvv 68,220,184
)"
R"(v_max3_f32 v218, v218, v60, v61
Mvvz 76,224,0
v_max3_f32 v218, v218, v62, v63
Mvv 76,228,24
Mvv 76,232,48
Mvv 76,236,72
s_nop 1
v_max3_f32 v219, v219, v68, v69
Mvvz 84,224,8
v_max3_f32 v219, v219, v70, v71
Mvv 84,228,32
Mvv 84,232,56
Mvv 84,236,80
Mvvz 92,224,16
Mvv 92,228,40
Mvv 92,232,64
)"
R"(Mvv 92,236,88
W 0
s_barrier
T 208,194,0
Mvv 76,240,96
T 210,194,2048
T 212,194,4096
Mvv 76,244,120
T 214,194,6144
T 216,195,0
Mvv 76,248,144
T 218,195,2048
Mvv 76,252,168
T 220,195,4096
Mvv 84,240,104
T 222,195,6144
)"
R"(Mvv 84,244,128
T 224,194,512
Mvv 84,248,152
T 226,194,2560
Mvv 84,252,176
T 228,194,4608
v_max3_f32 v217, v217, v76, v77
Mvv 92,240,112
T 230,194,6656
v_max3_f32 v217, v217, v78, v79
Mvv 92,244,136
T 232,195,512
v_cmp_gt_f32 vcc, v217, v211
Mvv 92,248,160
T 234,195,2560
Mvv 92,252,184
)"
R"(W 8
T 236,195,4608
v_max3_f32 v218, v218, v84, v85
Mav 0,208,100
T 238,195,6656
v_max3_f32 v218, v218, v86, v87
Mav 0,212,148
T 240,194,8192
v_cmp_gt_f32 s[68:69], v218, v212
Mav 4,208,108
T 242,194,10240
s_or_b64 vcc, vcc, s[68:69]
Mav 4,212,156
T 244,194,12288
v_max3_f32 v219, v219, v92, v93
Mav 8,208,116
)"
R"(T 246,194,14336
v_max3_f32 v219, v219, v94, v95
Mav 8,212,164
T 248,195,8192
v_cmp_gt_f32 s[70:71], v219, v213
Mav 12,216,100
W 8
T 250,195,10240
s_or_b64 vcc, vcc, s[70:71]
Mav 12,220,148
s_cbranch_vccnz .LrareA3_4
.LbackA3_4:
T 252,195,12288
F 4,214
Mav 16,216,108
T 254,195,14336
)"
R"(F 5,214
Mav 16,220,156
T 208,194,8704
F 6,214
Mav 20,216,116
T 210,194,10752
F 7,214
Mav 20,220,164
T 212,194,12800
F 28,214
Mav 24,224,100
T 214,194,14848
F 29,214
Mav 24,228,148
T 216,195,8704
F 30,214
)"
R"(Mav 28,224,108
T 218,195,10752
F 31,214
Mav 28,228,156
T 220,195,12800
F 12,215
Mav 32,224,116
T 222,195,14848
F 13,215
Mav 32,228,164
W 8
T 224,194,16384
F 14,215
Mav 36,232,100
T 226,194,18432
F 15,215
)"
R"(Mav 36,236,148
T 228,194,20480
F 36,215
Mav 40,232,108
T 230,194,22528
F 37,215
Mav 40,236,156
E 4
Mav 44,232,116
T 232,195,16384
F 38,215
Mav 44,236,164
T 234,195,18432
F 39,215
Mav 48,240,100
E 5
)"
R"(Mav 48,244,148
T 236,195,20480
C 4,4,5
Mav 52,240,108
E 6
Mav 52,244,156
E 7
Mav 56,240,116
T 238,195,22528
C 5,6,7
Mav 56,244,164
E 28
Mav 60,248,100
T 240,194,16896
F 20,216
Mav 60,252,148
)"
R"(T 242,194,18944
F 21,216
Mav 64,248,108
T 244,194,20992
F 22,216
Mav 64,252,156
T 246,194,23040
F 23,216
Mav 68,248,116
T 248,195,16896
F 44,216
Mav 68,252,164
W 6
T 250,195,18944
F 45,216
Mav 72,208,100
)"
R"(T 252,195,20992
F 46,216
Mav 72,212,148
T 254,195,23040
F 47,216
Mav 76,208,108
E 29
Mav 76,212,156
E 30
Mav 80,208,116
T 208,194,24576
C 6,28,29
Mav 80,212,164
E 31
Mav 84,216,100
T 210,194,26624
)"
R"(C 7,30,31
Mav 84,220,148
E 12
Mav 88,216,108
E 13
Mav 88,220,156
T 212,194,28672
C 12,12,13
Mav 92,216,116
E 14
Mav 92,220,164
E 15
Mav 96,224,100
T 214,194,30720
C 13,14,15
Mav 96,228,148
)"
R"(E 36
Mav 100,224,108
T 216,195,24576
F 52,214
Mav 100,228,156
T 218,195,26624
F 53,214
Mav 104,224,116
T 220,195,28672
F 54,214
Mav 104,228,164
T 222,195,30720
F 55,214
Mav 108,232,100
W 5
T 224,194,25088
)"
R"(F 76,214
Mav 108,236,148
T 226,194,27136
F 77,214
Mav 112,232,108
T 228,194,29184
F 78,214
Mav 112,236,156
T 230,194,31232
F 79,214
Mav 116,232,116
E 37
Mav 116,236,164
T 232,195,25088
C 14,36,37
Mav 192,204,100
)"
R"(E 38
Mav 192,204,148
E 39
Mav 196,204,108
T 234,195,27136
C 15,38,39
Mav 196,204,156
E 20
Mav 200,204,116
E 21
Mav 200,204,164
T 236,195,29184
C 20,20,21
Mav 120,240,100
E 22
Mav 120,244,148
)"
R"(E 23
Mav 124,240,108
T 238,195,31232
C 21,22,23
Mav 124,244,156
E 44
Mav 128,240,116
E 45
Mav 128,244,164
E 46
Mav 132,248,100
E 47
Mav 132,252,148
E 52
Mav 136,248,108
E 53
)"
R"(Mav 136,252,156
E 54
Mav 140,248,116
E 55
Mav 140,252,164
E 76
Mav 144,208,100
W 0
E 77
Mav 144,212,148
E 78
Mav 148,208,108
E 79
Mav 148,212,156
C 22,44,45
C 23,46,47
)"
R"(Mav 152,208,116
C 52,52,53
F 60,215
Mav 152,212,164
E 60
Mav 156,216,100
C 53,54,55
F 61,215
Mav 156,220,148
E 61
Mav 160,216,108
C 54,76,77
C 55,78,79
Mav 160,220,156
F 62,215
F 63,215
)"
R"(Mav 164,216,116
E 62
Mav 164,220,164
W 0
s_barrier
N 240,198,32768
Mav 168,224,100
N 244,199,32768
F 84,215
Mav 168,228,148
N 248,198,34816
F 85,215
Mav 172,224,108
N 252,199,34816
F 86,215
Mav 172,228,156
)"
R"(N 208,198,36864
F 87,215
Mav 176,224,116
N 212,199,36864
C 60,60,61
Mav 176,228,164
N 216,198,38912
F 68,216
Mav 180,232,100
N 220,199,38912
F 69,216
Mav 180,236,148
N 224,198,40960
F 70,216
Mav 184,232,108
N 228,199,40960
)"
R"(F 71,216
Mav 184,236,156
E 63
Mav 188,232,116
N 232,198,43008
C 61,62,63
Mav 188,236,164
s_cbranch_vccnz .LrareB3_4
.LbackB3_4:
W 7
N 236,199,43008
F 92,216
Mvvz 100,240,0
E 84
Mvv 100,244,24
E 85
)"
R"(Mvv 100,248,48
E 86
Mvv 100,252,72
E 87
Mvvz 108,240,8
E 68
Mvv 108,244,32
E 69
Mvv 108,248,56
E 70
Mvv 108,252,80
E 71
Mvvz 116,240,16
N 240,198,45056
C 62,84,85
Mvv 116,244,40
)"
R"(N 244,199,45056
C 63,86,87
Mvv 116,248,64
N 248,198,47104
F 93,216
Mvv 116,252,88
W 3
N 252,199,47104
F 94,216
Mvv 100,208,96
E 92
Mvv 100,212,120
E 93
Mvv 100,216,144
E 94
Mvv 100,220,168
)"
R"(F 95,216
C 68,68,69
Mvv 108,208,104
E 95
Mvv 108,212,128
C 69,70,71
Mvv 108,216,152
C 70,92,93
C 71,94,95
Mvv 108,220,176
v_max_f32 v217, v100, v101
Mvv 116,208,112
N 208,198,49152
v_max3_f32 v217, v217, v102, v103
Mvv 116,212,136
N 212,199,49152
)"
R"(Mvv 116,216,160
N 216,198,51200
Mvv 116,220,184
N 220,199,51200
v_max_f32 v218, v108, v109
Mvvz 124,224,0
v_max3_f32 v218, v218, v110, v111
Mvv 124,228,24
Mvv 124,232,48
Mvv 124,236,72
s_nop 0
v_max_f32 v219, v116, v117
Mvvz 132,224,8
v_max3_f32 v219, v219, v118, v119
Mvv 132,228,32
Mvv 132,232,56
)"
R"(Mvv 132,236,80
Mvvz 140,224,16
N 224,198,53248
Mvv 140,228,40
N 228,199,53248
Mvv 140,232,64
N 232,198,55296
Mvv 140,236,88
W 3
N 236,199,55296
Mvv 124,240,96
Mvv 124,244,120
Mvv 124,248,144
Mvv 124,252,168
Mvv 132,240,104
Mvv 132,244,128
)"
R"(Mvv 132,248,152
Mvv 132,252,176
s_nop 3
v_max3_f32 v217, v217, v124, v125
Mvv 140,240,112
N 240,198,57344
v_max3_f32 v217, v217, v126, v127
Mvv 140,244,136
N 244,199,57344
Mvv 140,248,160
N 248,198,59392
Mvv 140,252,184
N 252,199,59392
v_max3_f32 v218, v218, v132, v133
Mvvz 148,208,0
v_max3_f32 v218, v218, v134, v135
)"
R"(Mvv 148,212,24
Mvv 148,216,48
Mvv 148,220,72
s_nop 0
v_max3_f32 v219, v219, v140, v141
Mvvz 156,208,8
v_max3_f32 v219, v219, v142, v143
Mvv 156,212,32
Mvv 156,216,56
Mvv 156,220,80
Mvvz 164,208,16
N 208,198,61440
Mvv 164,212,40
N 212,199,61440
Mvv 164,216,64
N 216,198,63488
)"
R"(Mvv 164,220,88
W 3
N 220,199,63488
Mvv 148,224,96
Mvv 148,228,120
Mvv 148,232,144
Mvv 148,236,168
Mvv 156,224,104
Mvv 156,228,128
Mvv 156,232,152
Mvv 156,236,176
s_nop 3
v_max3_f32 v217, v217, v148, v149
Mvv 164,224,112
v_max3_f32 v217, v217, v150, v151
Mvv 164,228,136
)"
R"(Mvv 164,232,160
Mvv 164,236,184
v_max3_f32 v218, v218, v156, v157
Mvvz 172,240,0
v_max3_f32 v218, v218, v158, v159
Mvv 172,244,24
Mvv 172,248,48
Mvv 172,252,72
s_nop 1
v_max3_f32 v219, v219, v164, v165
Mvvz 180,240,8
v_max3_f32 v219, v219, v166, v167
Mvv 180,244,32
Mvv 180,248,56
Mvv 180,252,80
Mvvz 188,240,16
)"
R"(Mvv 188,244,40
Mvv 188,248,64
Mvv 188,252,88
W 0
s_barrier
T 224,194,32768
Mvv 172,208,96
T 226,194,34816
T 228,194,36864
Mvv 172,212,120
T 230,194,38912
T 232,195,32768
Mvv 172,216,144
T 234,195,34816
Mvv 172,220,168
T 236,195,36864
)"
R"(Mvv 180,208,104
T 238,195,38912
Mvv 180,212,128
T 240,194,33280
Mvv 180,216,152
T 242,194,35328
Mvv 180,220,176
T 244,194,37376
v_max3_f32 v217, v217, v172, v173
Mvv 188,208,112
T 246,194,39424
v_max3_f32 v217, v217, v174, v175
Mvv 188,212,136
T 248,195,33280
v_cmp_gt_f32 vcc, v217, v211
Mvv 188,216,160
)"
R"(T 250,195,35328
Mvv 188,220,184
W 8
T 252,195,37376
v_max3_f32 v218, v218, v180, v181
Mav 0,224,4
T 254,195,39424
v_max3_f32 v218, v218, v182, v183
Mav 0,228,52
T 208,194,40960
v_cmp_gt_f32 s[68:69], v218, v212
Mav 4,224,12
T 210,194,43008
s_or_b64 vcc, vcc, s[68:69]
Mav 4,228,60
T 212,194,45056
)"
R"(v_max3_f32 v219, v219, v188, v189
Mav 8,224,20
T 214,194,47104
v_max3_f32 v219, v219, v190, v191
Mav 8,228,68
T 216,195,40960
v_cmp_gt_f32 s[70:71], v219, v213
Mav 12,232,4
W 8
T 218,195,43008
s_or_b64 vcc, vcc, s[70:71]
Mav 12,236,52
s_cbranch_vccnz .LrareA3_5
.LbackA3_5:
T 220,195,45056
F 100,214
)"
R"(Mav 16,232,12
T 222,195,47104
F 101,214
Mav 16,236,60
T 224,194,41472
F 102,214
Mav 20,232,20
T 226,194,43520
F 103,214
Mav 20,236,68
T 228,194,45568
F 124,214
Mav 24,240,4
T 230,194,47616
F 125,214
Mav 24,244,52
)"
R"(T 232,195,41472
F 126,214
Mav 28,240,12
T 234,195,43520
F 127,214
Mav 28,244,60
T 236,195,45568
F 108,215
Mav 32,240,20
T 238,195,47616
F 109,215
Mav 32,244,68
W 8
T 240,194,49152
F 110,215
Mav 36,248,4
)"
R"(T 242,194,51200
F 111,215
Mav 36,252,52
T 244,194,53248
F 132,215
Mav 40,248,12
T 246,194,55296
F 133,215
Mav 40,252,60
E 100
Mav 44,248,20
T 248,195,49152
F 134,215
Mav 44,252,68
T 250,195,51200
F 135,215
)"
R"(Mav 48,208,4
E 101
Mav 48,212,52
T 252,195,53248
C 100,100,101
Mav 52,208,12
E 102
Mav 52,212,60
E 103
Mav 56,208,20
T 254,195,55296
C 101,102,103
Mav 56,212,68
E 124
Mav 60,216,4
T 208,194,49664
)"
R"(F 116,216
Mav 60,220,52
T 210,194,51712
F 117,216
Mav 64,216,12
T 212,194,53760
F 118,216
Mav 64,220,60
T 214,194,55808
F 119,216
Mav 68,216,20
T 216,195,49664
F 140,216
Mav 68,220,68
W 6
T 218,195,51712
)"
R"(F 141,216
Mav 72,224,4
T 220,195,53760
F 142,216
Mav 72,228,52
T 222,195,55808
F 143,216
Mav 76,224,12
E 125
Mav 76,228,60
E 126
Mav 80,224,20
T 224,194,57344
C 102,124,125
Mav 80,228,68
E 127
)"
R"(Mav 84,232,4
T 226,194,59392
C 103,126,127
Mav 84,236,52
E 108
Mav 88,232,12
E 109
Mav 88,236,60
T 228,194,61440
C 108,108,109
Mav 92,232,20
E 110
Mav 92,236,68
E 111
Mav 96,240,4
T 230,194,63488
)"
R"(C 109,110,111
Mav 96,244,52
E 132
Mav 100,240,12
T 232,195,57344
F 148,214
Mav 100,244,60
T 234,195,59392
F 149,214
Mav 104,240,20
T 236,195,61440
F 150,214
Mav 104,244,68
T 238,195,63488
F 151,214
Mav 108,248,4
)"
R"(W 5
T 240,194,57856
F 172,214
Mav 108,252,52
T 242,194,59904
F 173,214
Mav 112,248,12
T 244,194,61952
F 174,214
Mav 112,252,60
T 246,194,64000
F 175,214
Mav 116,248,20
E 133
Mav 116,252,68
T 248,195,57856
)"
R"(C 110,132,133
Mav 192,204,4
E 134
Mav 192,204,52
E 135
Mav 196,204,12
T 250,195,59904
C 111,134,135
Mav 196,204,60
E 116
Mav 200,204,20
E 117
Mav 200,204,68
T 252,195,61952
C 116,116,117
Mav 120,208,4
)"
R"(E 118
Mav 120,212,52
E 119
Mav 124,208,12
T 254,195,64000
C 117,118,119
Mav 124,212,60
E 140
Mav 128,208,20
E 141
Mav 128,212,68
E 142
Mav 132,216,4
E 143
Mav 132,220,52
E 148
)"
R"(Mav 136,216,12
E 149
Mav 136,220,60
E 150
Mav 140,216,20
E 151
Mav 140,220,68
E 172
Mav 144,224,4
W 0
E 173
Mav 144,228,52
E 174
Mav 148,224,12
E 175
Mav 148,228,60
)"
R"(C 118,140,141
C 119,142,143
Mav 152,224,20
C 148,148,149
F 156,215
Mav 152,228,68
E 156
Mav 156,232,4
C 149,150,151
F 157,215
Mav 156,236,52
E 157
Mav 160,232,12
C 150,172,173
C 151,174,175
Mav 160,236,60
)"
R"(F 158,215
F 159,215
Mav 164,232,20
E 158
Mav 164,236,68
W 0
s_barrier
N 208,202,0
Mav 168,240,4
N 212,203,0
F 180,215
Mav 168,244,52
N 216,202,2048
F 181,215
Mav 172,240,12
N 220,203,2048
)"
R"(F 182,215
Mav 172,244,60
N 224,202,4096
F 183,215
Mav 176,240,20
N 228,203,4096
C 156,156,157
Mav 176,244,68
N 232,202,6144
F 164,216
Mav 180,248,4
N 236,203,6144
F 165,216
Mav 180,252,52
N 240,202,8192
F 166,216
)"
R"(Mav 184,248,12
N 244,203,8192
F 167,216
Mav 184,252,60
E 159
Mav 188,248,20
N 248,202,10240
C 157,158,159
Mav 188,252,68
s_cbranch_vccnz .LrareB3_5
.LbackB3_5:
W 7
N 252,203,10240
F 188,216
Mvvz 4,208,0
E 180
)"
R"(Mvv 4,212,24
E 181
Mvv 4,216,48
E 182
Mvv 4,220,72
E 183
Mvvz 12,208,8
E 164
Mvv 12,212,32
E 165
Mvv 12,216,56
E 166
Mvv 12,220,80
E 167
Mvvz 20,208,16
N 208,202,12288
)"
R"(C 158,180,181
Mvv 20,212,40
N 212,203,12288
C 159,182,183
Mvv 20,216,64
N 216,202,14336
F 189,216
Mvv 20,220,88
W 3
N 220,203,14336
F 190,216
Mvv 4,224,96
E 188
Mvv 4,228,120
E 189
Mvv 4,232,144
)"
R"(E 190
Mvv 4,236,168
F 191,216
C 164,164,165
Mvv 12,224,104
E 191
Mvv 12,228,128
C 165,166,167
Mvv 12,232,152
C 166,188,189
C 167,190,191
Mvv 12,236,176
v_max_f32 v217, v4, v5
Mvv 20,224,112
N 224,202,16384
v_max3_f32 v217, v217, v6, v7
)"
R"(Mvv 20,228,136
N 228,203,16384
Mvv 20,232,160
N 232,202,18432
Mvv 20,236,184
N 236,203,18432
v_max_f32 v218, v12, v13
Mvvz 28,240,0
v_max3_f32 v218, v218, v14, v15
Mvv 28,244,24
Mvv 28,248,48
Mvv 28,252,72
s_nop 0
v_max_f32 v219, v20, v21
Mvvz 36,240,8
v_max3_f32 v219, v219, v22, v23
)"
R"(Mvv 36,244,32
Mvv 36,248,56
Mvv 36,252,80
Mvvz 44,240,16
N 240,202,20480
Mvv 44,244,40
N 244,203,20480
Mvv 44,248,64
N 248,202,22528
Mvv 44,252,88
W 3
N 252,203,22528
Mvv 28,208,96
Mvv 28,212,120
Mvv 28,216,144
Mvv 28,220,168
)"
R"(Mvv 36,208,104
Mvv 36,212,128
Mvv 36,216,152
Mvv 36,220,176
s_nop 3
v_max3_f32 v217, v217, v28, v29
Mvv 44,208,112
N 208,202,24576
v_max3_f32 v217, v217, v30, v31
Mvv 44,212,136
N 212,203,24576
Mvv 44,216,160
N 216,202,26624
Mvv 44,220,184
N 220,203,26624
v_max3_f32 v218, v218, v36, v37
)"
R"(Mvvz 52,224,0
v_max3_f32 v218, v218, v38, v39
Mvv 52,228,24
Mvv 52,232,48
Mvv 52,236,72
s_nop 0
v_max3_f32 v219, v219, v44, v45
Mvvz 60,224,8
v_max3_f32 v219, v219, v46, v47
Mvv 60,228,32
Mvv 60,232,56
Mvv 60,236,80
Mvvz 68,224,16
N 224,202,28672
Mvv 68,228,40
N 228,203,28672
)"
R"(Mvv 68,232,64
N 232,202,30720
Mvv 68,236,88
W 3
N 236,203,30720
Mvv 52,240,96
Mvv 52,244,120
Mvv 52,248,144
Mvv 52,252,168
Mvv 60,240,104
Mvv 60,244,128
Mvv 60,248,152
Mvv 60,252,176
s_nop 3
v_max3_f32 v217, v217, v52, v53
Mvv 68,240,112
)"
R"(v_max3_f32 v217, v217, v54, v55
Mvv 68,244,136
Mvv 68,248,160
Mvv 68,252,184
v_max3_f32 v218, v218, v60, v61
Mvvz 76,208,0
v_max3_f32 v218, v218, v62, v63
Mvv 76,212,24
Mvv 76,216,48
Mvv 76,220,72
s_nop 1
v_max3_f32 v219, v219, v68, v69
Mvvz 84,208,8
v_max3_f32 v219, v219, v70, v71
Mvv 84,212,32
Mvv 84,216,56
)"
R"(Mvv 84,220,80
Mvvz 92,208,16
Mvv 92,212,40
Mvv 92,216,64
Mvv 92,220,88
W 0
s_barrier
T 240,192,0
Mvv 76,224,96
T 242,192,2048
T 244,192,4096
Mvv 76,228,120
T 246,192,6144
T 248,193,0
Mvv 76,232,144
T 250,193,2048
)"
R"(Mvv 76,236,168
T 252,193,4096
Mvv 84,224,104
T 254,193,6144
Mvv 84,228,128
T 208,192,512
Mvv 84,232,152
T 210,192,2560
Mvv 84,236,176
T 212,192,4608
v_max3_f32 v217, v217, v76, v77
Mvv 92,224,112
T 214,192,6656
v_max3_f32 v217, v217, v78, v79
Mvv 92,228,136
T 216,193,512
)"
R"(v_cmp_gt_f32 vcc, v217, v211
Mvv 92,232,160
T 218,193,2560
Mvv 92,236,184
W 8
T 220,193,4608
v_max3_f32 v218, v218, v84, v85
Mav 0,240,100
T 222,193,6656
v_max3_f32 v218, v218, v86, v87
Mav 0,244,148
T 224,192,8192
v_cmp_gt_f32 s[68:69], v218, v212
Mav 4,240,108
T 226,192,10240
s_or_b64 vcc, vcc, s[68:69]
)"
R"(Mav 4,244,156
T 228,192,12288
v_max3_f32 v219, v219, v92, v93
Mav 8,240,116
T 230,192,14336
v_max3_f32 v219, v219, v94, v95
Mav 8,244,164
T 232,193,8192
v_cmp_gt_f32 s[70:71], v219, v213
Mav 12,248,100
W 8
T 234,193,10240
s_or_b64 vcc, vcc, s[70:71]
Mav 12,252,148
s_cbranch_vccnz .LrareA3_6
.LbackA3_6:
)"
R"(T 236,193,12288
F 4,214
Mav 16,248,108
T 238,193,14336
F 5,214
Mav 16,252,156
T 240,192,8704
F 6,214
Mav 20,248,116
T 242,192,10752
F 7,214
Mav 20,252,164
T 244,192,12800
F 28,214
Mav 24,208,100
T 246,192,14848
)"
R"(F 29,214
Mav 24,212,148
T 248,193,8704
F 30,214
Mav 28,208,108
T 250,193,10752
F 31,214
Mav 28,212,156
T 252,193,12800
F 12,215
Mav 32,208,116
T 254,193,14848
F 13,215
Mav 32,212,164
W 8
T 208,192,16384
)"
R"(F 14,215
Mav 36,216,100
T 210,192,18432
F 15,215
Mav 36,220,148
T 212,192,20480
F 36,215
Mav 40,216,108
T 214,192,22528
F 37,215
Mav 40,220,156
E 4
Mav 44,216,116
T 216,193,16384
F 38,215
Mav 44,220,164
)"
R"(T 218,193,18432
F 39,215
Mav 48,224,100
E 5
Mav 48,228,148
T 220,193,20480
C 4,4,5
Mav 52,224,108
E 6
Mav 52,228,156
E 7
Mav 56,224,116
T 222,193,22528
C 5,6,7
Mav 56,228,164
E 28
)"
R"(Mav 60,232,100
T 224,192,16896
F 20,216
Mav 60,236,148
T 226,192,18944
F 21,216
Mav 64,232,108
T 228,192,20992
F 22,216
Mav 64,236,156
T 230,192,23040
F 23,216
Mav 68,232,116
T 232,193,16896
F 44,216
Mav 68,236,164
)"
R"(W 6
T 234,193,18944
F 45,216
Mav 72,240,100
T 236,193,20992
F 46,216
Mav 72,244,148
T 238,193,23040
F 47,216
Mav 76,240,108
E 29
Mav 76,244,156
E 30
Mav 80,240,116
T 240,192,24576
C 6,28,29
)"
R"(Mav 80,244,164
E 31
Mav 84,248,100
T 242,192,26624
C 7,30,31
Mav 84,252,148
E 12
Mav 88,248,108
E 13
Mav 88,252,156
T 244,192,28672
C 12,12,13
Mav 92,248,116
E 14
Mav 92,252,164
E 15
)"
R"(Mav 96,208,100
T 246,192,30720
C 13,14,15
Mav 96,212,148
E 36
Mav 100,208,108
T 248,193,24576
F 52,214
Mav 100,212,156
T 250,193,26624
F 53,214
Mav 104,208,116
T 252,193,28672
F 54,214
Mav 104,212,164
T 254,193,30720
)"
R"(F 55,214
Mav 108,216,100
W 5
T 208,192,25088
F 76,214
Mav 108,220,148
T 210,192,27136
F 77,214
Mav 112,216,108
T 212,192,29184
F 78,214
Mav 112,220,156
T 214,192,31232
F 79,214
Mav 116,216,116
E 37
)"
R"(Mav 116,220,164
T 216,193,25088
C 14,36,37
Mav 192,204,100
E 38
Mav 192,204,148
E 39
Mav 196,204,108
T 218,193,27136
C 15,38,39
Mav 196,204,156
E 20
Mav 200,204,116
E 21
Mav 200,204,164
T 220,193,29184
)"
R"(C 20,20,21
Mav 120,224,100
E 22
Mav 120,228,148
E 23
Mav 124,224,108
T 222,193,31232
C 21,22,23
Mav 124,228,156
E 44
Mav 128,224,116
E 45
Mav 128,228,164
E 46
Mav 132,232,100
E 47
)"
R"(Mav 132,236,148
E 52
Mav 136,232,108
E 53
Mav 136,236,156
E 54
Mav 140,232,116
E 55
Mav 140,236,164
E 76
Mav 144,240,100
W 0
E 77
Mav 144,244,148
E 78
Mav 148,240,108
)"
R"(E 79
Mav 148,244,156
C 22,44,45
C 23,46,47
Mav 152,240,116
C 52,52,53
F 60,215
Mav 152,244,164
E 60
Mav 156,248,100
C 53,54,55
F 61,215
Mav 156,252,148
E 61
Mav 160,248,108
C 54,76,77
)"
R"(C 55,78,79
Mav 160,252,156
F 62,215
F 63,215
Mav 164,248,116
E 62
Mav 164,252,164
W 0
s_barrier
N 224,200,0
Mav 168,208,100
N 228,201,0
F 84,215
Mav 168,212,148
N 232,200,2048
F 85,215
)"
R"(Mav 172,208,108
N 236,201,2048
F 86,215
Mav 172,212,156
N 240,200,4096
F 87,215
Mav 176,208,116
N 244,201,4096
C 60,60,61
Mav 176,212,164
N 248,200,6144
F 68,216
Mav 180,216,100
N 252,201,6144
F 69,216
Mav 180,220,148
)"
R"(N 208,200,8192
F 70,216
Mav 184,216,108
N 212,201,8192
F 71,216
Mav 184,220,156
E 63
Mav 188,216,116
N 216,200,10240
C 61,62,63
Mav 188,220,164
s_cbranch_vccnz .LrareB3_6
.LbackB3_6:
W 7
N 220,201,10240
F 92,216
)"
R"(Mvvz 100,224,0
E 84
Mvv 100,228,24
E 85
Mvv 100,232,48
E 86
Mvv 100,236,72
E 87
Mvvz 108,224,8
E 68
Mvv 108,228,32
E 69
Mvv 108,232,56
E 70
Mvv 108,236,80
E 71
)"
R"(Mvvz 116,224,16
N 224,200,12288
C 62,84,85
Mvv 116,228,40
N 228,201,12288
C 63,86,87
Mvv 116,232,64
N 232,200,14336
F 93,216
Mvv 116,236,88
W 3
N 236,201,14336
F 94,216
Mvv 100,240,96
E 92
Mvv 100,244,120
)"
R"(E 93
Mvv 100,248,144
E 94
Mvv 100,252,168
F 95,216
C 68,68,69
Mvv 108,240,104
E 95
Mvv 108,244,128
C 69,70,71
Mvv 108,248,152
C 70,92,93
C 71,94,95
Mvv 108,252,176
v_max_f32 v217, v100, v101
Mvv 116,240,112
)"
R"(N 240,200,16384
v_max3_f32 v217, v217, v102, v103
Mvv 116,244,136
N 244,201,16384
Mvv 116,248,160
N 248,200,18432
Mvv 116,252,184
N 252,201,18432
v_max_f32 v218, v108, v109
Mvvz 124,208,0
v_max3_f32 v218, v218, v110, v111
Mvv 124,212,24
Mvv 124,216,48
Mvv 124,220,72
s_nop 0
v_max_f32 v219, v116, v117
)"
R"(Mvvz 132,208,8
v_max3_f32 v219, v219, v118, v119
Mvv 132,212,32
Mvv 132,216,56
Mvv 132,220,80
Mvvz 140,208,16
N 208,200,20480
Mvv 140,212,40
N 212,201,20480
Mvv 140,216,64
N 216,200,22528
Mvv 140,220,88
W 3
N 220,201,22528
Mvv 124,224,96
Mvv 124,228,120
)"
R"(Mvv 124,232,144
Mvv 124,236,168
Mvv 132,224,104
Mvv 132,228,128
Mvv 132,232,152
Mvv 132,236,176
s_nop 3
v_max3_f32 v217, v217, v124, v125
Mvv 140,224,112
N 224,200,24576
v_max3_f32 v217, v217, v126, v127
Mvv 140,228,136
N 228,201,24576
Mvv 140,232,160
N 232,200,26624
Mvv 140,236,184
)"
R"(N 236,201,26624
v_max3_f32 v218, v218, v132, v133
Mvvz 148,240,0
v_max3_f32 v218, v218, v134, v135
Mvv 148,244,24
Mvv 148,248,48
Mvv 148,252,72
s_nop 0
v_max3_f32 v219, v219, v140, v141
Mvvz 156,240,8
v_max3_f32 v219, v219, v142, v143
Mvv 156,244,32
Mvv 156,248,56
Mvv 156,252,80
Mvvz 164,240,16
N 240,200,28672
)"
R"(Mvv 164,244,40
N 244,201,28672
Mvv 164,248,64
N 248,200,30720
Mvv 164,252,88
W 3
N 252,201,30720
Mvv 148,208,96
Mvv 148,212,120
Mvv 148,216,144
Mvv 148,220,168
Mvv 156,208,104
Mvv 156,212,128
Mvv 156,216,152
Mvv 156,220,176
s_nop 3
)"
R"(v_max3_f32 v217, v217, v148, v149
Mvv 164,208,112
v_max3_f32 v217, v217, v150, v151
Mvv 164,212,136
Mvv 164,216,160
Mvv 164,220,184
v_max3_f32 v218, v218, v156, v157
Mvvz 172,224,0
v_max3_f32 v218, v218, v158, v159
Mvv 172,228,24
Mvv 172,232,48
Mvv 172,236,72
s_nop 1
v_max3_f32 v219, v219, v164, v165
Mvvz 180,224,8
v_max3_f32 v219, v219, v166, v167
)"
R"(Mvv 180,228,32
Mvv 180,232,56
Mvv 180,236,80
Mvvz 188,224,16
Mvv 188,228,40
Mvv 188,232,64
Mvv 188,236,88
W 0
s_barrier
T 208,192,32768
Mvv 172,240,96
T 210,192,34816
T 212,192,36864
Mvv 172,244,120
T 214,192,38912
T 216,193,32768
)"
R"(Mvv 172,248,144
T 218,193,34816
Mvv 172,252,168
T 220,193,36864
Mvv 180,240,104
T 222,193,38912
Mvv 180,244,128
T 224,192,33280
Mvv 180,248,152
T 226,192,35328
Mvv 180,252,176
T 228,192,37376
v_max3_f32 v217, v217, v172, v173
Mvv 188,240,112
T 230,192,39424
v_max3_f32 v217, v217, v174, v175
)"
R"(Mvv 188,244,136
T 232,193,33280
v_cmp_gt_f32 vcc, v217, v211
Mvv 188,248,160
T 234,193,35328
Mvv 188,252,184
W 8
T 236,193,37376
v_max3_f32 v218, v218, v180, v181
Mav 0,208,4
T 238,193,39424
v_max3_f32 v218, v218, v182, v183
Mav 0,212,52
T 240,192,40960
v_cmp_gt_f32 s[68:69], v218, v212
Mav 4,208,12
)"
R"(T 242,192,43008
s_or_b64 vcc, vcc, s[68:69]
Mav 4,212,60
T 244,192,45056
v_max3_f32 v219, v219, v188, v189
Mav 8,208,20
T 246,192,47104
v_max3_f32 v219, v219, v190, v191
Mav 8,212,68
T 248,193,40960
v_cmp_gt_f32 s[70:71], v219, v213
Mav 12,216,4
W 8
T 250,193,43008
s_or_b64 vcc, vcc, s[70:71]
Mav 12,220,52
)"
R"(s_cbranch_vccnz .LrareA3_7
.LbackA3_7:
T 252,193,45056
F 100,214
Mav 16,216,12
T 254,193,47104
F 101,214
Mav 16,220,60
T 208,192,41472
F 102,214
Mav 20,216,20
T 210,192,43520
F 103,214
Mav 20,220,68
T 212,192,45568
F 124,214
)"
R"(Mav 24,224,4
T 214,192,47616
F 125,214
Mav 24,228,52
T 216,193,41472
F 126,214
Mav 28,224,12
T 218,193,43520
F 127,214
Mav 28,228,60
T 220,193,45568
F 108,215
Mav 32,224,20
T 222,193,47616
F 109,215
Mav 32,228,68
)"
R"(W 8
T 224,192,49152
F 110,215
Mav 36,232,4
T 226,192,51200
F 111,215
Mav 36,236,52
T 228,192,53248
F 132,215
Mav 40,232,12
T 230,192,55296
F 133,215
Mav 40,236,60
E 100
Mav 44,232,20
T 232,193,49152
)"
R"(F 134,215
Mav 44,236,68
T 234,193,51200
F 135,215
Mav 48,240,4
E 101
Mav 48,244,52
T 236,193,53248
C 100,100,101
Mav 52,240,12
E 102
Mav 52,244,60
E 103
Mav 56,240,20
T 238,193,55296
C 101,102,103
)"
R"(Mav 56,244,68
E 124
Mav 60,248,4
T 240,192,49664
F 116,216
Mav 60,252,52
T 242,192,51712
F 117,216
Mav 64,248,12
T 244,192,53760
F 118,216
Mav 64,252,60
T 246,192,55808
F 119,216
Mav 68,248,20
T 248,193,49664
)"
R"(F 140,216
Mav 68,252,68
W 6
T 250,193,51712
F 141,216
Mav 72,208,4
T 252,193,53760
F 142,216
Mav 72,212,52
T 254,193,55808
F 143,216
Mav 76,208,12
E 125
Mav 76,212,60
E 126
Mav 80,208,20
)"
R"(T 208,192,57344
C 102,124,125
Mav 80,212,68
E 127
Mav 84,216,4
T 210,192,59392
C 103,126,127
Mav 84,220,52
E 108
Mav 88,216,12
E 109
Mav 88,220,60
T 212,192,61440
C 108,108,109
Mav 92,216,20
E 110
)"
R"(Mav 92,220,68
E 111
Mav 96,224,4
T 214,192,63488
C 109,110,111
Mav 96,228,52
E 132
Mav 100,224,12
T 216,193,57344
F 148,214
Mav 100,228,60
T 218,193,59392
F 149,214
Mav 104,224,20
T 220,193,61440
F 150,214
)"
R"(Mav 104,228,68
T 222,193,63488
F 151,214
Mav 108,232,4
W 5
T 224,192,57856
F 172,214
Mav 108,236,52
T 226,192,59904
F 173,214
Mav 112,232,12
T 228,192,61952
F 174,214
Mav 112,236,60
T 230,192,64000
F 175,214
)"
R"(Mav 116,232,20
E 133
Mav 116,236,68
T 232,193,57856
C 110,132,133
Mav 192,204,4
E 134
Mav 192,204,52
E 135
Mav 196,204,12
T 234,193,59904
C 111,134,135
Mav 196,204,60
E 116
Mav 200,204,20
E 117
)"
R"(Mav 200,204,68
T 236,193,61952
C 116,116,117
Mav 120,240,4
E 118
Mav 120,244,52
E 119
Mav 124,240,12
T 238,193,64000
C 117,118,119
Mav 124,244,60
E 140
Mav 128,240,20
E 141
Mav 128,244,68
E 142
)"
R"(Mav 132,248,4
E 143
Mav 132,252,52
E 148
Mav 136,248,12
E 149
Mav 136,252,60
E 150
Mav 140,248,20
E 151
Mav 140,252,68
E 172
Mav 144,208,4
W 0
E 173
Mav 144,212,52
)"
R"(E 174
Mav 148,208,12
E 175
Mav 148,212,60
C 118,140,141
C 119,142,143
Mav 152,208,20
C 148,148,149
F 156,215
Mav 152,212,68
E 156
Mav 156,216,4
C 149,150,151
F 157,215
Mav 156,220,52
E 157
)"
R"(Mav 160,216,12
C 150,172,173
C 151,174,175
Mav 160,220,60
F 158,215
F 159,215
Mav 164,216,20
E 158
Mav 164,220,68
E 159
Mav 168,224,4
F 180,215
F 181,215
Mav 168,228,52
E 180
Mav 172,224,12
)"
R"(E 181
Mav 172,228,60
F 182,215
F 183,215
Mav 176,224,20
E 182
Mav 176,228,68
E 183
Mav 180,232,4
C 156,156,157
F 164,216
Mav 180,236,52
E 164
Mav 184,232,12
C 157,158,159
F 165,216
)"
R"(Mav 184,236,60
E 165
Mav 188,232,20
C 158,180,181
C 159,182,183
Mav 188,236,68
s_cbranch_vccnz .LrareB3_7
.LbackB3_7:
F 166,216
F 167,216
F 188,216
F 189,216
F 190,216
F 191,216
E 166
E 167
)"
R"(E 188
E 189
E 190
E 191
C 164,164,165
C 165,166,167
C 166,188,189
C 167,190,191
R 0,0
R 1,1
R 2,2
R 3,3
R 8,4
R 9,5
R 10,6
R 11,7
)"
R"(R 16,8
R 17,9
R 18,10
R 19,11
R 4,12
R 5,13
R 6,14
R 7,15
R 12,16
R 13,17
R 14,18
R 15,19
R 20,20
R 21,21
R 22,22
R 23,23
)"
R"(R 24,24
R 25,25
R 26,26
R 27,27
R 32,28
R 33,29
R 34,30
R 35,31
R 40,32
R 41,33
R 42,34
R 43,35
R 28,36
R 29,37
R 30,38
R 31,39
)"
R"(R 36,40
R 37,41
R 38,42
R 39,43
R 44,44
R 45,45
R 46,46
R 47,47
R 48,48
R 49,49
R 50,50
R 51,51
R 56,52
R 57,53
R 58,54
R 59,55
)"
R"(R 64,56
R 65,57
R 66,58
R 67,59
R 52,60
R 53,61
R 54,62
R 55,63
R 60,64
R 61,65
R 62,66
R 63,67
R 68,68
R 69,69
R 70,70
R 71,71
)"
R"(R 72,72
R 73,73
R 74,74
R 75,75
R 80,76
R 81,77
R 82,78
R 83,79
R 88,80
R 89,81
R 90,82
R 91,83
R 76,84
R 77,85
R 78,86
R 79,87
)"
R"(R 84,88
R 85,89
R 86,90
R 87,91
R 92,92
R 93,93
R 94,94
R 95,95
R 96,96
R 97,97
R 98,98
R 99,99
R 104,100
R 105,101
R 106,102
R 107,103
)"
R"(R 112,104
R 113,105
R 114,106
R 115,107
R 120,120
R 121,121
R 122,122
R 123,123
R 128,124
R 129,125
W 0
s_barrier
T 240,194,32768
T 242,194,34816
T 244,194,36864
T 246,194,38912
)"
R"(T 248,195,32768
T 250,195,34816
T 252,195,36864
T 254,195,38912
T 208,194,33280
Mav 192,204,100
T 210,194,35328
R 130,126
Mav 192,204,148
T 212,194,37376
R 131,127
Mav 196,204,108
T 214,194,39424
R 136,128
Mav 196,204,156
T 216,195,33280
)"
R"(R 137,129
Mav 200,204,116
T 218,195,35328
R 138,130
Mav 200,204,164
W 12
T 220,195,37376
R 139,131
Mvv 0,240,100
W 11
T 222,195,39424
R 144,144
Mvv 0,244,148
T 224,194,40960
R 145,145
Mvv 8,240,108
)"
R"(T 226,194,43008
R 146,146
Mvv 8,244,156
T 228,194,45056
R 147,147
Mvv 16,240,116
T 230,194,47104
R 152,148
Mvv 16,244,164
W 8
T 232,195,40960
R 153,149
Mvv 4,248,100
T 234,195,43008
R 154,150
Mvv 4,252,148
)"
R"(T 236,195,45056
R 155,151
Mvv 12,248,108
T 238,195,47104
R 160,152
Mvv 12,252,156
T 240,194,41472
R 161,153
Mvv 20,248,116
T 242,194,43520
R 162,154
Mvv 20,252,164
T 244,194,45568
R 163,155
Mvv 24,208,100
T 246,194,47616
)"
R"(R 168,168
Mvv 24,212,148
T 248,195,41472
R 169,169
Mvv 32,208,108
T 250,195,43520
R 170,170
Mvv 32,212,156
T 252,195,45568
R 171,171
Mvv 40,208,116
T 254,195,47616
R 176,172
Mvv 40,212,164
W 8
T 208,194,49152
)"
R"(R 177,173
Mvv 28,216,100
T 210,194,51200
R 178,174
Mvv 28,220,148
T 212,194,53248
R 179,175
Mvv 36,216,108
T 214,194,55296
R 184,176
Mvv 36,220,156
R 185,177
R 186,178
Mvv 44,216,116
T 216,195,49152
R 187,179
)"
R"(Mvv 44,220,164
T 218,195,51200
R 220,192
Mvv 48,224,100
T 220,195,53248
R 221,196
Mvv 48,228,148
T 222,195,55296
R 222,200
Mvv 56,224,108
v_rcp_f32 v220, v220
Mvv 56,228,156
buffer_store_dword v220, v206, s[72:75], 0 offen offset:0 sc1
Mvv 64,224,116
T 224,194,49664
C 0,0,1
)"
R"(Mvv 64,228,164
T 226,194,51712
C 1,2,3
Mvv 52,232,100
T 228,194,53760
C 2,4,5
Mvv 52,236,148
T 230,194,55808
C 3,6,7
Mvv 60,232,108
v_rcp_f32 v221, v221
Mvv 60,236,156
buffer_store_dword v221, v206, s[72:75], 0 offen offset:64 sc1
Mvv 68,232,116
T 232,195,49664
C 8,8,9
)"
R"(Mvv 68,236,164
W 5
T 234,195,51712
C 9,10,11
Mvv 72,240,100
T 236,195,53760
C 10,12,13
Mvv 72,244,148
T 238,195,55808
C 11,14,15
Mvv 80,240,108
v_rcp_f32 v222, v222
Mvv 80,244,156
buffer_store_dword v222, v206, s[72:75], 0 offen offset:128 sc1
Mvv 88,240,116
T 240,194,57344
)"
R"(C 16,16,17
Mvv 88,244,164
T 242,194,59392
C 17,18,19
Mvv 76,248,100
T 244,194,61440
C 18,20,21
Mvv 76,252,148
T 246,194,63488
C 19,22,23
Mvv 84,248,108
C 24,24,25
C 32,32,33
Mvv 84,252,156
C 25,26,27
C 33,34,35
)"
R"(Mvv 92,248,116
T 248,195,57344
C 26,28,29
Mvv 92,252,164
T 250,195,59392
C 27,30,31
Mvv 96,208,100
T 252,195,61440
C 34,36,37
Mvv 96,212,148
T 254,195,63488
C 35,38,39
Mvv 104,208,108
C 40,40,41
C 48,48,49
Mvv 104,212,156
)"
R"(C 41,42,43
C 49,50,51
Mvv 112,208,116
T 208,194,57856
C 42,44,45
Mvv 112,212,164
T 210,194,59904
C 43,46,47
Mav 108,216,100
T 212,194,61952
C 50,52,53
Mav 108,220,148
T 214,194,64000
C 51,54,55
Mav 112,216,108
C 56,56,57
)"
R"(C 64,64,65
Mav 112,220,156
C 57,58,59
C 65,66,67
Mav 116,216,116
T 216,195,57856
C 58,60,61
Mav 116,220,164
W 5
T 218,195,59904
C 59,62,63
Mvv 120,224,100
T 220,195,61952
C 66,68,69
Mvv 120,228,148
T 222,195,64000
)"
R"(C 67,70,71
Mvv 128,224,108
C 72,72,73
C 80,80,81
Mvv 128,228,156
C 73,74,75
C 81,82,83
Mvv 136,224,116
C 74,76,77
C 75,78,79
Mvv 136,228,164
C 82,84,85
C 83,86,87
Mav 132,232,100
C 88,88,89
C 96,96,97
)"
R"(Mav 132,236,148
C 89,90,91
C 97,98,99
Mav 136,232,108
C 90,92,93
C 91,94,95
Mav 136,236,156
R 242,108
R 243,109
Mav 140,232,116
C 98,242,243
R 244,110
Mav 140,236,164
R 245,111
C 104,104,105
Mvv 144,240,100
)"
R"(C 99,244,245
C 105,106,107
Mvv 144,244,148
R 246,112
R 247,113
Mvv 152,240,108
C 106,246,247
R 248,114
Mvv 152,244,156
R 249,115
C 112,112,113
Mvv 160,240,116
C 107,248,249
C 113,114,115
Mvv 160,244,164
R 250,116
)"
R"(R 251,117
Mav 156,248,100
C 114,250,251
R 242,118
Mav 156,252,148
R 243,119
C 120,120,121
Mav 160,248,108
C 115,242,243
C 121,122,123
Mav 160,252,156
R 244,132
R 245,133
Mav 164,248,116
C 122,244,245
R 246,134
)"
R"(Mav 164,252,164
W 0
s_barrier
T 224,196,0
Mvv 168,208,100
T 226,196,2048
R 247,135
Mvv 168,212,148
T 228,196,4096
C 123,246,247
Mvv 176,208,108
T 230,196,6144
C 128,128,129
Mvv 176,212,156
T 232,196,8192
C 129,130,131
)"
R"(Mvv 184,208,116
T 234,196,10240
R 248,136
Mvv 184,212,164
T 236,196,12288
R 249,137
Mav 180,216,100
T 238,196,14336
C 130,248,249
Mav 180,220,148
T 240,197,0
R 250,138
Mav 184,216,108
T 242,197,2048
R 251,139
Mav 184,220,156
)"
R"(T 244,197,4096
C 131,250,251
Mav 188,216,116
T 246,197,6144
C 136,136,137
Mav 188,220,164
W 8
T 248,197,8192
C 137,138,139
Mvvz 4,224,0
T 250,197,10240
R 242,140
Mvv 4,228,24
W 8
T 252,197,12288
R 243,141
)"
R"(Mvv 4,232,48
W 7
T 254,197,14336
C 138,242,243
Mvv 4,236,72
T 208,196,512
R 244,142
Mvvz 12,224,8
T 210,196,2560
R 245,143
Mvv 12,228,32
T 212,196,4608
C 139,244,245
Mvv 12,232,56
T 214,196,6656
C 144,144,145
)"
R"(Mvv 12,236,80
T 216,196,8704
C 145,146,147
Mvvz 20,224,16
ds_write_b128 v232, v[16:19] offset:0
Mvv 20,228,40
T 218,196,10752
R 246,156
Mvv 20,232,64
T 220,196,12800
R 247,157
Mvv 20,236,88
W 8
ds_write_b128 v232, v[40:43] offset:1024
Mvvz 28,240,0
T 222,196,14848
)"
R"(C 146,246,247
Mvv 28,244,24
T 224,197,512
R 248,158
Mvv 28,248,48
ds_write_b128 v232, v[64:67] offset:2048
Mvv 28,252,72
T 226,197,2560
R 249,159
Mvvz 36,240,8
T 228,197,4608
C 147,248,249
Mvv 36,244,32
ds_write_b128 v232, v[88:91] offset:3072
Mvv 36,248,56
T 230,197,6656
)"
R"(C 152,152,153
Mvv 36,252,80
T 232,197,8704
C 153,154,155
Mvvz 44,240,16
ds_write_b128 v232, v[112:115] offset:4096
Mvv 44,244,40
T 234,197,10752
R 250,160
Mvv 44,248,64
T 236,197,12800
R 251,161
Mvv 44,252,88
W 8
ds_write_b128 v232, v[136:139] offset:5120
Mvvz 52,208,0
)"
R"(T 238,197,14848
C 154,250,251
Mvv 52,212,24
T 240,196,16384
R 242,162
Mvv 52,216,48
T 242,196,18432
R 243,163
Mvv 52,220,72
T 244,196,20480
C 155,242,243
Mvvz 60,208,8
T 246,196,22528
C 160,160,161
Mvv 60,212,32
T 248,196,24576
)"
R"(C 161,162,163
Mvv 60,216,56
T 250,196,26624
R 244,164
Mvv 60,220,80
T 252,196,28672
R 245,165
Mvvz 68,208,16
T 254,196,30720
C 162,244,245
Mvv 68,212,40
T 208,197,16384
R 246,166
Mvv 68,216,64
T 210,197,18432
R 247,167
)"
R"(Mvv 68,220,88
W 8
T 212,197,20480
C 163,246,247
Mvvz 76,224,0
ds_write_b128 v232, v[160:163] offset:6144
Mvv 76,228,24
T 214,197,22528
C 168,168,169
Mvv 76,232,48
T 216,197,24576
C 169,170,171
Mvv 76,236,72
T 218,197,26624
R 248,180
Mvvz 84,224,8
)"
R"(T 220,197,28672
R 249,181
Mvv 84,228,32
T 222,197,30720
C 170,248,249
Mvv 84,232,56
R 250,182
R 251,183
Mvv 84,236,80
C 171,250,251
C 176,176,177
Mvvz 92,224,16
T 224,196,16896
C 177,178,179
Mvv 92,228,40
T 226,196,18944
)"
R"(R 242,184
Mvv 92,232,64
T 228,196,20992
R 243,185
Mvv 92,236,88
T 230,196,23040
C 178,242,243
Mvv 4,240,96
W 6
T 232,196,25088
R 244,186
Mvv 4,244,120
T 234,196,27136
R 245,187
Mvv 4,248,144
T 236,196,29184
)"
R"(C 179,244,245
Mvv 4,252,168
T 238,196,31232
C 184,184,185
Mvv 12,240,104
C 185,186,187
R 246,188
Mvv 12,244,128
R 247,189
R 248,190
Mvv 12,248,152
C 186,246,247
R 249,191
Mvv 12,252,176
C 187,248,249
C 4,4,5
)"
R"(Mvv 20,240,112
ds_write_b128 v232, v[184:187] offset:7168
Mvv 20,244,136
T 240,197,16896
C 5,6,7
Mvv 20,248,160
T 242,197,18944
Mvv 20,252,184
T 244,197,20992
C 12,12,13
Mvv 28,208,96
T 246,197,23040
C 13,14,15
Mvv 28,212,120
T 248,197,25088
Mvv 28,216,144
)"
R"(W 6
T 250,197,27136
Mvv 28,220,168
T 252,197,29184
C 20,20,21
Mvv 36,208,104
T 254,197,31232
C 21,22,23
Mvv 36,212,128
Mvv 36,216,152
Mvv 36,220,176
C 6,28,29
C 7,30,31
Mvv 44,208,112
buffer_store_dwordx4 v[4:7], v223, s[28:31], s55 offen offset:0 sc1
Mvv 44,212,136
)"
R"(Mvv 44,216,160
Mvv 44,220,184
s_nop 0
C 14,36,37
C 15,38,39
Mvv 52,224,96
buffer_store_dwordx4 v[12:15], v224, s[28:31], s55 offen offset:0 sc1
Mvv 52,228,120
Mvv 52,232,144
Mvv 52,236,168
C 22,44,45
C 23,46,47
Mvv 60,224,104
buffer_store_dwordx4 v[20:23], v225, s[28:31], s55 offen offset:0 sc1
Mvv 60,228,128
Mvv 60,232,152
)"
R"(Mvv 60,236,176
s_nop 0
C 52,52,53
Mvv 68,224,112
C 53,54,55
Mvv 68,228,136
Mvv 68,232,160
Mvv 68,236,184
W 0
s_barrier
T 208,192,0
Mvv 76,240,96
T 210,192,2048
C 60,60,61
Mvv 76,244,120
T 212,192,4096
)"
R"(C 61,62,63
Mvv 76,248,144
T 214,192,6144
T 216,192,8192
Mvv 76,252,168
T 218,192,10240
C 68,68,69
Mvv 84,240,104
T 220,192,12288
C 69,70,71
Mvv 84,244,128
T 222,192,14336
T 224,192,16384
Mvv 84,248,152
T 226,192,18432
Mvv 84,252,176
)"
R"(T 228,192,20480
C 54,76,77
Mvv 92,240,112
T 230,192,22528
C 55,78,79
Mvv 92,244,136
buffer_store_dwordx4 v[52:55], v223, s[28:31], s55 offen offset:1024 sc1
Mvv 92,248,160
T 232,192,24576
T 234,192,26624
Mvv 92,252,184
W 9
T 236,192,28672
C 62,84,85
Mvvz 100,208,0
T 238,192,30720
)"
R"(C 63,86,87
Mvv 100,212,24
W 9
buffer_store_dwordx4 v[60:63], v224, s[28:31], s55 offen offset:1024 sc1
Mvv 100,216,48
W 7
T 240,193,0
Mvv 100,220,72
T 242,193,2048
C 70,92,93
Mvvz 108,208,8
T 244,193,4096
C 71,94,95
Mvv 108,212,32
buffer_store_dwordx4 v[68:71], v225, s[28:31], s55 offen offset:1024 sc1
Mvv 108,216,56
)"
R"(T 246,193,6144
Mvv 108,220,80
T 248,193,8192
Mvvz 116,208,16
T 250,193,10240
Mvv 116,212,40
T 252,193,12288
Mvv 116,216,64
T 254,193,14336
Mvv 116,220,88
W 7
T 208,193,16384
Mvv 100,224,96
T 210,193,18432
Mvv 100,228,120
T 212,193,20480
)"
R"(Mvv 100,232,144
T 214,193,22528
Mvv 100,236,168
T 216,193,24576
Mvv 108,224,104
T 218,193,26624
Mvv 108,228,128
T 220,193,28672
Mvv 108,232,152
T 222,193,30720
Mvv 108,236,176
C 100,100,101
Mvv 116,224,112
T 224,192,512
C 101,102,103
Mvv 116,228,136
)"
R"(T 226,192,2560
Mvv 116,232,160
T 228,192,4608
Mvv 116,236,184
W 7
T 230,192,6656
C 108,108,109
Mvvz 124,240,0
T 232,192,8704
C 109,110,111
Mvv 124,244,24
T 234,192,10752
Mvv 124,248,48
T 236,192,12800
Mvv 124,252,72
T 238,192,14848
)"
R"(C 116,116,117
Mvvz 132,240,8
C 117,118,119
Mvv 132,244,32
Mvv 132,248,56
Mvv 132,252,80
Mvvz 140,240,16
T 240,192,16896
T 242,192,18944
Mvv 140,244,40
T 244,192,20992
Mvv 140,248,64
T 246,192,23040
Mvv 140,252,88
T 248,192,25088
Mvv 124,208,96
)"
R"(T 250,192,27136
Mvv 124,212,120
W 6
T 252,192,29184
Mvv 124,216,144
T 254,192,31232
Mvv 124,220,168
Mvv 132,208,104
Mvv 132,212,128
Mvv 132,216,152
Mvv 132,220,176
s_nop 3
C 102,124,125
C 103,126,127
Mvv 140,208,112
buffer_store_dwordx4 v[100:103], v223, s[28:31], s56 offen offset:0 sc1
)"
R"(Mvv 140,212,136
T 208,193,512
T 210,193,2560
Mvv 140,216,160
T 212,193,4608
T 214,193,6656
Mvv 140,220,184
T 216,193,8704
C 110,132,133
Mvvz 148,224,0
T 218,193,10752
C 111,134,135
Mvv 148,228,24
buffer_store_dwordx4 v[108:111], v224, s[28:31], s56 offen offset:0 sc1
Mvv 148,232,48
T 220,193,12800
)"
R"(T 222,193,14848
Mvv 148,236,72
C 118,140,141
C 119,142,143
Mvvz 156,224,8
buffer_store_dwordx4 v[116:119], v225, s[28:31], s56 offen offset:0 sc1
Mvv 156,228,32
T 224,193,16896
T 226,193,18944
Mvv 156,232,56
T 228,193,20992
T 230,193,23040
Mvv 156,236,80
W 8
T 232,193,25088
T 234,193,27136
)"
R"(Mvv 148,240,96
T 236,193,29184
T 238,193,31232
Mvv 148,244,120
Mvv 148,248,144
Mvv 148,252,168
Mvv 156,240,104
Mvv 156,244,128
Mvv 156,248,152
Mvv 156,252,176
s_nop 3
C 148,148,149
Mvvz 172,208,0
C 149,150,151
Mvv 172,212,24
W 0
)"
R"(Mvv 172,216,48
Mvv 172,220,72
W 0
s_barrier
T 240,194,0
Mvvz 180,208,8
T 242,194,2048
C 156,156,157
Mvv 180,212,32
T 244,194,4096
C 157,158,159
Mvv 180,216,56
T 246,194,6144
T 248,194,8192
Mvv 180,220,80
T 250,194,10240
)"
R"(T 252,194,12288
Mvv 172,224,96
T 254,194,14336
Mvv 172,228,120
T 208,194,16384
Mvv 172,232,144
T 210,194,18432
Mvv 172,236,168
T 212,194,20480
Mvv 180,224,104
T 214,194,22528
Mvv 180,228,128
T 216,194,24576
Mvv 180,232,152
T 218,194,26624
Mvv 180,236,176
)"
R"(W 9
T 220,194,28672
C 150,172,173
Mvvz 4,240,0
T 222,194,30720
C 151,174,175
Mvv 4,244,24
W 8
buffer_store_dwordx4 v[148:151], v223, s[28:31], s56 offen offset:1024 sc1
Mvv 4,248,48
T 224,195,0
Mvv 4,252,72
T 226,195,2048
C 158,180,181
Mvvz 12,240,8
T 228,195,4096
)"
R"(C 159,182,183
Mvv 12,244,32
buffer_store_dwordx4 v[156:159], v224, s[28:31], s56 offen offset:1024 sc1
Mvv 12,248,56
T 230,195,6144
Mvv 12,252,80
T 232,195,8192
Mvvz 20,240,16
T 234,195,10240
Mvv 20,244,40
T 236,195,12288
Mvv 20,248,64
T 238,195,14336
Mvv 20,252,88
W 7
T 240,195,16384
)"
R"(Mvv 4,208,96
T 242,195,18432
Mvv 4,212,120
T 244,195,20480
Mvv 4,216,144
T 246,195,22528
Mvv 4,220,168
T 248,195,24576
Mvv 12,208,104
T 250,195,26624
Mvv 12,212,128
T 252,195,28672
Mvv 12,216,152
T 254,195,30720
Mvv 12,220,176
C 4,4,5
)"
R"(Mvv 20,208,112
T 208,194,512
C 5,6,7
Mvv 20,212,136
T 210,194,2560
Mvv 20,216,160
T 212,194,4608
Mvv 20,220,184
W 7
T 214,194,6656
C 12,12,13
Mvvz 28,224,0
T 216,194,8704
C 13,14,15
Mvv 28,228,24
T 218,194,10752
)"
R"(Mvv 28,232,48
T 220,194,12800
Mvv 28,236,72
T 222,194,14848
C 20,20,21
Mvvz 36,224,8
C 21,22,23
Mvv 36,228,32
Mvv 36,232,56
Mvv 36,236,80
Mvvz 44,224,16
T 224,194,16896
Mvv 44,228,40
T 226,194,18944
Mvv 44,232,64
T 228,194,20992
)"
R"(Mvv 44,236,88
T 230,194,23040
Mvv 28,240,96
T 232,194,25088
Mvv 28,244,120
W 5
T 234,194,27136
Mvv 28,248,144
T 236,194,29184
Mvv 28,252,168
T 238,194,31232
Mvv 36,240,104
Mvv 36,244,128
Mvv 36,248,152
Mvv 36,252,176
s_nop 2
)"
R"(C 6,28,29
C 7,30,31
Mvv 44,240,112
buffer_store_dwordx4 v[4:7], v223, s[28:31], s57 offen offset:0 sc1
Mvv 44,244,136
T 240,195,512
Mvv 44,248,160
T 242,195,2560
Mvv 44,252,184
T 244,195,4608
C 14,36,37
Mvvz 52,208,0
T 246,195,6656
C 15,38,39
Mvv 52,212,24
buffer_store_dwordx4 v[12:15], v224, s[28:31], s57 offen offset:0 sc1
)"
R"(Mvv 52,216,48
T 248,195,8704
T 250,195,10752
Mvv 52,220,72
T 252,195,12800
C 22,44,45
Mvvz 60,208,8
T 254,195,14848
C 23,46,47
Mvv 60,212,32
buffer_store_dwordx4 v[20:23], v225, s[28:31], s57 offen offset:0 sc1
Mvv 60,216,56
T 208,195,16896
T 210,195,18944
Mvv 60,220,80
W 8
)"
R"(T 212,195,20992
Mvv 52,224,96
T 214,195,23040
Mvv 52,228,120
T 216,195,25088
Mvv 52,232,144
T 218,195,27136
Mvv 52,236,168
T 220,195,29184
Mvv 60,224,104
T 222,195,31232
Mvv 60,228,128
T 224,192,32768
Mvv 60,232,152
T 226,192,34816
Mvv 60,236,176
)"
R"(T 228,192,36864
C 52,52,53
Mvvz 76,240,0
W 8
T 230,192,38912
C 53,54,55
Mvv 76,244,24
T 232,192,40960
Mvv 76,248,48
T 234,192,43008
Mvv 76,252,72

T 236,192,45056
C 60,60,61
Mvvz 84,240,8
T 238,192,47104
)"
R"(C 61,62,63
Mvv 84,244,32
T 240,192,49152
Mvv 84,248,56
T 242,192,51200
Mvv 84,252,80
T 244,192,53248
Mvv 76,208,96
W 8
T 246,192,55296
Mvv 76,212,120
T 248,192,57344
Mvv 76,216,144
T 250,192,59392
Mvv 76,220,168
T 252,192,61440
)"
R"(Mvv 84,208,104
T 254,192,63488
Mvv 84,212,128
T 208,193,32768
Mvv 84,216,152
T 210,193,34816
Mvv 84,220,176
T 212,193,36864
C 54,76,77
Mvvz 100,224,0
W 8
T 214,193,38912
C 55,78,79
Mvv 100,228,24
buffer_store_dwordx4 v[52:55], v223, s[28:31], s57 offen offset:1024 sc1
Mvv 100,232,48
)"
R"(T 216,193,40960
Mvv 100,236,72
T 218,193,43008
C 62,84,85
Mvvz 108,224,8
T 220,193,45056
C 63,86,87
Mvv 108,228,32
buffer_store_dwordx4 v[60:63], v224, s[28:31], s57 offen offset:1024 sc1
Mvv 108,232,56
T 222,193,47104
Mvv 108,236,80
Mvvz 116,224,16
T 224,193,49152
Mvv 116,228,40
T 226,193,51200
)"
R"(Mvv 116,232,64
T 228,193,53248
Mvv 116,236,88
T 230,193,55296
Mvv 100,240,96
W 6
T 232,193,57344
Mvv 100,244,120
T 234,193,59392
Mvv 100,248,144
T 236,193,61440
Mvv 100,252,168
T 238,193,63488
Mvv 108,240,104
Mvv 108,244,128
Mvv 108,248,152
)"
R"(Mvv 108,252,176
s_nop 2
C 100,100,101
Mvv 116,240,112
T 240,192,33280
C 101,102,103
Mvv 116,244,136
T 242,192,35328
Mvv 116,248,160
T 244,192,37376
Mvv 116,252,184
T 246,192,39424
C 108,108,109
Mvvz 124,208,0
T 248,192,41472
C 109,110,111
)"
R"(Mvv 124,212,24
T 250,192,43520
Mvv 124,216,48
W 6
T 252,192,45568
Mvv 124,220,72
T 254,192,47616
C 116,116,117
Mvvz 132,208,8
C 117,118,119
Mvv 132,212,32
Mvv 132,216,56
Mvv 132,220,80
Mvvz 140,208,16
T 208,192,49664
T 210,192,51712
)"
R"(Mvv 140,212,40
T 212,192,53760
Mvv 140,216,64
T 214,192,55808
Mvv 140,220,88
T 216,192,57856
Mvv 124,224,96
T 218,192,59904
Mvv 124,228,120
T 220,192,61952
Mvv 124,232,144
T 222,192,64000
Mvv 124,236,168
Mvv 132,224,104
Mvv 132,228,128
Mvv 132,232,152
)"
R"(Mvv 132,236,176
s_nop 3
C 102,124,125
C 103,126,127
Mvv 140,224,112
buffer_store_dwordx4 v[100:103], v223, s[28:31], s58 offen offset:0 sc1
Mvv 140,228,136
T 224,193,33280
T 226,193,35328
Mvv 140,232,160
T 228,193,37376
T 230,193,39424
Mvv 140,236,184
W 4
T 232,193,41472
C 110,132,133
)"
R"(Mvvz 148,240,0
T 234,193,43520
C 111,134,135
Mvv 148,244,24
buffer_store_dwordx4 v[108:111], v224, s[28:31], s58 offen offset:0 sc1
Mvv 148,248,48
T 236,193,45568
T 238,193,47616
Mvv 148,252,72
C 118,140,141
C 119,142,143
Mvvz 156,240,8
buffer_store_dwordx4 v[116:119], v225, s[28:31], s58 offen offset:0 sc1
Mvv 156,244,32
T 240,193,49664
T 242,193,51712
)"
R"(Mvv 156,248,56
T 244,193,53760
T 246,193,55808
Mvv 156,252,80
T 248,193,57856
T 250,193,59904
Mvv 148,208,96
T 252,193,61952
T 254,193,64000
Mvv 148,212,120
Mvv 148,216,144
Mvv 148,220,168
Mvv 156,208,104
Mvv 156,212,128
Mvv 156,216,152
Mvv 156,220,176
)"
R"(W 4
s_nop 2
C 148,148,149
Mvvz 172,224,0
C 149,150,151
Mvv 172,228,24
Mvv 172,232,48
Mvv 172,236,72
W 0
s_barrier
T 208,196,0
Mvvz 180,224,8
T 210,196,2048
C 156,156,157
Mvv 180,228,32
T 212,196,4096
)"
R"(C 157,158,159
Mvv 180,232,56
T 214,196,6144
T 216,196,8192
Mvv 180,236,80
T 218,196,10240
T 220,196,12288
Mvv 172,240,96
T 222,196,14336
Mvv 172,244,120
T 224,196,16384
Mvv 172,248,144
T 226,196,18432
Mvv 172,252,168
T 228,196,20480
Mvv 180,240,104
)"
R"(T 230,196,22528
Mvv 180,244,128
T 232,196,24576
Mvv 180,248,152
T 234,196,26624
Mvv 180,252,176
W 9
T 236,196,28672
C 150,172,173
Mvvz 4,208,0
T 238,196,30720
C 151,174,175
Mvv 4,212,24
W 8
buffer_store_dwordx4 v[148:151], v223, s[28:31], s58 offen offset:1024 sc1
Mvv 4,216,48
)"
R"(T 240,197,0
Mvv 4,220,72
T 242,197,2048
C 158,180,181
Mvvz 12,208,8
T 244,197,4096
C 159,182,183
Mvv 12,212,32
buffer_store_dwordx4 v[156:159], v224, s[28:31], s58 offen offset:1024 sc1
Mvv 12,216,56
T 246,197,6144
Mvv 12,220,80
T 248,197,8192
Mvvz 20,208,16
T 250,197,10240
Mvv 20,212,40
)"
R"(T 252,197,12288
Mvv 20,216,64
T 254,197,14336
Mvv 20,220,88
W 7
T 208,197,16384
Mvv 4,224,96
T 210,197,18432
Mvv 4,228,120
T 212,197,20480
Mvv 4,232,144
T 214,197,22528
Mvv 4,236,168
T 216,197,24576
Mvv 12,224,104
T 218,197,26624
)"
R"(Mvv 12,228,128
T 220,197,28672
Mvv 12,232,152
T 222,197,30720
Mvv 12,236,176
C 4,4,5
Mvv 20,224,112
T 224,196,512
C 5,6,7
Mvv 20,228,136
T 226,196,2560
Mvv 20,232,160
T 228,196,4608
Mvv 20,236,184
W 7
T 230,196,6656
)"
R"(C 12,12,13
Mvvz 28,240,0
T 232,196,8704
C 13,14,15
Mvv 28,244,24
T 234,196,10752
Mvv 28,248,48
T 236,196,12800
Mvv 28,252,72
T 238,196,14848
C 20,20,21
Mvvz 36,240,8
C 21,22,23
Mvv 36,244,32
Mvv 36,248,56
Mvv 36,252,80
)"
R"(Mvvz 44,240,16
T 240,196,16896
T 242,196,18944
Mvv 44,244,40
T 244,196,20992
Mvv 44,248,64
T 246,196,23040
Mvv 44,252,88
T 248,196,25088
Mvv 28,208,96
T 250,196,27136
Mvv 28,212,120
W 6
T 252,196,29184
Mvv 28,216,144
T 254,196,31232
)"
R"(Mvv 28,220,168
Mvv 36,208,104
Mvv 36,212,128
Mvv 36,216,152
Mvv 36,220,176
s_nop 3
C 6,28,29
C 7,30,31
Mvv 44,208,112
buffer_store_dwordx4 v[4:7], v223, s[28:31], s59 offen offset:0 sc1
Mvv 44,212,136
T 208,197,512
T 210,197,2560
Mvv 44,216,160
T 212,197,4608
T 214,197,6656
)"
R"(Mvv 44,220,184
T 216,197,8704
C 14,36,37
Mvvz 52,224,0
T 218,197,10752
C 15,38,39
Mvv 52,228,24
buffer_store_dwordx4 v[12:15], v224, s[28:31], s59 offen offset:0 sc1
Mvv 52,232,48
T 220,197,12800
T 222,197,14848
Mvv 52,236,72
C 22,44,45
C 23,46,47
Mvvz 60,224,8
buffer_store_dwordx4 v[20:23], v225, s[28:31], s59 offen offset:0 sc1
)"
R"(Mvv 60,228,32
T 224,197,16896
T 226,197,18944
Mvv 60,232,56
T 228,197,20992
T 230,197,23040
Mvv 60,236,80
W 8
T 232,197,25088
T 234,197,27136
Mvv 52,240,96
T 236,197,29184
T 238,197,31232
Mvv 52,244,120
Mvv 52,248,144
Mvv 52,252,168
)"
R"(Mvv 60,240,104
Mvv 60,244,128
Mvv 60,248,152
Mvv 60,252,176
s_nop 3
C 52,52,53
Mvvz 76,208,0
C 53,54,55
Mvv 76,212,24
W 0
Mvv 76,216,48
Mvv 76,220,72
s_waitcnt vmcnt(3) lgkmcnt(0)
s_barrier
T 240,192,0
Mvvz 84,208,8
)"
R"(T 242,192,2048
C 60,60,61
Mvv 84,212,32
T 244,192,4096
C 61,62,63
Mvv 84,216,56
T 246,192,6144
T 248,192,8192
Mvv 84,220,80
T 250,192,10240
T 252,192,12288
Mvv 76,224,96
T 254,192,14336
Mvv 76,228,120
T 208,192,16384
Mvv 76,232,144
)"
R"(T 210,192,18432
Mvv 76,236,168
T 212,192,20480
Mvv 84,224,104
T 214,192,22528
Mvv 84,228,128
T 216,192,24576
Mvv 84,232,152
T 218,192,26624
Mvv 84,236,176
W 9
T 220,192,28672
C 54,76,77
Mvvz 100,240,0
T 222,192,30720
C 55,78,79
)"
R"(Mvv 100,244,24
W 8
buffer_store_dwordx4 v[52:55], v223, s[28:31], s59 offen offset:1024 sc1
Mvv 100,248,48
T 224,193,0
Mvv 100,252,72
T 226,193,2048
C 62,84,85
Mvvz 108,240,8
T 228,193,4096
C 63,86,87
Mvv 108,244,32
buffer_store_dwordx4 v[60:63], v224, s[28:31], s59 offen offset:1024 sc1
Mvv 108,248,56
T 230,193,6144
Mvv 108,252,80
)"
R"(T 232,193,8192
Mvvz 116,240,16
T 234,193,10240
Mvv 116,244,40
T 236,193,12288
Mvv 116,248,64
T 238,193,14336
Mvv 116,252,88
W 7
T 240,193,16384
Mvv 100,208,96
T 242,193,18432
Mvv 100,212,120
T 244,193,20480
Mvv 100,216,144
T 246,193,22528
)"
R"(Mvv 100,220,168
T 248,193,24576
Mvv 108,208,104
T 250,193,26624
Mvv 108,212,128
T 252,193,28672
Mvv 108,216,152
T 254,193,30720
Mvv 108,220,176
C 100,100,101
Mvv 116,208,112
T 208,192,512
C 101,102,103
Mvv 116,212,136
T 210,192,2560
Mvv 116,216,160
)"
R"(T 212,192,4608
Mvv 116,220,184
W 7
T 214,192,6656
C 108,108,109
Mvvz 124,224,0
T 216,192,8704
C 109,110,111
Mvv 124,228,24
T 218,192,10752
Mvv 124,232,48
T 220,192,12800
Mvv 124,236,72
T 222,192,14848
C 116,116,117
Mvvz 132,224,8
)"
R"(C 117,118,119
Mvv 132,228,32
Mvv 132,232,56
Mvv 132,236,80
Mvvz 140,224,16
T 224,192,16896
Mvv 140,228,40
T 226,192,18944
Mvv 140,232,64
T 228,192,20992
Mvv 140,236,88
T 230,192,23040
Mvv 124,240,96
T 232,192,25088
Mvv 124,244,120
W 5
)"
R"(T 234,192,27136
Mvv 124,248,144
T 236,192,29184
Mvv 124,252,168
T 238,192,31232
Mvv 132,240,104
Mvv 132,244,128
Mvv 132,248,152
Mvv 132,252,176
s_nop 2
C 102,124,125
C 103,126,127
Mvv 140,240,112
buffer_store_dwordx4 v[100:103], v223, s[28:31], s60 offen offset:0 sc1
Mvv 140,244,136
T 240,193,512
)"
R"(Mvv 140,248,160
T 242,193,2560
Mvv 140,252,184
T 244,193,4608
C 110,132,133
Mvvz 148,208,0
T 246,193,6656
C 111,134,135
Mvv 148,212,24
buffer_store_dwordx4 v[108:111], v224, s[28:31], s60 offen offset:0 sc1
Mvv 148,216,48
T 248,193,8704
T 250,193,10752
Mvv 148,220,72
T 252,193,12800
C 118,140,141
)"
R"(Mvvz 156,208,8
T 254,193,14848
C 119,142,143
Mvv 156,212,32
buffer_store_dwordx4 v[116:119], v225, s[28:31], s60 offen offset:0 sc1
Mvv 156,216,56
T 208,193,16896
T 210,193,18944
Mvv 156,220,80
W 8
T 212,193,20992
Mvv 148,224,96
T 214,193,23040
Mvv 148,228,120
T 216,193,25088
Mvv 148,232,144
)"
R"(T 218,193,27136
Mvv 148,236,168
T 220,193,29184
Mvv 156,224,104
T 222,193,31232
Mvv 156,228,128
T 224,194,32768
Mvv 156,232,152
T 226,194,34816
Mvv 156,236,176
T 228,194,36864
C 148,148,149
Mvvz 172,240,0
W 8
T 230,194,38912
C 149,150,151
)"
R"(Mvv 172,244,24
T 232,194,40960
Mvv 172,248,48
T 234,194,43008
Mvv 172,252,72

T 236,194,45056
C 156,156,157
Mvvz 180,240,8
T 238,194,47104
C 157,158,159
Mvv 180,244,32
T 240,194,49152
Mvv 180,248,56
T 242,194,51200
Mvv 180,252,80
)"
R"(T 244,194,53248
Mvv 172,208,96
W 8
T 246,194,55296
Mvv 172,212,120
T 248,194,57344
Mvv 172,216,144
T 250,194,59392
Mvv 172,220,168
T 252,194,61440
Mvv 180,208,104
T 254,194,63488
Mvv 180,212,128
T 208,195,32768
Mvv 180,216,152
T 210,195,34816
)"
R"(Mvv 180,220,176
T 212,195,36864
C 150,172,173
Mvvz 4,224,0
W 8
T 214,195,38912
C 151,174,175
Mvv 4,228,24
buffer_store_dwordx4 v[148:151], v223, s[28:31], s60 offen offset:1024 sc1
Mvv 4,232,48
T 216,195,40960
Mvv 4,236,72
T 218,195,43008
C 158,180,181
Mvvz 12,224,8
T 220,195,45056
)"
R"(C 159,182,183
Mvv 12,228,32
buffer_store_dwordx4 v[156:159], v224, s[28:31], s60 offen offset:1024 sc1
Mvv 12,232,56
T 222,195,47104
Mvv 12,236,80
Mvvz 20,224,16
T 224,195,49152
Mvv 20,228,40
T 226,195,51200
Mvv 20,232,64
T 228,195,53248
Mvv 20,236,88
T 230,195,55296
Mvv 4,240,96
W 6
)"
R"(T 232,195,57344
Mvv 4,244,120
T 234,195,59392
Mvv 4,248,144
T 236,195,61440
Mvv 4,252,168
T 238,195,63488
Mvv 12,240,104
Mvv 12,244,128
Mvv 12,248,152
Mvv 12,252,176
s_nop 2
C 4,4,5
Mvv 20,240,112
T 240,194,33280
C 5,6,7
)"
R"(Mvv 20,244,136
T 242,194,35328
Mvv 20,248,160
T 244,194,37376
Mvv 20,252,184
T 246,194,39424
C 12,12,13
Mvvz 28,208,0
T 248,194,41472
C 13,14,15
Mvv 28,212,24
T 250,194,43520
Mvv 28,216,48
W 6
T 252,194,45568
Mvv 28,220,72
)"
R"(T 254,194,47616
C 20,20,21
Mvvz 36,208,8
C 21,22,23
Mvv 36,212,32
Mvv 36,216,56
Mvv 36,220,80
Mvvz 44,208,16
T 208,194,49664
Mvv 44,212,40
T 210,194,51712
Mvv 44,216,64
T 212,194,53760
Mvv 44,220,88
T 214,194,55808
Mvv 28,224,96
)"
R"(T 216,194,57856
Mvv 28,228,120
T 218,194,59904
Mvv 28,232,144
T 220,194,61952
Mvv 28,236,168
T 222,194,64000
Mvv 36,224,104
Mvv 36,228,128
Mvv 36,232,152
Mvv 36,236,176
s_nop 2
C 6,28,29
C 7,30,31
Mvv 44,224,112
buffer_load_dword v237, v207, s[72:75], 0 offen sc1
)"
R"(Mvv 44,228,136
buffer_store_dwordx4 v[4:7], v223, s[28:31], s61 offen offset:0 sc1
Mvv 44,232,160
T 224,195,33280
T 226,195,35328
Mvv 44,236,184
W 3
T 228,195,37376
C 14,36,37
Mvvz 52,240,0
T 230,195,39424
C 15,38,39
Mvv 52,244,24
buffer_store_dwordx4 v[12:15], v224, s[28:31], s61 offen offset:0 sc1
Mvv 52,248,48
T 232,195,41472
)"
R"(T 234,195,43520
Mvv 52,252,72
T 236,195,45568
C 22,44,45
Mvvz 60,240,8
T 238,195,47616
C 23,46,47
Mvv 60,244,32
buffer_store_dwordx4 v[20:23], v225, s[28:31], s61 offen offset:0 sc1
Mvv 60,248,56
T 240,195,49664
T 242,195,51712
Mvv 60,252,80
T 244,195,53760
Mvv 52,208,96
T 246,195,55808
)"
R"(Mvv 52,212,120
T 248,195,57856
Mvv 52,216,144
W 9
T 250,195,59904
Mvv 52,220,168
T 252,195,61952
Mvv 60,208,104
T 254,195,64000
Mvv 60,212,128
Mvv 60,216,152
Mvv 60,220,176
s_nop 1
C 52,52,53
Mvvz 76,224,0
C 53,54,55
)"
R"(Mvv 76,228,24
W 4
Mvv 76,232,48
Mvv 76,236,72
C 60,60,61
Mvvz 84,224,8
C 61,62,63
Mvv 84,228,32
Mvv 84,232,56
Mvv 84,236,80
Mvv 76,240,96
Mvv 76,244,120
W 0
Mvv 76,248,144
Mvv 76,252,168
Mvv 84,240,104
)"
R"(Mvv 84,244,128
Mvv 84,248,152
Mvv 84,252,176
s_nop 3
C 54,76,77
C 55,78,79
buffer_store_dwordx4 v[52:55], v223, s[28:31], s61 offen offset:1024 sc1
s_nop 0
C 62,84,85
C 63,86,87
buffer_store_dwordx4 v[60:63], v224, s[28:31], s61 offen offset:1024 sc1
s_waitcnt vmcnt(5)
.Ltail:
v_lshlrev_b32 v200, 2, v207
v_and_b32 v221, 15, v255
v_bfe_u32 v202, v255, 4, 2
)"
R"(v_lshlrev_b32 v202, 10, v202
v_lshl_add_u32 v202, v221, 2, v202
v_add_u32 v203, 4096, v202
v_add_u32 v204, 8192, v202
v_add_u32 v205, 12288, v202
s_lshl_b32 s45, 0x10001, s78
s_mov_b32 s46, 1048576
.LspinA:
v_cmp_ne_u32 vcc, v237, v233
s_sub_u32 s46, s46, 1
s_cbranch_scc1 .LgoA
s_or_b32 s44, vcc_lo, s45
s_and_b32 s80, s44, 0xffff
s_cmp_eq_u32 s80, 0xffff
s_cbranch_scc1 .LgoA
s_sleep 8
)"
R"(buffer_load_dword v237, v207, s[72:75], 0 offen sc1
s_waitcnt vmcnt(0)
s_branch .LspinA
.LgoA:
buffer_load_dword v238, v202, s[72:75], s36 offen offset:1024 sc1
buffer_load_dword v239, v203, s[72:75], s36 offen offset:1024 sc1
buffer_load_dword v240, v204, s[72:75], s36 offen offset:1024 sc1
buffer_load_dword v241, v205, s[72:75], s36 offen offset:1024 sc1
buffer_load_dwordx4 v[0:3], v200, s[8:11], s34 offen sc1
s_add_u32 s64, s34, 143360
buffer_load_dwordx4 v[4:7], v200, s[8:11], s64 offen sc1
s_add_u32 s64, s34, 286720
buffer_load_dwordx4 v[8:11], v200, s[8:11], s64 offen sc1
s_add_u32 s64, s34, 430080
buffer_load_dwordx4 v[12:15], v200, s[8:11], s64 offen sc1
s_add_u32 s64, s34, 573440
)"
R"(buffer_load_dwordx4 v[16:19], v200, s[8:11], s64 offen sc1
v_add_u32 v216, 1, v233
v_add_u32 v217, 2, v233
buffer_store_dword v217, v252, s[72:75], 0 offen sc1
s_lshl2_add_u32 s80, s78, 64
s_cmp_eq_u32 s76, 3
s_cselect_b32 s80, s80, 1
v_cmp_eq_u32 vcc, s80, v207
v_mov_b32 v218, 0x100000
v_bfe_u32 v220, v255, 4, 1
v_cndmask_b32 v218, v218, v207, vcc
v_mul_u32_u24 v201, 896, v221
v_bfe_u32 v221, v255, 5, 1
v_lshl_or_b32 v220, v220, 1, v221
v_lshl_add_u32 v201, v220, 4, v201
s_mov_b32 s68, 0xffff0000
)"
R"(s_waitcnt vmcnt(10)
s_barrier
buffer_store_dword v216, v218, s[72:75], 0 offen sc1
s_cmp_eq_u32 s76, 2
s_cselect_b32 s80, s78, 99
s_cmp_lt_u32 s80, 12
s_cselect_b32 s69, 1, 0
s_cbranch_scc0 .Lnoextra
buffer_load_dword v246, v202, s[72:75], s42 offen offset:1024 sc1
buffer_load_dword v247, v203, s[72:75], s42 offen offset:1024 sc1
buffer_load_dword v248, v204, s[72:75], s42 offen offset:1024 sc1
buffer_load_dword v249, v205, s[72:75], s42 offen offset:1024 sc1
buffer_load_dwordx4 v[128:131], v200, s[8:11], s40 offen sc1
s_add_u32 s64, s40, 143360
buffer_load_dwordx4 v[132:135], v200, s[8:11], s64 offen sc1
s_add_u32 s64, s40, 286720
)"
R"(buffer_load_dwordx4 v[136:139], v200, s[8:11], s64 offen sc1
s_add_u32 s64, s40, 430080
buffer_load_dwordx4 v[140:143], v200, s[8:11], s64 offen sc1
s_add_u32 s64, s40, 573440
buffer_load_dwordx4 v[144:147], v200, s[8:11], s64 offen sc1
s_add_u32 s64, s40, 716800
buffer_load_dwordx4 v[148:151], v200, s[8:11], s64 offen sc1
s_add_u32 s64, s40, 860160
buffer_load_dwordx4 v[152:155], v200, s[8:11], s64 offen sc1
s_add_u32 s64, s40, 1003520
buffer_load_dwordx4 v[156:159], v200, s[8:11], s64 offen sc1
s_add_u32 s64, s40, 1146880
buffer_load_dwordx4 v[160:163], v200, s[8:11], s64 offen sc1
s_add_u32 s64, s40, 1290240
buffer_load_dwordx4 v[164:167], v200, s[8:11], s64 offen sc1
s_add_u32 s64, s40, 1433600
)"
R"(buffer_load_dwordx4 v[168:171], v200, s[8:11], s64 offen sc1
s_add_u32 s64, s40, 1576960
buffer_load_dwordx4 v[172:175], v200, s[8:11], s64 offen sc1
s_add_u32 s64, s40, 1720320
buffer_load_dwordx4 v[176:179], v200, s[8:11], s64 offen sc1
s_add_u32 s64, s40, 1863680
buffer_load_dwordx4 v[180:183], v200, s[8:11], s64 offen sc1
s_add_u32 s64, s40, 2007040
buffer_load_dwordx4 v[184:187], v200, s[8:11], s64 offen sc1
s_add_u32 s64, s40, 2150400
buffer_load_dwordx4 v[188:191], v200, s[8:11], s64 offen sc1
.Lnoextra:
buffer_load_dword v242, v202, s[72:75], s39 offen offset:1024 sc1
buffer_load_dword v243, v203, s[72:75], s39 offen offset:1024 sc1
buffer_load_dword v244, v204, s[72:75], s39 offen offset:1024 sc1
buffer_load_dword v245, v205, s[72:75], s39 offen offset:1024 sc1
)"
R"(s_add_u32 s64, s34, 716800
buffer_load_dwordx4 v[20:23], v200, s[8:11], s64 offen sc1
s_add_u32 s64, s34, 860160
buffer_load_dwordx4 v[24:27], v200, s[8:11], s64 offen sc1
s_add_u32 s64, s34, 1003520
buffer_load_dwordx4 v[28:31], v200, s[8:11], s64 offen sc1
s_add_u32 s64, s34, 1146880
buffer_load_dwordx4 v[32:35], v200, s[8:11], s64 offen sc1
s_add_u32 s64, s34, 1290240
buffer_load_dwordx4 v[36:39], v200, s[8:11], s64 offen sc1
s_add_u32 s64, s34, 1433600
buffer_load_dwordx4 v[40:43], v200, s[8:11], s64 offen sc1
s_add_u32 s64, s34, 1576960
buffer_load_dwordx4 v[44:47], v200, s[8:11], s64 offen sc1
s_add_u32 s64, s34, 1720320
buffer_load_dwordx4 v[48:51], v200, s[8:11], s64 offen sc1
)"
R"(s_add_u32 s64, s34, 1863680
buffer_load_dwordx4 v[52:55], v200, s[8:11], s64 offen sc1
buffer_load_dword v237, v207, s[72:75], 0 offen sc1
s_add_u32 s64, s34, 2007040
buffer_load_dwordx4 v[56:59], v200, s[8:11], s64 offen sc1
s_add_u32 s64, s34, 2150400
buffer_load_dwordx4 v[60:63], v200, s[8:11], s64 offen sc1
s_waitcnt vmcnt(2)
s_mov_b32 s43, 0xffff
s_mov_b32 s70, 0
s_mov_b32 s46, 1048576
.LroundB:
v_cmp_ne_u32 vcc, v237, v233
s_sub_u32 s46, s46, 1
s_cselect_b32 s80, -1, s45
s_or_b32 s44, vcc_lo, s80
)"
R"(s_andn2_b32 s44, s44, s43
s_cbranch_scc0 .Lnone
s_or_b32 s43, s43, s44
s_bitcmp1_b32 s44, 16
s_cbranch_scc0 .Lskip0
buffer_load_dwordx4 v[64:67], v200, s[8:11], s37 offen sc1
.Lskip0:
s_bitcmp1_b32 s44, 17
s_cbranch_scc0 .Lskip1
s_add_u32 s64, s37, 143360
buffer_load_dwordx4 v[68:71], v200, s[8:11], s64 offen sc1
.Lskip1:
s_bitcmp1_b32 s44, 18
s_cbranch_scc0 .Lskip2
s_add_u32 s64, s37, 286720
buffer_load_dwordx4 v[72:75], v200, s[8:11], s64 offen sc1
)"
R"(.Lskip2:
s_bitcmp1_b32 s44, 19
s_cbranch_scc0 .Lskip3
s_add_u32 s64, s37, 430080
buffer_load_dwordx4 v[76:79], v200, s[8:11], s64 offen sc1
.Lskip3:
s_bitcmp1_b32 s44, 20
s_cbranch_scc0 .Lskip4
s_add_u32 s64, s37, 573440
buffer_load_dwordx4 v[80:83], v200, s[8:11], s64 offen sc1
.Lskip4:
s_bitcmp1_b32 s44, 21
s_cbranch_scc0 .Lskip5
s_add_u32 s64, s37, 716800
buffer_load_dwordx4 v[84:87], v200, s[8:11], s64 offen sc1
.Lskip5:
)"
R"(s_bitcmp1_b32 s44, 22
s_cbranch_scc0 .Lskip6
s_add_u32 s64, s37, 860160
buffer_load_dwordx4 v[88:91], v200, s[8:11], s64 offen sc1
.Lskip6:
s_bitcmp1_b32 s44, 23
s_cbranch_scc0 .Lskip7
s_add_u32 s64, s37, 1003520
buffer_load_dwordx4 v[92:95], v200, s[8:11], s64 offen sc1
.Lskip7:
s_bitcmp1_b32 s44, 24
s_cbranch_scc0 .Lskip8
s_add_u32 s64, s37, 1146880
buffer_load_dwordx4 v[96:99], v200, s[8:11], s64 offen sc1
.Lskip8:
s_bitcmp1_b32 s44, 25
)"
R"(s_cbranch_scc0 .Lskip9
s_add_u32 s64, s37, 1290240
buffer_load_dwordx4 v[100:103], v200, s[8:11], s64 offen sc1
.Lskip9:
s_bitcmp1_b32 s44, 26
s_cbranch_scc0 .Lskip10
s_add_u32 s64, s37, 1433600
buffer_load_dwordx4 v[104:107], v200, s[8:11], s64 offen sc1
.Lskip10:
s_bitcmp1_b32 s44, 27
s_cbranch_scc0 .Lskip11
s_add_u32 s64, s37, 1576960
buffer_load_dwordx4 v[108:111], v200, s[8:11], s64 offen sc1
.Lskip11:
s_bitcmp1_b32 s44, 28
s_cbranch_scc0 .Lskip12
)"
R"(s_add_u32 s64, s37, 1720320
buffer_load_dwordx4 v[112:115], v200, s[8:11], s64 offen sc1
.Lskip12:
s_bitcmp1_b32 s44, 29
s_cbranch_scc0 .Lskip13
s_add_u32 s64, s37, 1863680
buffer_load_dwordx4 v[116:119], v200, s[8:11], s64 offen sc1
.Lskip13:
s_bitcmp1_b32 s44, 30
s_cbranch_scc0 .Lskip14
s_add_u32 s64, s37, 2007040
buffer_load_dwordx4 v[120:123], v200, s[8:11], s64 offen sc1
.Lskip14:
s_bitcmp1_b32 s44, 31
s_cbranch_scc0 .Lskip15
s_add_u32 s64, s37, 2150400
)"
R"(buffer_load_dwordx4 v[124:127], v200, s[8:11], s64 offen sc1
.Lskip15:
s_cmp_eq_u32 s44, s68
s_cbranch_scc1 .Lfast
s_cmp_eq_u32 s43, -1
s_cbranch_scc1 .Llast
s_branch .Lagain
.Lnone:
s_sleep 8
.Lagain:
buffer_load_dword v237, v207, s[72:75], 0 offen sc1
s_add_u32 s70, s70, 1
s_cmp_eq_u32 s70, 2
s_cbranch_scc0 .Lwait
s_call_b64 s[66:67], .LredA
.Lwait:
)"
R"(s_waitcnt vmcnt(0)
s_branch .LroundB
.Lfast:
buffer_store_dword v217, v218, s[72:75], 0 offen sc1
s_waitcnt vmcnt(17)
s_call_b64 s[66:67], .LredA
s_waitcnt vmcnt(17)
v_mov_b32 v192, v242
v_mov_b32 v196, v242
s_nop 1
v_permlane32_swap_b32 v192, v196
s_nop 0
v_mov_b32 v194, v192
v_mov_b32 v198, v196
s_nop 1
v_permlane16_swap_b32 v192, v194
)"
R"(v_permlane16_swap_b32 v196, v198
v_lshlrev_b32 v220, 16, v64
v_and_b32 v221, s68, v64
v_lshlrev_b32 v222, 16, v65
v_and_b32 v223, s68, v65
v_lshlrev_b32 v224, 16, v66
v_and_b32 v225, s68, v66
v_lshlrev_b32 v226, 16, v67
v_and_b32 v227, s68, v67
v_pk_mul_f32 v[208:209], v[220:221], v[192:193] op_sel_hi:[1,0]
v_pk_mul_f32 v[210:211], v[222:223], v[192:193] op_sel_hi:[1,0]
v_pk_mul_f32 v[212:213], v[224:225], v[192:193] op_sel_hi:[1,0]
v_pk_mul_f32 v[214:215], v[226:227], v[192:193] op_sel_hi:[1,0]
s_waitcnt vmcnt(16)
v_lshlrev_b32 v220, 16, v68
v_and_b32 v221, s68, v68
)"
R"(v_lshlrev_b32 v222, 16, v69
v_and_b32 v223, s68, v69
v_lshlrev_b32 v224, 16, v70
v_and_b32 v225, s68, v70
v_lshlrev_b32 v226, 16, v71
v_and_b32 v227, s68, v71
v_pk_fma_f32 v[208:209], v[220:221], v[194:195], v[208:209] op_sel_hi:[1,0,1]
v_pk_fma_f32 v[210:211], v[222:223], v[194:195], v[210:211] op_sel_hi:[1,0,1]
v_pk_fma_f32 v[212:213], v[224:225], v[194:195], v[212:213] op_sel_hi:[1,0,1]
v_pk_fma_f32 v[214:215], v[226:227], v[194:195], v[214:215] op_sel_hi:[1,0,1]
s_waitcnt vmcnt(15)
v_lshlrev_b32 v220, 16, v72
v_and_b32 v221, s68, v72
v_lshlrev_b32 v222, 16, v73
v_and_b32 v223, s68, v73
v_lshlrev_b32 v224, 16, v74
)"
R"(v_and_b32 v225, s68, v74
v_lshlrev_b32 v226, 16, v75
v_and_b32 v227, s68, v75
v_pk_fma_f32 v[208:209], v[220:221], v[196:197], v[208:209] op_sel_hi:[1,0,1]
v_pk_fma_f32 v[210:211], v[222:223], v[196:197], v[210:211] op_sel_hi:[1,0,1]
v_pk_fma_f32 v[212:213], v[224:225], v[196:197], v[212:213] op_sel_hi:[1,0,1]
v_pk_fma_f32 v[214:215], v[226:227], v[196:197], v[214:215] op_sel_hi:[1,0,1]
s_waitcnt vmcnt(14)
v_lshlrev_b32 v220, 16, v76
v_and_b32 v221, s68, v76
v_lshlrev_b32 v222, 16, v77
v_and_b32 v223, s68, v77
v_lshlrev_b32 v224, 16, v78
v_and_b32 v225, s68, v78
v_lshlrev_b32 v226, 16, v79
v_and_b32 v227, s68, v79
)"
R"(v_pk_fma_f32 v[208:209], v[220:221], v[198:199], v[208:209] op_sel_hi:[1,0,1]
v_pk_fma_f32 v[210:211], v[222:223], v[198:199], v[210:211] op_sel_hi:[1,0,1]
v_pk_fma_f32 v[212:213], v[224:225], v[198:199], v[212:213] op_sel_hi:[1,0,1]
v_pk_fma_f32 v[214:215], v[226:227], v[198:199], v[214:215] op_sel_hi:[1,0,1]
s_waitcnt vmcnt(13)
v_mov_b32 v192, v243
v_mov_b32 v196, v243
s_nop 1
v_permlane32_swap_b32 v192, v196
s_nop 0
v_mov_b32 v194, v192
v_mov_b32 v198, v196
s_nop 1
v_permlane16_swap_b32 v192, v194
v_permlane16_swap_b32 v196, v198
v_lshlrev_b32 v220, 16, v80
)"
R"(v_and_b32 v221, s68, v80
v_lshlrev_b32 v222, 16, v81
v_and_b32 v223, s68, v81
v_lshlrev_b32 v224, 16, v82
v_and_b32 v225, s68, v82
v_lshlrev_b32 v226, 16, v83
v_and_b32 v227, s68, v83
v_pk_fma_f32 v[208:209], v[220:221], v[192:193], v[208:209] op_sel_hi:[1,0,1]
v_pk_fma_f32 v[210:211], v[222:223], v[192:193], v[210:211] op_sel_hi:[1,0,1]
v_pk_fma_f32 v[212:213], v[224:225], v[192:193], v[212:213] op_sel_hi:[1,0,1]
v_pk_fma_f32 v[214:215], v[226:227], v[192:193], v[214:215] op_sel_hi:[1,0,1]
s_waitcnt vmcnt(12)
v_lshlrev_b32 v220, 16, v84
v_and_b32 v221, s68, v84
v_lshlrev_b32 v222, 16, v85
v_and_b32 v223, s68, v85
)"
R"(v_lshlrev_b32 v224, 16, v86
v_and_b32 v225, s68, v86
v_lshlrev_b32 v226, 16, v87
v_and_b32 v227, s68, v87
v_pk_fma_f32 v[208:209], v[220:221], v[194:195], v[208:209] op_sel_hi:[1,0,1]
v_pk_fma_f32 v[210:211], v[222:223], v[194:195], v[210:211] op_sel_hi:[1,0,1]
v_pk_fma_f32 v[212:213], v[224:225], v[194:195], v[212:213] op_sel_hi:[1,0,1]
v_pk_fma_f32 v[214:215], v[226:227], v[194:195], v[214:215] op_sel_hi:[1,0,1]
s_waitcnt vmcnt(11)
v_lshlrev_b32 v220, 16, v88
v_and_b32 v221, s68, v88
v_lshlrev_b32 v222, 16, v89
v_and_b32 v223, s68, v89
v_lshlrev_b32 v224, 16, v90
v_and_b32 v225, s68, v90
v_lshlrev_b32 v226, 16, v91
)"
R"(v_and_b32 v227, s68, v91
v_pk_fma_f32 v[208:209], v[220:221], v[196:197], v[208:209] op_sel_hi:[1,0,1]
v_pk_fma_f32 v[210:211], v[222:223], v[196:197], v[210:211] op_sel_hi:[1,0,1]
v_pk_fma_f32 v[212:213], v[224:225], v[196:197], v[212:213] op_sel_hi:[1,0,1]
v_pk_fma_f32 v[214:215], v[226:227], v[196:197], v[214:215] op_sel_hi:[1,0,1]
s_waitcnt vmcnt(10)
v_lshlrev_b32 v220, 16, v92
v_and_b32 v221, s68, v92
v_lshlrev_b32 v222, 16, v93
v_and_b32 v223, s68, v93
v_lshlrev_b32 v224, 16, v94
v_and_b32 v225, s68, v94
v_lshlrev_b32 v226, 16, v95
v_and_b32 v227, s68, v95
v_pk_fma_f32 v[208:209], v[220:221], v[198:199], v[208:209] op_sel_hi:[1,0,1]
v_pk_fma_f32 v[210:211], v[222:223], v[198:199], v[210:211] op_sel_hi:[1,0,1]
)"
R"(v_pk_fma_f32 v[212:213], v[224:225], v[198:199], v[212:213] op_sel_hi:[1,0,1]
v_pk_fma_f32 v[214:215], v[226:227], v[198:199], v[214:215] op_sel_hi:[1,0,1]
s_waitcnt vmcnt(9)
v_mov_b32 v192, v244
v_mov_b32 v196, v244
s_nop 1
v_permlane32_swap_b32 v192, v196
s_nop 0
v_mov_b32 v194, v192
v_mov_b32 v198, v196
s_nop 1
v_permlane16_swap_b32 v192, v194
v_permlane16_swap_b32 v196, v198
v_lshlrev_b32 v220, 16, v96
v_and_b32 v221, s68, v96
v_lshlrev_b32 v222, 16, v97
)"
R"(v_and_b32 v223, s68, v97
v_lshlrev_b32 v224, 16, v98
v_and_b32 v225, s68, v98
v_lshlrev_b32 v226, 16, v99
v_and_b32 v227, s68, v99
v_pk_fma_f32 v[208:209], v[220:221], v[192:193], v[208:209] op_sel_hi:[1,0,1]
v_pk_fma_f32 v[210:211], v[222:223], v[192:193], v[210:211] op_sel_hi:[1,0,1]
v_pk_fma_f32 v[212:213], v[224:225], v[192:193], v[212:213] op_sel_hi:[1,0,1]
v_pk_fma_f32 v[214:215], v[226:227], v[192:193], v[214:215] op_sel_hi:[1,0,1]
s_waitcnt vmcnt(8)
v_lshlrev_b32 v220, 16, v100
v_and_b32 v221, s68, v100
v_lshlrev_b32 v222, 16, v101
v_and_b32 v223, s68, v101
v_lshlrev_b32 v224, 16, v102
v_and_b32 v225, s68, v102
)"
R"(v_lshlrev_b32 v226, 16, v103
v_and_b32 v227, s68, v103
v_pk_fma_f32 v[208:209], v[220:221], v[194:195], v[208:209] op_sel_hi:[1,0,1]
v_pk_fma_f32 v[210:211], v[222:223], v[194:195], v[210:211] op_sel_hi:[1,0,1]
v_pk_fma_f32 v[212:213], v[224:225], v[194:195], v[212:213] op_sel_hi:[1,0,1]
v_pk_fma_f32 v[214:215], v[226:227], v[194:195], v[214:215] op_sel_hi:[1,0,1]
s_waitcnt vmcnt(7)
v_lshlrev_b32 v220, 16, v104
v_and_b32 v221, s68, v104
v_lshlrev_b32 v222, 16, v105
v_and_b32 v223, s68, v105
v_lshlrev_b32 v224, 16, v106
v_and_b32 v225, s68, v106
v_lshlrev_b32 v226, 16, v107
v_and_b32 v227, s68, v107
v_pk_fma_f32 v[208:209], v[220:221], v[196:197], v[208:209] op_sel_hi:[1,0,1]
)"
R"(v_pk_fma_f32 v[210:211], v[222:223], v[196:197], v[210:211] op_sel_hi:[1,0,1]
v_pk_fma_f32 v[212:213], v[224:225], v[196:197], v[212:213] op_sel_hi:[1,0,1]
v_pk_fma_f32 v[214:215], v[226:227], v[196:197], v[214:215] op_sel_hi:[1,0,1]
s_waitcnt vmcnt(6)
v_lshlrev_b32 v220, 16, v108
v_and_b32 v221, s68, v108
v_lshlrev_b32 v222, 16, v109
v_and_b32 v223, s68, v109
v_lshlrev_b32 v224, 16, v110
v_and_b32 v225, s68, v110
v_lshlrev_b32 v226, 16, v111
v_and_b32 v227, s68, v111
v_pk_fma_f32 v[208:209], v[220:221], v[198:199], v[208:209] op_sel_hi:[1,0,1]
v_pk_fma_f32 v[210:211], v[222:223], v[198:199], v[210:211] op_sel_hi:[1,0,1]
v_pk_fma_f32 v[212:213], v[224:225], v[198:199], v[212:213] op_sel_hi:[1,0,1]
v_pk_fma_f32 v[214:215], v[226:227], v[198:199], v[214:215] op_sel_hi:[1,0,1]
)"
R"(s_waitcnt vmcnt(5)
v_mov_b32 v192, v245
v_mov_b32 v196, v245
s_nop 1
v_permlane32_swap_b32 v192, v196
s_nop 0
v_mov_b32 v194, v192
v_mov_b32 v198, v196
s_nop 1
v_permlane16_swap_b32 v192, v194
v_permlane16_swap_b32 v196, v198
v_lshlrev_b32 v220, 16, v112
v_and_b32 v221, s68, v112
v_lshlrev_b32 v222, 16, v113
v_and_b32 v223, s68, v113
v_lshlrev_b32 v224, 16, v114
)"
R"(v_and_b32 v225, s68, v114
v_lshlrev_b32 v226, 16, v115
v_and_b32 v227, s68, v115
v_pk_fma_f32 v[208:209], v[220:221], v[192:193], v[208:209] op_sel_hi:[1,0,1]
v_pk_fma_f32 v[210:211], v[222:223], v[192:193], v[210:211] op_sel_hi:[1,0,1]
v_pk_fma_f32 v[212:213], v[224:225], v[192:193], v[212:213] op_sel_hi:[1,0,1]
v_pk_fma_f32 v[214:215], v[226:227], v[192:193], v[214:215] op_sel_hi:[1,0,1]
s_waitcnt vmcnt(4)
v_lshlrev_b32 v220, 16, v116
v_and_b32 v221, s68, v116
v_lshlrev_b32 v222, 16, v117
v_and_b32 v223, s68, v117
v_lshlrev_b32 v224, 16, v118
v_and_b32 v225, s68, v118
v_lshlrev_b32 v226, 16, v119
v_and_b32 v227, s68, v119
)"
R"(v_pk_fma_f32 v[208:209], v[220:221], v[194:195], v[208:209] op_sel_hi:[1,0,1]
v_pk_fma_f32 v[210:211], v[222:223], v[194:195], v[210:211] op_sel_hi:[1,0,1]
v_pk_fma_f32 v[212:213], v[224:225], v[194:195], v[212:213] op_sel_hi:[1,0,1]
v_pk_fma_f32 v[214:215], v[226:227], v[194:195], v[214:215] op_sel_hi:[1,0,1]
s_waitcnt vmcnt(3)
v_lshlrev_b32 v220, 16, v120
v_and_b32 v221, s68, v120
v_lshlrev_b32 v222, 16, v121
v_and_b32 v223, s68, v121
v_lshlrev_b32 v224, 16, v122
v_and_b32 v225, s68, v122
v_lshlrev_b32 v226, 16, v123
v_and_b32 v227, s68, v123
v_pk_fma_f32 v[208:209], v[220:221], v[196:197], v[208:209] op_sel_hi:[1,0,1]
v_pk_fma_f32 v[210:211], v[222:223], v[196:197], v[210:211] op_sel_hi:[1,0,1]
v_pk_fma_f32 v[212:213], v[224:225], v[196:197], v[212:213] op_sel_hi:[1,0,1]
)"
R"(v_pk_fma_f32 v[214:215], v[226:227], v[196:197], v[214:215] op_sel_hi:[1,0,1]
s_waitcnt vmcnt(2)
v_lshlrev_b32 v220, 16, v124
v_and_b32 v221, s68, v124
v_lshlrev_b32 v222, 16, v125
v_and_b32 v223, s68, v125
v_lshlrev_b32 v224, 16, v126
v_and_b32 v225, s68, v126
v_lshlrev_b32 v226, 16, v127
v_and_b32 v227, s68, v127
v_pk_fma_f32 v[208:209], v[220:221], v[198:199], v[208:209] op_sel_hi:[1,0,1]
v_pk_fma_f32 v[210:211], v[222:223], v[198:199], v[210:211] op_sel_hi:[1,0,1]
v_pk_fma_f32 v[212:213], v[224:225], v[198:199], v[212:213] op_sel_hi:[1,0,1]
v_pk_fma_f32 v[214:215], v[226:227], v[198:199], v[214:215] op_sel_hi:[1,0,1]
C 64,208,209
C 65,210,211
)"
R"(C 66,212,213
C 67,214,215
s_nop 1
v_permlane16_swap_b32 v64, v66
v_permlane16_swap_b32 v65, v67
buffer_store_dwordx4 v[64:67], v201, s[12:15], s38 offen sc1
s_branch .Ldone
.Llast:
buffer_store_dword v217, v218, s[72:75], 0 offen sc1
s_cmp_ge_u32 s70, 2
s_cbranch_scc1 .Lfin
s_call_b64 s[66:67], .LredA
.Lfin:
s_waitcnt vmcnt(0)
v_mov_b32 v192, v242
v_mov_b32 v196, v242
)"
R"(s_nop 1
v_permlane32_swap_b32 v192, v196
s_nop 0
v_mov_b32 v194, v192
v_mov_b32 v198, v196
s_nop 1
v_permlane16_swap_b32 v192, v194
v_permlane16_swap_b32 v196, v198
v_lshlrev_b32 v220, 16, v64
v_and_b32 v221, s68, v64
v_lshlrev_b32 v222, 16, v65
v_and_b32 v223, s68, v65
v_lshlrev_b32 v224, 16, v66
v_and_b32 v225, s68, v66
v_lshlrev_b32 v226, 16, v67
v_and_b32 v227, s68, v67
)"
R"(v_pk_mul_f32 v[208:209], v[220:221], v[192:193] op_sel_hi:[1,0]
v_pk_mul_f32 v[210:211], v[222:223], v[192:193] op_sel_hi:[1,0]
v_pk_mul_f32 v[212:213], v[224:225], v[192:193] op_sel_hi:[1,0]
v_pk_mul_f32 v[214:215], v[226:227], v[192:193] op_sel_hi:[1,0]
v_lshlrev_b32 v220, 16, v68
v_and_b32 v221, s68, v68
v_lshlrev_b32 v222, 16, v69
v_and_b32 v223, s68, v69
v_lshlrev_b32 v224, 16, v70
v_and_b32 v225, s68, v70
v_lshlrev_b32 v226, 16, v71
v_and_b32 v227, s68, v71
v_pk_fma_f32 v[208:209], v[220:221], v[194:195], v[208:209] op_sel_hi:[1,0,1]
v_pk_fma_f32 v[210:211], v[222:223], v[194:195], v[210:211] op_sel_hi:[1,0,1]
v_pk_fma_f32 v[212:213], v[224:225], v[194:195], v[212:213] op_sel_hi:[1,0,1]
v_pk_fma_f32 v[214:215], v[226:227], v[194:195], v[214:215] op_sel_hi:[1,0,1]
)"
R"(v_lshlrev_b32 v220, 16, v72
v_and_b32 v221, s68, v72
v_lshlrev_b32 v222, 16, v73
v_and_b32 v223, s68, v73
v_lshlrev_b32 v224, 16, v74
v_and_b32 v225, s68, v74
v_lshlrev_b32 v226, 16, v75
v_and_b32 v227, s68, v75
v_pk_fma_f32 v[208:209], v[220:221], v[196:197], v[208:209] op_sel_hi:[1,0,1]
v_pk_fma_f32 v[210:211], v[222:223], v[196:197], v[210:211] op_sel_hi:[1,0,1]
v_pk_fma_f32 v[212:213], v[224:225], v[196:197], v[212:213] op_sel_hi:[1,0,1]
v_pk_fma_f32 v[214:215], v[226:227], v[196:197], v[214:215] op_sel_hi:[1,0,1]
v_lshlrev_b32 v220, 16, v76
v_and_b32 v221, s68, v76
v_lshlrev_b32 v222, 16, v77
v_and_b32 v223, s68, v77
)"
R"(v_lshlrev_b32 v224, 16, v78
v_and_b32 v225, s68, v78
v_lshlrev_b32 v226, 16, v79
v_and_b32 v227, s68, v79
v_pk_fma_f32 v[208:209], v[220:221], v[198:199], v[208:209] op_sel_hi:[1,0,1]
v_pk_fma_f32 v[210:211], v[222:223], v[198:199], v[210:211] op_sel_hi:[1,0,1]
v_pk_fma_f32 v[212:213], v[224:225], v[198:199], v[212:213] op_sel_hi:[1,0,1]
v_pk_fma_f32 v[214:215], v[226:227], v[198:199], v[214:215] op_sel_hi:[1,0,1]
v_mov_b32 v192, v243
v_mov_b32 v196, v243
s_nop 1
v_permlane32_swap_b32 v192, v196
s_nop 0
v_mov_b32 v194, v192
v_mov_b32 v198, v196
s_nop 1
)"
R"(v_permlane16_swap_b32 v192, v194
v_permlane16_swap_b32 v196, v198
v_lshlrev_b32 v220, 16, v80
v_and_b32 v221, s68, v80
v_lshlrev_b32 v222, 16, v81
v_and_b32 v223, s68, v81
v_lshlrev_b32 v224, 16, v82
v_and_b32 v225, s68, v82
v_lshlrev_b32 v226, 16, v83
v_and_b32 v227, s68, v83
v_pk_fma_f32 v[208:209], v[220:221], v[192:193], v[208:209] op_sel_hi:[1,0,1]
v_pk_fma_f32 v[210:211], v[222:223], v[192:193], v[210:211] op_sel_hi:[1,0,1]
v_pk_fma_f32 v[212:213], v[224:225], v[192:193], v[212:213] op_sel_hi:[1,0,1]
v_pk_fma_f32 v[214:215], v[226:227], v[192:193], v[214:215] op_sel_hi:[1,0,1]
v_lshlrev_b32 v220, 16, v84
v_and_b32 v221, s68, v84
)"
R"(v_lshlrev_b32 v222, 16, v85
v_and_b32 v223, s68, v85
v_lshlrev_b32 v224, 16, v86
v_and_b32 v225, s68, v86
v_lshlrev_b32 v226, 16, v87
v_and_b32 v227, s68, v87
v_pk_fma_f32 v[208:209], v[220:221], v[194:195], v[208:209] op_sel_hi:[1,0,1]
v_pk_fma_f32 v[210:211], v[222:223], v[194:195], v[210:211] op_sel_hi:[1,0,1]
v_pk_fma_f32 v[212:213], v[224:225], v[194:195], v[212:213] op_sel_hi:[1,0,1]
v_pk_fma_f32 v[214:215], v[226:227], v[194:195], v[214:215] op_sel_hi:[1,0,1]
v_lshlrev_b32 v220, 16, v88
v_and_b32 v221, s68, v88
v_lshlrev_b32 v222, 16, v89
v_and_b32 v223, s68, v89
v_lshlrev_b32 v224, 16, v90
v_and_b32 v225, s68, v90
)"
R"(v_lshlrev_b32 v226, 16, v91
v_and_b32 v227, s68, v91
v_pk_fma_f32 v[208:209], v[220:221], v[196:197], v[208:209] op_sel_hi:[1,0,1]
v_pk_fma_f32 v[210:211], v[222:223], v[196:197], v[210:211] op_sel_hi:[1,0,1]
v_pk_fma_f32 v[212:213], v[224:225], v[196:197], v[212:213] op_sel_hi:[1,0,1]
v_pk_fma_f32 v[214:215], v[226:227], v[196:197], v[214:215] op_sel_hi:[1,0,1]
v_lshlrev_b32 v220, 16, v92
v_and_b32 v221, s68, v92
v_lshlrev_b32 v222, 16, v93
v_and_b32 v223, s68, v93
v_lshlrev_b32 v224, 16, v94
v_and_b32 v225, s68, v94
v_lshlrev_b32 v226, 16, v95
v_and_b32 v227, s68, v95
v_pk_fma_f32 v[208:209], v[220:221], v[198:199], v[208:209] op_sel_hi:[1,0,1]
v_pk_fma_f32 v[210:211], v[222:223], v[198:199], v[210:211] op_sel_hi:[1,0,1]
)"
R"(v_pk_fma_f32 v[212:213], v[224:225], v[198:199], v[212:213] op_sel_hi:[1,0,1]
v_pk_fma_f32 v[214:215], v[226:227], v[198:199], v[214:215] op_sel_hi:[1,0,1]
v_mov_b32 v192, v244
v_mov_b32 v196, v244
s_nop 1
v_permlane32_swap_b32 v192, v196
s_nop 0
v_mov_b32 v194, v192
v_mov_b32 v198, v196
s_nop 1
v_permlane16_swap_b32 v192, v194
v_permlane16_swap_b32 v196, v198
v_lshlrev_b32 v220, 16, v96
v_and_b32 v221, s68, v96
v_lshlrev_b32 v222, 16, v97
v_and_b32 v223, s68, v97
)"
R"(v_lshlrev_b32 v224, 16, v98
v_and_b32 v225, s68, v98
v_lshlrev_b32 v226, 16, v99
v_and_b32 v227, s68, v99
v_pk_fma_f32 v[208:209], v[220:221], v[192:193], v[208:209] op_sel_hi:[1,0,1]
v_pk_fma_f32 v[210:211], v[222:223], v[192:193], v[210:211] op_sel_hi:[1,0,1]
v_pk_fma_f32 v[212:213], v[224:225], v[192:193], v[212:213] op_sel_hi:[1,0,1]
v_pk_fma_f32 v[214:215], v[226:227], v[192:193], v[214:215] op_sel_hi:[1,0,1]
v_lshlrev_b32 v220, 16, v100
v_and_b32 v221, s68, v100
v_lshlrev_b32 v222, 16, v101
v_and_b32 v223, s68, v101
v_lshlrev_b32 v224, 16, v102
v_and_b32 v225, s68, v102
v_lshlrev_b32 v226, 16, v103
v_and_b32 v227, s68, v103
)"
R"(v_pk_fma_f32 v[208:209], v[220:221], v[194:195], v[208:209] op_sel_hi:[1,0,1]
v_pk_fma_f32 v[210:211], v[222:223], v[194:195], v[210:211] op_sel_hi:[1,0,1]
v_pk_fma_f32 v[212:213], v[224:225], v[194:195], v[212:213] op_sel_hi:[1,0,1]
v_pk_fma_f32 v[214:215], v[226:227], v[194:195], v[214:215] op_sel_hi:[1,0,1]
v_lshlrev_b32 v220, 16, v104
v_and_b32 v221, s68, v104
v_lshlrev_b32 v222, 16, v105
v_and_b32 v223, s68, v105
v_lshlrev_b32 v224, 16, v106
v_and_b32 v225, s68, v106
v_lshlrev_b32 v226, 16, v107
v_and_b32 v227, s68, v107
v_pk_fma_f32 v[208:209], v[220:221], v[196:197], v[208:209] op_sel_hi:[1,0,1]
v_pk_fma_f32 v[210:211], v[222:223], v[196:197], v[210:211] op_sel_hi:[1,0,1]
v_pk_fma_f32 v[212:213], v[224:225], v[196:197], v[212:213] op_sel_hi:[1,0,1]
v_pk_fma_f32 v[214:215], v[226:227], v[196:197], v[214:215] op_sel_hi:[1,0,1]
)"
R"(v_lshlrev_b32 v220, 16, v108
v_and_b32 v221, s68, v108
v_lshlrev_b32 v222, 16, v109
v_and_b32 v223, s68, v109
v_lshlrev_b32 v224, 16, v110
v_and_b32 v225, s68, v110
v_lshlrev_b32 v226, 16, v111
v_and_b32 v227, s68, v111
v_pk_fma_f32 v[208:209], v[220:221], v[198:199], v[208:209] op_sel_hi:[1,0,1]
v_pk_fma_f32 v[210:211], v[222:223], v[198:199], v[210:211] op_sel_hi:[1,0,1]
v_pk_fma_f32 v[212:213], v[224:225], v[198:199], v[212:213] op_sel_hi:[1,0,1]
v_pk_fma_f32 v[214:215], v[226:227], v[198:199], v[214:215] op_sel_hi:[1,0,1]
v_mov_b32 v192, v245
v_mov_b32 v196, v245
s_nop 1
v_permlane32_swap_b32 v192, v196
)"
R"(s_nop 0
v_mov_b32 v194, v192
v_mov_b32 v198, v196
s_nop 1
v_permlane16_swap_b32 v192, v194
v_permlane16_swap_b32 v196, v198
v_lshlrev_b32 v220, 16, v112
v_and_b32 v221, s68, v112
v_lshlrev_b32 v222, 16, v113
v_and_b32 v223, s68, v113
v_lshlrev_b32 v224, 16, v114
v_and_b32 v225, s68, v114
v_lshlrev_b32 v226, 16, v115
v_and_b32 v227, s68, v115
v_pk_fma_f32 v[208:209], v[220:221], v[192:193], v[208:209] op_sel_hi:[1,0,1]
v_pk_fma_f32 v[210:211], v[222:223], v[192:193], v[210:211] op_sel_hi:[1,0,1]
)"
R"(v_pk_fma_f32 v[212:213], v[224:225], v[192:193], v[212:213] op_sel_hi:[1,0,1]
v_pk_fma_f32 v[214:215], v[226:227], v[192:193], v[214:215] op_sel_hi:[1,0,1]
v_lshlrev_b32 v220, 16, v116
v_and_b32 v221, s68, v116
v_lshlrev_b32 v222, 16, v117
v_and_b32 v223, s68, v117
v_lshlrev_b32 v224, 16, v118
v_and_b32 v225, s68, v118
v_lshlrev_b32 v226, 16, v119
v_and_b32 v227, s68, v119
v_pk_fma_f32 v[208:209], v[220:221], v[194:195], v[208:209] op_sel_hi:[1,0,1]
v_pk_fma_f32 v[210:211], v[222:223], v[194:195], v[210:211] op_sel_hi:[1,0,1]
v_pk_fma_f32 v[212:213], v[224:225], v[194:195], v[212:213] op_sel_hi:[1,0,1]
v_pk_fma_f32 v[214:215], v[226:227], v[194:195], v[214:215] op_sel_hi:[1,0,1]
v_lshlrev_b32 v220, 16, v120
v_and_b32 v221, s68, v120
)"
R"(v_lshlrev_b32 v222, 16, v121
v_and_b32 v223, s68, v121
v_lshlrev_b32 v224, 16, v122
v_and_b32 v225, s68, v122
v_lshlrev_b32 v226, 16, v123
v_and_b32 v227, s68, v123
v_pk_fma_f32 v[208:209], v[220:221], v[196:197], v[208:209] op_sel_hi:[1,0,1]
v_pk_fma_f32 v[210:211], v[222:223], v[196:197], v[210:211] op_sel_hi:[1,0,1]
v_pk_fma_f32 v[212:213], v[224:225], v[196:197], v[212:213] op_sel_hi:[1,0,1]
v_pk_fma_f32 v[214:215], v[226:227], v[196:197], v[214:215] op_sel_hi:[1,0,1]
v_lshlrev_b32 v220, 16, v124
v_and_b32 v221, s68, v124
v_lshlrev_b32 v222, 16, v125
v_and_b32 v223, s68, v125
v_lshlrev_b32 v224, 16, v126
v_and_b32 v225, s68, v126
)"
R"(v_lshlrev_b32 v226, 16, v127
v_and_b32 v227, s68, v127
v_pk_fma_f32 v[208:209], v[220:221], v[198:199], v[208:209] op_sel_hi:[1,0,1]
v_pk_fma_f32 v[210:211], v[222:223], v[198:199], v[210:211] op_sel_hi:[1,0,1]
v_pk_fma_f32 v[212:213], v[224:225], v[198:199], v[212:213] op_sel_hi:[1,0,1]
v_pk_fma_f32 v[214:215], v[226:227], v[198:199], v[214:215] op_sel_hi:[1,0,1]
C 64,208,209
C 65,210,211
C 66,212,213
C 67,214,215
s_nop 1
v_permlane16_swap_b32 v64, v66
v_permlane16_swap_b32 v65, v67
buffer_store_dwordx4 v[64:67], v201, s[12:15], s38 offen sc1
s_branch .Ldone
.LredA:
)"
R"(v_mov_b32 v192, v238
v_mov_b32 v196, v238
s_nop 1
v_permlane32_swap_b32 v192, v196
s_nop 0
v_mov_b32 v194, v192
v_mov_b32 v198, v196
s_nop 1
v_permlane16_swap_b32 v192, v194
v_permlane16_swap_b32 v196, v198
v_lshlrev_b32 v220, 16, v0
v_and_b32 v221, s68, v0
v_lshlrev_b32 v222, 16, v1
v_and_b32 v223, s68, v1
v_lshlrev_b32 v224, 16, v2
v_and_b32 v225, s68, v2
)"
R"(v_lshlrev_b32 v226, 16, v3
v_and_b32 v227, s68, v3
v_pk_mul_f32 v[208:209], v[220:221], v[192:193] op_sel_hi:[1,0]
v_pk_mul_f32 v[210:211], v[222:223], v[192:193] op_sel_hi:[1,0]
v_pk_mul_f32 v[212:213], v[224:225], v[192:193] op_sel_hi:[1,0]
v_pk_mul_f32 v[214:215], v[226:227], v[192:193] op_sel_hi:[1,0]
v_lshlrev_b32 v220, 16, v4
v_and_b32 v221, s68, v4
v_lshlrev_b32 v222, 16, v5
v_and_b32 v223, s68, v5
v_lshlrev_b32 v224, 16, v6
v_and_b32 v225, s68, v6
v_lshlrev_b32 v226, 16, v7
v_and_b32 v227, s68, v7
v_pk_fma_f32 v[208:209], v[220:221], v[194:195], v[208:209] op_sel_hi:[1,0,1]
v_pk_fma_f32 v[210:211], v[222:223], v[194:195], v[210:211] op_sel_hi:[1,0,1]
)"
R"(v_pk_fma_f32 v[212:213], v[224:225], v[194:195], v[212:213] op_sel_hi:[1,0,1]
v_pk_fma_f32 v[214:215], v[226:227], v[194:195], v[214:215] op_sel_hi:[1,0,1]
v_lshlrev_b32 v220, 16, v8
v_and_b32 v221, s68, v8
v_lshlrev_b32 v222, 16, v9
v_and_b32 v223, s68, v9
v_lshlrev_b32 v224, 16, v10
v_and_b32 v225, s68, v10
v_lshlrev_b32 v226, 16, v11
v_and_b32 v227, s68, v11
v_pk_fma_f32 v[208:209], v[220:221], v[196:197], v[208:209] op_sel_hi:[1,0,1]
v_pk_fma_f32 v[210:211], v[222:223], v[196:197], v[210:211] op_sel_hi:[1,0,1]
v_pk_fma_f32 v[212:213], v[224:225], v[196:197], v[212:213] op_sel_hi:[1,0,1]
v_pk_fma_f32 v[214:215], v[226:227], v[196:197], v[214:215] op_sel_hi:[1,0,1]
v_lshlrev_b32 v220, 16, v12
v_and_b32 v221, s68, v12
)"
R"(v_lshlrev_b32 v222, 16, v13
v_and_b32 v223, s68, v13
v_lshlrev_b32 v224, 16, v14
v_and_b32 v225, s68, v14
v_lshlrev_b32 v226, 16, v15
v_and_b32 v227, s68, v15
v_pk_fma_f32 v[208:209], v[220:221], v[198:199], v[208:209] op_sel_hi:[1,0,1]
v_pk_fma_f32 v[210:211], v[222:223], v[198:199], v[210:211] op_sel_hi:[1,0,1]
v_pk_fma_f32 v[212:213], v[224:225], v[198:199], v[212:213] op_sel_hi:[1,0,1]
v_pk_fma_f32 v[214:215], v[226:227], v[198:199], v[214:215] op_sel_hi:[1,0,1]
v_mov_b32 v192, v239
v_mov_b32 v196, v239
s_nop 1
v_permlane32_swap_b32 v192, v196
s_nop 0
v_mov_b32 v194, v192
)"
R"(v_mov_b32 v198, v196
s_nop 1
v_permlane16_swap_b32 v192, v194
v_permlane16_swap_b32 v196, v198
v_lshlrev_b32 v220, 16, v16
v_and_b32 v221, s68, v16
v_lshlrev_b32 v222, 16, v17
v_and_b32 v223, s68, v17
v_lshlrev_b32 v224, 16, v18
v_and_b32 v225, s68, v18
v_lshlrev_b32 v226, 16, v19
v_and_b32 v227, s68, v19
v_pk_fma_f32 v[208:209], v[220:221], v[192:193], v[208:209] op_sel_hi:[1,0,1]
v_pk_fma_f32 v[210:211], v[222:223], v[192:193], v[210:211] op_sel_hi:[1,0,1]
v_pk_fma_f32 v[212:213], v[224:225], v[192:193], v[212:213] op_sel_hi:[1,0,1]
v_pk_fma_f32 v[214:215], v[226:227], v[192:193], v[214:215] op_sel_hi:[1,0,1]
)"
R"(v_lshlrev_b32 v220, 16, v20
v_and_b32 v221, s68, v20
v_lshlrev_b32 v222, 16, v21
v_and_b32 v223, s68, v21
v_lshlrev_b32 v224, 16, v22
v_and_b32 v225, s68, v22
v_lshlrev_b32 v226, 16, v23
v_and_b32 v227, s68, v23
v_pk_fma_f32 v[208:209], v[220:221], v[194:195], v[208:209] op_sel_hi:[1,0,1]
v_pk_fma_f32 v[210:211], v[222:223], v[194:195], v[210:211] op_sel_hi:[1,0,1]
v_pk_fma_f32 v[212:213], v[224:225], v[194:195], v[212:213] op_sel_hi:[1,0,1]
v_pk_fma_f32 v[214:215], v[226:227], v[194:195], v[214:215] op_sel_hi:[1,0,1]
v_lshlrev_b32 v220, 16, v24
v_and_b32 v221, s68, v24
v_lshlrev_b32 v222, 16, v25
v_and_b32 v223, s68, v25
)"
R"(v_lshlrev_b32 v224, 16, v26
v_and_b32 v225, s68, v26
v_lshlrev_b32 v226, 16, v27
v_and_b32 v227, s68, v27
v_pk_fma_f32 v[208:209], v[220:221], v[196:197], v[208:209] op_sel_hi:[1,0,1]
v_pk_fma_f32 v[210:211], v[222:223], v[196:197], v[210:211] op_sel_hi:[1,0,1]
v_pk_fma_f32 v[212:213], v[224:225], v[196:197], v[212:213] op_sel_hi:[1,0,1]
v_pk_fma_f32 v[214:215], v[226:227], v[196:197], v[214:215] op_sel_hi:[1,0,1]
v_lshlrev_b32 v220, 16, v28
v_and_b32 v221, s68, v28
v_lshlrev_b32 v222, 16, v29
v_and_b32 v223, s68, v29
v_lshlrev_b32 v224, 16, v30
v_and_b32 v225, s68, v30
v_lshlrev_b32 v226, 16, v31
v_and_b32 v227, s68, v31
)"
R"(v_pk_fma_f32 v[208:209], v[220:221], v[198:199], v[208:209] op_sel_hi:[1,0,1]
v_pk_fma_f32 v[210:211], v[222:223], v[198:199], v[210:211] op_sel_hi:[1,0,1]
v_pk_fma_f32 v[212:213], v[224:225], v[198:199], v[212:213] op_sel_hi:[1,0,1]
v_pk_fma_f32 v[214:215], v[226:227], v[198:199], v[214:215] op_sel_hi:[1,0,1]
v_mov_b32 v192, v240
v_mov_b32 v196, v240
s_nop 1
v_permlane32_swap_b32 v192, v196
s_nop 0
v_mov_b32 v194, v192
v_mov_b32 v198, v196
s_nop 1
v_permlane16_swap_b32 v192, v194
v_permlane16_swap_b32 v196, v198
v_lshlrev_b32 v220, 16, v32
v_and_b32 v221, s68, v32
)"
R"(v_lshlrev_b32 v222, 16, v33
v_and_b32 v223, s68, v33
v_lshlrev_b32 v224, 16, v34
v_and_b32 v225, s68, v34
v_lshlrev_b32 v226, 16, v35
v_and_b32 v227, s68, v35
v_pk_fma_f32 v[208:209], v[220:221], v[192:193], v[208:209] op_sel_hi:[1,0,1]
v_pk_fma_f32 v[210:211], v[222:223], v[192:193], v[210:211] op_sel_hi:[1,0,1]
v_pk_fma_f32 v[212:213], v[224:225], v[192:193], v[212:213] op_sel_hi:[1,0,1]
v_pk_fma_f32 v[214:215], v[226:227], v[192:193], v[214:215] op_sel_hi:[1,0,1]
v_lshlrev_b32 v220, 16, v36
v_and_b32 v221, s68, v36
v_lshlrev_b32 v222, 16, v37
v_and_b32 v223, s68, v37
v_lshlrev_b32 v224, 16, v38
v_and_b32 v225, s68, v38
)"
R"(v_lshlrev_b32 v226, 16, v39
v_and_b32 v227, s68, v39
v_pk_fma_f32 v[208:209], v[220:221], v[194:195], v[208:209] op_sel_hi:[1,0,1]
v_pk_fma_f32 v[210:211], v[222:223], v[194:195], v[210:211] op_sel_hi:[1,0,1]
v_pk_fma_f32 v[212:213], v[224:225], v[194:195], v[212:213] op_sel_hi:[1,0,1]
v_pk_fma_f32 v[214:215], v[226:227], v[194:195], v[214:215] op_sel_hi:[1,0,1]
v_lshlrev_b32 v220, 16, v40
v_and_b32 v221, s68, v40
v_lshlrev_b32 v222, 16, v41
v_and_b32 v223, s68, v41
v_lshlrev_b32 v224, 16, v42
v_and_b32 v225, s68, v42
v_lshlrev_b32 v226, 16, v43
v_and_b32 v227, s68, v43
v_pk_fma_f32 v[208:209], v[220:221], v[196:197], v[208:209] op_sel_hi:[1,0,1]
v_pk_fma_f32 v[210:211], v[222:223], v[196:197], v[210:211] op_sel_hi:[1,0,1]
)"
R"(v_pk_fma_f32 v[212:213], v[224:225], v[196:197], v[212:213] op_sel_hi:[1,0,1]
v_pk_fma_f32 v[214:215], v[226:227], v[196:197], v[214:215] op_sel_hi:[1,0,1]
v_lshlrev_b32 v220, 16, v44
v_and_b32 v221, s68, v44
v_lshlrev_b32 v222, 16, v45
v_and_b32 v223, s68, v45
v_lshlrev_b32 v224, 16, v46
v_and_b32 v225, s68, v46
v_lshlrev_b32 v226, 16, v47
v_and_b32 v227, s68, v47
v_pk_fma_f32 v[208:209], v[220:221], v[198:199], v[208:209] op_sel_hi:[1,0,1]
v_pk_fma_f32 v[210:211], v[222:223], v[198:199], v[210:211] op_sel_hi:[1,0,1]
v_pk_fma_f32 v[212:213], v[224:225], v[198:199], v[212:213] op_sel_hi:[1,0,1]
v_pk_fma_f32 v[214:215], v[226:227], v[198:199], v[214:215] op_sel_hi:[1,0,1]
v_mov_b32 v192, v241
v_mov_b32 v196, v241
)"
R"(s_nop 1
v_permlane32_swap_b32 v192, v196
s_nop 0
v_mov_b32 v194, v192
v_mov_b32 v198, v196
s_nop 1
v_permlane16_swap_b32 v192, v194
v_permlane16_swap_b32 v196, v198
v_lshlrev_b32 v220, 16, v48
v_and_b32 v221, s68, v48
v_lshlrev_b32 v222, 16, v49
v_and_b32 v223, s68, v49
v_lshlrev_b32 v224, 16, v50
v_and_b32 v225, s68, v50
v_lshlrev_b32 v226, 16, v51
v_and_b32 v227, s68, v51
)"
R"(v_pk_fma_f32 v[208:209], v[220:221], v[192:193], v[208:209] op_sel_hi:[1,0,1]
v_pk_fma_f32 v[210:211], v[222:223], v[192:193], v[210:211] op_sel_hi:[1,0,1]
v_pk_fma_f32 v[212:213], v[224:225], v[192:193], v[212:213] op_sel_hi:[1,0,1]
v_pk_fma_f32 v[214:215], v[226:227], v[192:193], v[214:215] op_sel_hi:[1,0,1]
v_lshlrev_b32 v220, 16, v52
v_and_b32 v221, s68, v52
v_lshlrev_b32 v222, 16, v53
v_and_b32 v223, s68, v53
v_lshlrev_b32 v224, 16, v54
v_and_b32 v225, s68, v54
v_lshlrev_b32 v226, 16, v55
v_and_b32 v227, s68, v55
v_pk_fma_f32 v[208:209], v[220:221], v[194:195], v[208:209] op_sel_hi:[1,0,1]
v_pk_fma_f32 v[210:211], v[222:223], v[194:195], v[210:211] op_sel_hi:[1,0,1]
v_pk_fma_f32 v[212:213], v[224:225], v[194:195], v[212:213] op_sel_hi:[1,0,1]
v_pk_fma_f32 v[214:215], v[226:227], v[194:195], v[214:215] op_sel_hi:[1,0,1]
)"
R"(v_lshlrev_b32 v220, 16, v56
v_and_b32 v221, s68, v56
v_lshlrev_b32 v222, 16, v57
v_and_b32 v223, s68, v57
v_lshlrev_b32 v224, 16, v58
v_and_b32 v225, s68, v58
v_lshlrev_b32 v226, 16, v59
v_and_b32 v227, s68, v59
v_pk_fma_f32 v[208:209], v[220:221], v[196:197], v[208:209] op_sel_hi:[1,0,1]
v_pk_fma_f32 v[210:211], v[222:223], v[196:197], v[210:211] op_sel_hi:[1,0,1]
v_pk_fma_f32 v[212:213], v[224:225], v[196:197], v[212:213] op_sel_hi:[1,0,1]
v_pk_fma_f32 v[214:215], v[226:227], v[196:197], v[214:215] op_sel_hi:[1,0,1]
v_lshlrev_b32 v220, 16, v60
v_and_b32 v221, s68, v60
v_lshlrev_b32 v222, 16, v61
v_and_b32 v223, s68, v61
)"
R"(v_lshlrev_b32 v224, 16, v62
v_and_b32 v225, s68, v62
v_lshlrev_b32 v226, 16, v63
v_and_b32 v227, s68, v63
v_pk_fma_f32 v[208:209], v[220:221], v[198:199], v[208:209] op_sel_hi:[1,0,1]
v_pk_fma_f32 v[210:211], v[222:223], v[198:199], v[210:211] op_sel_hi:[1,0,1]
v_pk_fma_f32 v[212:213], v[224:225], v[198:199], v[212:213] op_sel_hi:[1,0,1]
v_pk_fma_f32 v[214:215], v[226:227], v[198:199], v[214:215] op_sel_hi:[1,0,1]
C 0,208,209
C 1,210,211
C 2,212,213
C 3,214,215
s_nop 1
v_permlane16_swap_b32 v0, v2
v_permlane16_swap_b32 v1, v3
buffer_store_dwordx4 v[0:3], v201, s[12:15], s35 offen sc1
)"
R"(s_cmp_eq_u32 s69, 0
s_cbranch_scc1 .LredAx
v_mov_b32 v192, v246
v_mov_b32 v196, v246
s_nop 1
v_permlane32_swap_b32 v192, v196
s_nop 0
v_mov_b32 v194, v192
v_mov_b32 v198, v196
s_nop 1
v_permlane16_swap_b32 v192, v194
v_permlane16_swap_b32 v196, v198
v_lshlrev_b32 v220, 16, v128
v_and_b32 v221, s68, v128
v_lshlrev_b32 v222, 16, v129
v_and_b32 v223, s68, v129
)"
R"(v_lshlrev_b32 v224, 16, v130
v_and_b32 v225, s68, v130
v_lshlrev_b32 v226, 16, v131
v_and_b32 v227, s68, v131
v_pk_mul_f32 v[208:209], v[220:221], v[192:193] op_sel_hi:[1,0]
v_pk_mul_f32 v[210:211], v[222:223], v[192:193] op_sel_hi:[1,0]
v_pk_mul_f32 v[212:213], v[224:225], v[192:193] op_sel_hi:[1,0]
v_pk_mul_f32 v[214:215], v[226:227], v[192:193] op_sel_hi:[1,0]
v_lshlrev_b32 v220, 16, v132
v_and_b32 v221, s68, v132
v_lshlrev_b32 v222, 16, v133
v_and_b32 v223, s68, v133
v_lshlrev_b32 v224, 16, v134
v_and_b32 v225, s68, v134
v_lshlrev_b32 v226, 16, v135
v_and_b32 v227, s68, v135
)"
R"(v_pk_fma_f32 v[208:209], v[220:221], v[194:195], v[208:209] op_sel_hi:[1,0,1]
v_pk_fma_f32 v[210:211], v[222:223], v[194:195], v[210:211] op_sel_hi:[1,0,1]
v_pk_fma_f32 v[212:213], v[224:225], v[194:195], v[212:213] op_sel_hi:[1,0,1]
v_pk_fma_f32 v[214:215], v[226:227], v[194:195], v[214:215] op_sel_hi:[1,0,1]
v_lshlrev_b32 v220, 16, v136
v_and_b32 v221, s68, v136
v_lshlrev_b32 v222, 16, v137
v_and_b32 v223, s68, v137
v_lshlrev_b32 v224, 16, v138
v_and_b32 v225, s68, v138
v_lshlrev_b32 v226, 16, v139
v_and_b32 v227, s68, v139
v_pk_fma_f32 v[208:209], v[220:221], v[196:197], v[208:209] op_sel_hi:[1,0,1]
v_pk_fma_f32 v[210:211], v[222:223], v[196:197], v[210:211] op_sel_hi:[1,0,1]
v_pk_fma_f32 v[212:213], v[224:225], v[196:197], v[212:213] op_sel_hi:[1,0,1]
v_pk_fma_f32 v[214:215], v[226:227], v[196:197], v[214:215] op_sel_hi:[1,0,1]
)"
R"(v_lshlrev_b32 v220, 16, v140
v_and_b32 v221, s68, v140
v_lshlrev_b32 v222, 16, v141
v_and_b32 v223, s68, v141
v_lshlrev_b32 v224, 16, v142
v_and_b32 v225, s68, v142
v_lshlrev_b32 v226, 16, v143
v_and_b32 v227, s68, v143
v_pk_fma_f32 v[208:209], v[220:221], v[198:199], v[208:209] op_sel_hi:[1,0,1]
v_pk_fma_f32 v[210:211], v[222:223], v[198:199], v[210:211] op_sel_hi:[1,0,1]
v_pk_fma_f32 v[212:213], v[224:225], v[198:199], v[212:213] op_sel_hi:[1,0,1]
v_pk_fma_f32 v[214:215], v[226:227], v[198:199], v[214:215] op_sel_hi:[1,0,1]
v_mov_b32 v192, v247
v_mov_b32 v196, v247
s_nop 1
v_permlane32_swap_b32 v192, v196
)"
R"(s_nop 0
v_mov_b32 v194, v192
v_mov_b32 v198, v196
s_nop 1
v_permlane16_swap_b32 v192, v194
v_permlane16_swap_b32 v196, v198
v_lshlrev_b32 v220, 16, v144
v_and_b32 v221, s68, v144
v_lshlrev_b32 v222, 16, v145
v_and_b32 v223, s68, v145
v_lshlrev_b32 v224, 16, v146
v_and_b32 v225, s68, v146
v_lshlrev_b32 v226, 16, v147
v_and_b32 v227, s68, v147
v_pk_fma_f32 v[208:209], v[220:221], v[192:193], v[208:209] op_sel_hi:[1,0,1]
v_pk_fma_f32 v[210:211], v[222:223], v[192:193], v[210:211] op_sel_hi:[1,0,1]
)"
R"(v_pk_fma_f32 v[212:213], v[224:225], v[192:193], v[212:213] op_sel_hi:[1,0,1]
v_pk_fma_f32 v[214:215], v[226:227], v[192:193], v[214:215] op_sel_hi:[1,0,1]
v_lshlrev_b32 v220, 16, v148
v_and_b32 v221, s68, v148
v_lshlrev_b32 v222, 16, v149
v_and_b32 v223, s68, v149
v_lshlrev_b32 v224, 16, v150
v_and_b32 v225, s68, v150
v_lshlrev_b32 v226, 16, v151
v_and_b32 v227, s68, v151
v_pk_fma_f32 v[208:209], v[220:221], v[194:195], v[208:209] op_sel_hi:[1,0,1]
v_pk_fma_f32 v[210:211], v[222:223], v[194:195], v[210:211] op_sel_hi:[1,0,1]
v_pk_fma_f32 v[212:213], v[224:225], v[194:195], v[212:213] op_sel_hi:[1,0,1]
v_pk_fma_f32 v[214:215], v[226:227], v[194:195], v[214:215] op_sel_hi:[1,0,1]
v_lshlrev_b32 v220, 16, v152
v_and_b32 v221, s68, v152
)"
R"(v_lshlrev_b32 v222, 16, v153
v_and_b32 v223, s68, v153
v_lshlrev_b32 v224, 16, v154
v_and_b32 v225, s68, v154
v_lshlrev_b32 v226, 16, v155
v_and_b32 v227, s68, v155
v_pk_fma_f32 v[208:209], v[220:221], v[196:197], v[208:209] op_sel_hi:[1,0,1]
v_pk_fma_f32 v[210:211], v[222:223], v[196:197], v[210:211] op_sel_hi:[1,0,1]
v_pk_fma_f32 v[212:213], v[224:225], v[196:197], v[212:213] op_sel_hi:[1,0,1]
v_pk_fma_f32 v[214:215], v[226:227], v[196:197], v[214:215] op_sel_hi:[1,0,1]
v_lshlrev_b32 v220, 16, v156
v_and_b32 v221, s68, v156
v_lshlrev_b32 v222, 16, v157
v_and_b32 v223, s68, v157
v_lshlrev_b32 v224, 16, v158
v_and_b32 v225, s68, v158
)"
R"(v_lshlrev_b32 v226, 16, v159
v_and_b32 v227, s68, v159
v_pk_fma_f32 v[208:209], v[220:221], v[198:199], v[208:209] op_sel_hi:[1,0,1]
v_pk_fma_f32 v[210:211], v[222:223], v[198:199], v[210:211] op_sel_hi:[1,0,1]
v_pk_fma_f32 v[212:213], v[224:225], v[198:199], v[212:213] op_sel_hi:[1,0,1]
v_pk_fma_f32 v[214:215], v[226:227], v[198:199], v[214:215] op_sel_hi:[1,0,1]
v_mov_b32 v192, v248
v_mov_b32 v196, v248
s_nop 1
v_permlane32_swap_b32 v192, v196
s_nop 0
v_mov_b32 v194, v192
v_mov_b32 v198, v196
s_nop 1
v_permlane16_swap_b32 v192, v194
v_permlane16_swap_b32 v196, v198
)"
R"(v_lshlrev_b32 v220, 16, v160
v_and_b32 v221, s68, v160
v_lshlrev_b32 v222, 16, v161
v_and_b32 v223, s68, v161
v_lshlrev_b32 v224, 16, v162
v_and_b32 v225, s68, v162
v_lshlrev_b32 v226, 16, v163
v_and_b32 v227, s68, v163
v_pk_fma_f32 v[208:209], v[220:221], v[192:193], v[208:209] op_sel_hi:[1,0,1]
v_pk_fma_f32 v[210:211], v[222:223], v[192:193], v[210:211] op_sel_hi:[1,0,1]
v_pk_fma_f32 v[212:213], v[224:225], v[192:193], v[212:213] op_sel_hi:[1,0,1]
v_pk_fma_f32 v[214:215], v[226:227], v[192:193], v[214:215] op_sel_hi:[1,0,1]
v_lshlrev_b32 v220, 16, v164
v_and_b32 v221, s68, v164
v_lshlrev_b32 v222, 16, v165
v_and_b32 v223, s68, v165
)"
R"(v_lshlrev_b32 v224, 16, v166
v_and_b32 v225, s68, v166
v_lshlrev_b32 v226, 16, v167
v_and_b32 v227, s68, v167
v_pk_fma_f32 v[208:209], v[220:221], v[194:195], v[208:209] op_sel_hi:[1,0,1]
v_pk_fma_f32 v[210:211], v[222:223], v[194:195], v[210:211] op_sel_hi:[1,0,1]
v_pk_fma_f32 v[212:213], v[224:225], v[194:195], v[212:213] op_sel_hi:[1,0,1]
v_pk_fma_f32 v[214:215], v[226:227], v[194:195], v[214:215] op_sel_hi:[1,0,1]
v_lshlrev_b32 v220, 16, v168
v_and_b32 v221, s68, v168
v_lshlrev_b32 v222, 16, v169
v_and_b32 v223, s68, v169
v_lshlrev_b32 v224, 16, v170
v_and_b32 v225, s68, v170
v_lshlrev_b32 v226, 16, v171
v_and_b32 v227, s68, v171
)"
R"(v_pk_fma_f32 v[208:209], v[220:221], v[196:197], v[208:209] op_sel_hi:[1,0,1]
v_pk_fma_f32 v[210:211], v[222:223], v[196:197], v[210:211] op_sel_hi:[1,0,1]
v_pk_fma_f32 v[212:213], v[224:225], v[196:197], v[212:213] op_sel_hi:[1,0,1]
v_pk_fma_f32 v[214:215], v[226:227], v[196:197], v[214:215] op_sel_hi:[1,0,1]
v_lshlrev_b32 v220, 16, v172
v_and_b32 v221, s68, v172
v_lshlrev_b32 v222, 16, v173
v_and_b32 v223, s68, v173
v_lshlrev_b32 v224, 16, v174
v_and_b32 v225, s68, v174
v_lshlrev_b32 v226, 16, v175
v_and_b32 v227, s68, v175
v_pk_fma_f32 v[208:209], v[220:221], v[198:199], v[208:209] op_sel_hi:[1,0,1]
v_pk_fma_f32 v[210:211], v[222:223], v[198:199], v[210:211] op_sel_hi:[1,0,1]
v_pk_fma_f32 v[212:213], v[224:225], v[198:199], v[212:213] op_sel_hi:[1,0,1]
v_pk_fma_f32 v[214:215], v[226:227], v[198:199], v[214:215] op_sel_hi:[1,0,1]
)"
R"(v_mov_b32 v192, v249
v_mov_b32 v196, v249
s_nop 1
v_permlane32_swap_b32 v192, v196
s_nop 0
v_mov_b32 v194, v192
v_mov_b32 v198, v196
s_nop 1
v_permlane16_swap_b32 v192, v194
v_permlane16_swap_b32 v196, v198
v_lshlrev_b32 v220, 16, v176
v_and_b32 v221, s68, v176
v_lshlrev_b32 v222, 16, v177
v_and_b32 v223, s68, v177
v_lshlrev_b32 v224, 16, v178
v_and_b32 v225, s68, v178
)"
R"(v_lshlrev_b32 v226, 16, v179
v_and_b32 v227, s68, v179
v_pk_fma_f32 v[208:209], v[220:221], v[192:193], v[208:209] op_sel_hi:[1,0,1]
v_pk_fma_f32 v[210:211], v[222:223], v[192:193], v[210:211] op_sel_hi:[1,0,1]
v_pk_fma_f32 v[212:213], v[224:225], v[192:193], v[212:213] op_sel_hi:[1,0,1]
v_pk_fma_f32 v[214:215], v[226:227], v[192:193], v[214:215] op_sel_hi:[1,0,1]
v_lshlrev_b32 v220, 16, v180
v_and_b32 v221, s68, v180
v_lshlrev_b32 v222, 16, v181
v_and_b32 v223, s68, v181
v_lshlrev_b32 v224, 16, v182
v_and_b32 v225, s68, v182
v_lshlrev_b32 v226, 16, v183
v_and_b32 v227, s68, v183
v_pk_fma_f32 v[208:209], v[220:221], v[194:195], v[208:209] op_sel_hi:[1,0,1]
v_pk_fma_f32 v[210:211], v[222:223], v[194:195], v[210:211] op_sel_hi:[1,0,1]
)"
R"(v_pk_fma_f32 v[212:213], v[224:225], v[194:195], v[212:213] op_sel_hi:[1,0,1]
v_pk_fma_f32 v[214:215], v[226:227], v[194:195], v[214:215] op_sel_hi:[1,0,1]
v_lshlrev_b32 v220, 16, v184
v_and_b32 v221, s68, v184
v_lshlrev_b32 v222, 16, v185
v_and_b32 v223, s68, v185
v_lshlrev_b32 v224, 16, v186
v_and_b32 v225, s68, v186
v_lshlrev_b32 v226, 16, v187
v_and_b32 v227, s68, v187
v_pk_fma_f32 v[208:209], v[220:221], v[196:197], v[208:209] op_sel_hi:[1,0,1]
v_pk_fma_f32 v[210:211], v[222:223], v[196:197], v[210:211] op_sel_hi:[1,0,1]
v_pk_fma_f32 v[212:213], v[224:225], v[196:197], v[212:213] op_sel_hi:[1,0,1]
v_pk_fma_f32 v[214:215], v[226:227], v[196:197], v[214:215] op_sel_hi:[1,0,1]
v_lshlrev_b32 v220, 16, v188
v_and_b32 v221, s68, v188
)"
R"(v_lshlrev_b32 v222, 16, v189
v_and_b32 v223, s68, v189
v_lshlrev_b32 v224, 16, v190
v_and_b32 v225, s68, v190
v_lshlrev_b32 v226, 16, v191
v_and_b32 v227, s68, v191
v_pk_fma_f32 v[208:209], v[220:221], v[198:199], v[208:209] op_sel_hi:[1,0,1]
v_pk_fma_f32 v[210:211], v[222:223], v[198:199], v[210:211] op_sel_hi:[1,0,1]
v_pk_fma_f32 v[212:213], v[224:225], v[198:199], v[212:213] op_sel_hi:[1,0,1]
v_pk_fma_f32 v[214:215], v[226:227], v[198:199], v[214:215] op_sel_hi:[1,0,1]
C 128,208,209
C 129,210,211
C 130,212,213
C 131,214,215
s_nop 1
v_permlane16_swap_b32 v128, v130
)"
R"(v_permlane16_swap_b32 v129, v131
buffer_store_dwordx4 v[128:131], v201, s[12:15], s41 offen sc1
.LredAx:
s_setpc_b64 s[66:67]
.LrareA2_1:
s_call_b64 s[66:67], .LsubA2
s_branch .LbackA2_1
.LrareA2_2:
s_call_b64 s[66:67], .LsubA2
s_branch .LbackA2_2
.LrareA2_3:
s_call_b64 s[66:67], .LsubA2
s_branch .LbackA2_3
.LrareA2_4:
s_call_b64 s[66:67], .LsubA2
s_branch .LbackA2_4
)"
R"(.LrareA2_5:
s_call_b64 s[66:67], .LsubA2
s_branch .LbackA2_5
.LrareA2_6:
s_call_b64 s[66:67], .LsubA2
s_branch .LbackA2_6
.LrareA2_7:
s_call_b64 s[66:67], .LsubA2
s_branch .LbackA2_7
.LsubA2:
v_mov_b32 v238, v217
v_mov_b32 v239, v218
s_nop 0
v_permlane32_swap_b32 v217, v238
v_permlane32_swap_b32 v218, v239
v_max_f32 v217, v217, v238
)"
R"(v_max_f32 v218, v218, v239
v_mov_b32 v238, v217
v_mov_b32 v239, v218
s_nop 0
v_permlane16_swap_b32 v217, v238
v_permlane16_swap_b32 v218, v239
v_max_f32 v217, v217, v238
v_max_f32 v218, v218, v239
v_max_f32 v208, v208, v217
v_max_f32 v209, v209, v218
v_mul_f32 v217, -s63, v208
v_mul_f32 v218, -s63, v209
v_add_f32 v211, s65, v208
v_add_f32 v212, s65, v209
v_sub_f32 v234, v217, v214
v_sub_f32 v235, v218, v215
)"
R"(v_mov_b32 v214, v217
v_mov_b32 v215, v218
E 234
E 235
s_nop 1
s_setpc_b64 s[66:67]
.LrareB2_1:
s_call_b64 s[66:67], .LsubB2
s_branch .LbackB2_1
.LrareB2_2:
s_call_b64 s[66:67], .LsubB2
s_branch .LbackB2_2
.LrareB2_3:
s_call_b64 s[66:67], .LsubB2
s_branch .LbackB2_3
.LrareB2_4:
)"
R"(s_call_b64 s[66:67], .LsubB2
s_branch .LbackB2_4
.LrareB2_5:
s_call_b64 s[66:67], .LsubB2
s_branch .LbackB2_5
.LrareB2_6:
s_call_b64 s[66:67], .LsubB2
s_branch .LbackB2_6
.LrareB2_7:
s_call_b64 s[66:67], .LsubB2
s_branch .LbackB2_7
.LsubB2:
s_nop 7
R 238,0
R 239,1
R 240,2
)"
R"(R 241,3
v_mul_f32 v238, v238, v234
v_mul_f32 v239, v239, v234
v_mul_f32 v240, v240, v234
v_mul_f32 v241, v241, v234
v_accvgpr_write_b32 a0, v238
v_accvgpr_write_b32 a1, v239
v_accvgpr_write_b32 a2, v240
v_accvgpr_write_b32 a3, v241
R 238,4
R 239,5
R 240,6
R 241,7
v_mul_f32 v238, v238, v235
v_mul_f32 v239, v239, v235
v_mul_f32 v240, v240, v235
)"
R"(v_mul_f32 v241, v241, v235
v_accvgpr_write_b32 a4, v238
v_accvgpr_write_b32 a5, v239
v_accvgpr_write_b32 a6, v240
v_accvgpr_write_b32 a7, v241
R 238,8
R 239,9
R 240,10
R 241,11
v_mul_f32 v238, v238, v234
v_mul_f32 v239, v239, v234
v_mul_f32 v240, v240, v234
v_mul_f32 v241, v241, v234
v_accvgpr_write_b32 a8, v238
v_accvgpr_write_b32 a9, v239
v_accvgpr_write_b32 a10, v240
)"
R"(v_accvgpr_write_b32 a11, v241
R 238,12
R 239,13
R 240,14
R 241,15
v_mul_f32 v238, v238, v235
v_mul_f32 v239, v239, v235
v_mul_f32 v240, v240, v235
v_mul_f32 v241, v241, v235
v_accvgpr_write_b32 a12, v238
v_accvgpr_write_b32 a13, v239
v_accvgpr_write_b32 a14, v240
v_accvgpr_write_b32 a15, v241
R 238,16
R 239,17
R 240,18
)"
R"(R 241,19
v_mul_f32 v238, v238, v234
v_mul_f32 v239, v239, v234
v_mul_f32 v240, v240, v234
v_mul_f32 v241, v241, v234
v_accvgpr_write_b32 a16, v238
v_accvgpr_write_b32 a17, v239
v_accvgpr_write_b32 a18, v240
v_accvgpr_write_b32 a19, v241
R 238,20
R 239,21
R 240,22
R 241,23
v_mul_f32 v238, v238, v235
v_mul_f32 v239, v239, v235
v_mul_f32 v240, v240, v235
)"
R"(v_mul_f32 v241, v241, v235
v_accvgpr_write_b32 a20, v238
v_accvgpr_write_b32 a21, v239
v_accvgpr_write_b32 a22, v240
v_accvgpr_write_b32 a23, v241
R 238,24
R 239,25
R 240,26
R 241,27
v_mul_f32 v238, v238, v234
v_mul_f32 v239, v239, v234
v_mul_f32 v240, v240, v234
v_mul_f32 v241, v241, v234
v_accvgpr_write_b32 a24, v238
v_accvgpr_write_b32 a25, v239
v_accvgpr_write_b32 a26, v240
)"
R"(v_accvgpr_write_b32 a27, v241
R 238,28
R 239,29
R 240,30
R 241,31
v_mul_f32 v238, v238, v235
v_mul_f32 v239, v239, v235
v_mul_f32 v240, v240, v235
v_mul_f32 v241, v241, v235
v_accvgpr_write_b32 a28, v238
v_accvgpr_write_b32 a29, v239
v_accvgpr_write_b32 a30, v240
v_accvgpr_write_b32 a31, v241
R 238,32
R 239,33
R 240,34
)"
R"(R 241,35
v_mul_f32 v238, v238, v234
v_mul_f32 v239, v239, v234
v_mul_f32 v240, v240, v234
v_mul_f32 v241, v241, v234
v_accvgpr_write_b32 a32, v238
v_accvgpr_write_b32 a33, v239
v_accvgpr_write_b32 a34, v240
v_accvgpr_write_b32 a35, v241
R 238,36
R 239,37
R 240,38
R 241,39
v_mul_f32 v238, v238, v235
v_mul_f32 v239, v239, v235
v_mul_f32 v240, v240, v235
)"
R"(v_mul_f32 v241, v241, v235
v_accvgpr_write_b32 a36, v238
v_accvgpr_write_b32 a37, v239
v_accvgpr_write_b32 a38, v240
v_accvgpr_write_b32 a39, v241
R 238,40
R 239,41
R 240,42
R 241,43
v_mul_f32 v238, v238, v234
v_mul_f32 v239, v239, v234
v_mul_f32 v240, v240, v234
v_mul_f32 v241, v241, v234
v_accvgpr_write_b32 a40, v238
v_accvgpr_write_b32 a41, v239
v_accvgpr_write_b32 a42, v240
)"
R"(v_accvgpr_write_b32 a43, v241
R 238,44
R 239,45
R 240,46
R 241,47
v_mul_f32 v238, v238, v235
v_mul_f32 v239, v239, v235
v_mul_f32 v240, v240, v235
v_mul_f32 v241, v241, v235
v_accvgpr_write_b32 a44, v238
v_accvgpr_write_b32 a45, v239
v_accvgpr_write_b32 a46, v240
v_accvgpr_write_b32 a47, v241
R 238,48
R 239,49
R 240,50
)"
R"(R 241,51
v_mul_f32 v238, v238, v234
v_mul_f32 v239, v239, v234
v_mul_f32 v240, v240, v234
v_mul_f32 v241, v241, v234
v_accvgpr_write_b32 a48, v238
v_accvgpr_write_b32 a49, v239
v_accvgpr_write_b32 a50, v240
v_accvgpr_write_b32 a51, v241
R 238,52
R 239,53
R 240,54
R 241,55
v_mul_f32 v238, v238, v235
v_mul_f32 v239, v239, v235
v_mul_f32 v240, v240, v235
)"
R"(v_mul_f32 v241, v241, v235
v_accvgpr_write_b32 a52, v238
v_accvgpr_write_b32 a53, v239
v_accvgpr_write_b32 a54, v240
v_accvgpr_write_b32 a55, v241
R 238,56
R 239,57
R 240,58
R 241,59
v_mul_f32 v238, v238, v234
v_mul_f32 v239, v239, v234
v_mul_f32 v240, v240, v234
v_mul_f32 v241, v241, v234
v_accvgpr_write_b32 a56, v238
v_accvgpr_write_b32 a57, v239
v_accvgpr_write_b32 a58, v240
)"
R"(v_accvgpr_write_b32 a59, v241
R 238,60
R 239,61
R 240,62
R 241,63
v_mul_f32 v238, v238, v235
v_mul_f32 v239, v239, v235
v_mul_f32 v240, v240, v235
v_mul_f32 v241, v241, v235
v_accvgpr_write_b32 a60, v238
v_accvgpr_write_b32 a61, v239
v_accvgpr_write_b32 a62, v240
v_accvgpr_write_b32 a63, v241
R 238,64
R 239,65
R 240,66
)"
R"(R 241,67
v_mul_f32 v238, v238, v234
v_mul_f32 v239, v239, v234
v_mul_f32 v240, v240, v234
v_mul_f32 v241, v241, v234
v_accvgpr_write_b32 a64, v238
v_accvgpr_write_b32 a65, v239
v_accvgpr_write_b32 a66, v240
v_accvgpr_write_b32 a67, v241
R 238,68
R 239,69
R 240,70
R 241,71
v_mul_f32 v238, v238, v235
v_mul_f32 v239, v239, v235
v_mul_f32 v240, v240, v235
)"
R"(v_mul_f32 v241, v241, v235
v_accvgpr_write_b32 a68, v238
v_accvgpr_write_b32 a69, v239
v_accvgpr_write_b32 a70, v240
v_accvgpr_write_b32 a71, v241
R 238,72
R 239,73
R 240,74
R 241,75
v_mul_f32 v238, v238, v234
v_mul_f32 v239, v239, v234
v_mul_f32 v240, v240, v234
v_mul_f32 v241, v241, v234
v_accvgpr_write_b32 a72, v238
v_accvgpr_write_b32 a73, v239
v_accvgpr_write_b32 a74, v240
)"
R"(v_accvgpr_write_b32 a75, v241
R 238,76
R 239,77
R 240,78
R 241,79
v_mul_f32 v238, v238, v235
v_mul_f32 v239, v239, v235
v_mul_f32 v240, v240, v235
v_mul_f32 v241, v241, v235
v_accvgpr_write_b32 a76, v238
v_accvgpr_write_b32 a77, v239
v_accvgpr_write_b32 a78, v240
v_accvgpr_write_b32 a79, v241
R 238,80
R 239,81
R 240,82
)"
R"(R 241,83
v_mul_f32 v238, v238, v234
v_mul_f32 v239, v239, v234
v_mul_f32 v240, v240, v234
v_mul_f32 v241, v241, v234
v_accvgpr_write_b32 a80, v238
v_accvgpr_write_b32 a81, v239
v_accvgpr_write_b32 a82, v240
v_accvgpr_write_b32 a83, v241
R 238,84
R 239,85
R 240,86
R 241,87
v_mul_f32 v238, v238, v235
v_mul_f32 v239, v239, v235
v_mul_f32 v240, v240, v235
)"
R"(v_mul_f32 v241, v241, v235
v_accvgpr_write_b32 a84, v238
v_accvgpr_write_b32 a85, v239
v_accvgpr_write_b32 a86, v240
v_accvgpr_write_b32 a87, v241
R 238,88
R 239,89
R 240,90
R 241,91
v_mul_f32 v238, v238, v234
v_mul_f32 v239, v239, v234
v_mul_f32 v240, v240, v234
v_mul_f32 v241, v241, v234
v_accvgpr_write_b32 a88, v238
v_accvgpr_write_b32 a89, v239
v_accvgpr_write_b32 a90, v240
)"
R"(v_accvgpr_write_b32 a91, v241
R 238,92
R 239,93
R 240,94
R 241,95
v_mul_f32 v238, v238, v235
v_mul_f32 v239, v239, v235
v_mul_f32 v240, v240, v235
v_mul_f32 v241, v241, v235
v_accvgpr_write_b32 a92, v238
v_accvgpr_write_b32 a93, v239
v_accvgpr_write_b32 a94, v240
v_accvgpr_write_b32 a95, v241
R 238,96
R 239,97
R 240,98
)"
R"(R 241,99
v_mul_f32 v238, v238, v234
v_mul_f32 v239, v239, v234
v_mul_f32 v240, v240, v234
v_mul_f32 v241, v241, v234
v_accvgpr_write_b32 a96, v238
v_accvgpr_write_b32 a97, v239
v_accvgpr_write_b32 a98, v240
v_accvgpr_write_b32 a99, v241
R 238,100
R 239,101
R 240,102
R 241,103
v_mul_f32 v238, v238, v235
v_mul_f32 v239, v239, v235
v_mul_f32 v240, v240, v235
)"
R"(v_mul_f32 v241, v241, v235
v_accvgpr_write_b32 a100, v238
v_accvgpr_write_b32 a101, v239
v_accvgpr_write_b32 a102, v240
v_accvgpr_write_b32 a103, v241
R 238,104
R 239,105
R 240,106
R 241,107
v_mul_f32 v238, v238, v234
v_mul_f32 v239, v239, v234
v_mul_f32 v240, v240, v234
v_mul_f32 v241, v241, v234
v_accvgpr_write_b32 a104, v238
v_accvgpr_write_b32 a105, v239
v_accvgpr_write_b32 a106, v240
)"
R"(v_accvgpr_write_b32 a107, v241
R 238,108
R 239,109
R 240,110
R 241,111
v_mul_f32 v238, v238, v235
v_mul_f32 v239, v239, v235
v_mul_f32 v240, v240, v235
v_mul_f32 v241, v241, v235
v_accvgpr_write_b32 a108, v238
v_accvgpr_write_b32 a109, v239
v_accvgpr_write_b32 a110, v240
v_accvgpr_write_b32 a111, v241
R 238,112
R 239,113
R 240,114
)"
R"(R 241,115
v_mul_f32 v238, v238, v234
v_mul_f32 v239, v239, v234
v_mul_f32 v240, v240, v234
v_mul_f32 v241, v241, v234
v_accvgpr_write_b32 a112, v238
v_accvgpr_write_b32 a113, v239
v_accvgpr_write_b32 a114, v240
v_accvgpr_write_b32 a115, v241
R 238,116
R 239,117
R 240,118
R 241,119
v_mul_f32 v238, v238, v235
v_mul_f32 v239, v239, v235
v_mul_f32 v240, v240, v235
)"
R"(v_mul_f32 v241, v241, v235
v_accvgpr_write_b32 a116, v238
v_accvgpr_write_b32 a117, v239
v_accvgpr_write_b32 a118, v240
v_accvgpr_write_b32 a119, v241
R 238,120
R 239,121
R 240,122
R 241,123
v_mul_f32 v238, v238, v234
v_mul_f32 v239, v239, v234
v_mul_f32 v240, v240, v234
v_mul_f32 v241, v241, v234
v_accvgpr_write_b32 a120, v238
v_accvgpr_write_b32 a121, v239
v_accvgpr_write_b32 a122, v240
)"
R"(v_accvgpr_write_b32 a123, v241
R 238,124
R 239,125
R 240,126
R 241,127
v_mul_f32 v238, v238, v235
v_mul_f32 v239, v239, v235
v_mul_f32 v240, v240, v235
v_mul_f32 v241, v241, v235
v_accvgpr_write_b32 a124, v238
v_accvgpr_write_b32 a125, v239
v_accvgpr_write_b32 a126, v240
v_accvgpr_write_b32 a127, v241
R 238,192
R 239,193
R 240,194
)"
R"(R 241,195
v_mul_f32 v238, v238, v234
v_mul_f32 v239, v239, v234
v_mul_f32 v240, v240, v234
v_mul_f32 v241, v241, v234
v_accvgpr_write_b32 a192, v238
v_accvgpr_write_b32 a193, v239
v_accvgpr_write_b32 a194, v240
v_accvgpr_write_b32 a195, v241
R 238,196
R 239,197
R 240,198
R 241,199
v_mul_f32 v238, v238, v235
v_mul_f32 v239, v239, v235
v_mul_f32 v240, v240, v235
)"
R"(v_mul_f32 v241, v241, v235
v_accvgpr_write_b32 a196, v238
v_accvgpr_write_b32 a197, v239
v_accvgpr_write_b32 a198, v240
v_accvgpr_write_b32 a199, v241
s_nop 3
s_setpc_b64 s[66:67]
.LrareA3_1:
s_call_b64 s[66:67], .LsubA3
s_branch .LbackA3_1
.LrareA3_2:
s_call_b64 s[66:67], .LsubA3
s_branch .LbackA3_2
.LrareA3_3:
s_call_b64 s[66:67], .LsubA3
s_branch .LbackA3_3
)"
R"(.LrareA3_4:
s_call_b64 s[66:67], .LsubA3
s_branch .LbackA3_4
.LrareA3_5:
s_call_b64 s[66:67], .LsubA3
s_branch .LbackA3_5
.LrareA3_6:
s_call_b64 s[66:67], .LsubA3
s_branch .LbackA3_6
.LrareA3_7:
s_call_b64 s[66:67], .LsubA3
s_branch .LbackA3_7
.LsubA3:
v_mov_b32 v238, v217
v_mov_b32 v239, v218
v_mov_b32 v240, v219
)"
R"(v_permlane32_swap_b32 v217, v238
v_permlane32_swap_b32 v218, v239
v_permlane32_swap_b32 v219, v240
v_max_f32 v217, v217, v238
v_max_f32 v218, v218, v239
v_max_f32 v219, v219, v240
v_mov_b32 v238, v217
v_mov_b32 v239, v218
v_mov_b32 v240, v219
v_permlane16_swap_b32 v217, v238
v_permlane16_swap_b32 v218, v239
v_permlane16_swap_b32 v219, v240
v_max_f32 v217, v217, v238
v_max_f32 v218, v218, v239
v_max_f32 v219, v219, v240
v_max_f32 v208, v208, v217
)"
R"(v_max_f32 v209, v209, v218
v_max_f32 v210, v210, v219
v_mul_f32 v217, -s63, v208
v_mul_f32 v218, -s63, v209
v_mul_f32 v219, -s63, v210
v_add_f32 v211, s65, v208
v_add_f32 v212, s65, v209
v_add_f32 v213, s65, v210
v_sub_f32 v234, v217, v214
v_sub_f32 v235, v218, v215
v_sub_f32 v236, v219, v216
v_mov_b32 v214, v217
v_mov_b32 v215, v218
v_mov_b32 v216, v219
E 234
E 235
)"
R"(E 236
s_nop 1
s_setpc_b64 s[66:67]
.LrareB3_1:
s_call_b64 s[66:67], .LsubB3
s_branch .LbackB3_1
.LrareB3_2:
s_call_b64 s[66:67], .LsubB3
s_branch .LbackB3_2
.LrareB3_3:
s_call_b64 s[66:67], .LsubB3
s_branch .LbackB3_3
.LrareB3_4:
s_call_b64 s[66:67], .LsubB3
s_branch .LbackB3_4
.LrareB3_5:
)"
R"(s_call_b64 s[66:67], .LsubB3
s_branch .LbackB3_5
.LrareB3_6:
s_call_b64 s[66:67], .LsubB3
s_branch .LbackB3_6
.LrareB3_7:
s_call_b64 s[66:67], .LsubB3
s_branch .LbackB3_7
.LsubB3:
s_nop 7
R 238,0
R 239,1
R 240,2
R 241,3
v_mul_f32 v238, v238, v234
v_mul_f32 v239, v239, v234
)"
R"(v_mul_f32 v240, v240, v234
v_mul_f32 v241, v241, v234
v_accvgpr_write_b32 a0, v238
v_accvgpr_write_b32 a1, v239
v_accvgpr_write_b32 a2, v240
v_accvgpr_write_b32 a3, v241
R 238,4
R 239,5
R 240,6
R 241,7
v_mul_f32 v238, v238, v235
v_mul_f32 v239, v239, v235
v_mul_f32 v240, v240, v235
v_mul_f32 v241, v241, v235
v_accvgpr_write_b32 a4, v238
v_accvgpr_write_b32 a5, v239
)"
R"(v_accvgpr_write_b32 a6, v240
v_accvgpr_write_b32 a7, v241
R 238,8
R 239,9
R 240,10
R 241,11
v_mul_f32 v238, v238, v236
v_mul_f32 v239, v239, v236
v_mul_f32 v240, v240, v236
v_mul_f32 v241, v241, v236
v_accvgpr_write_b32 a8, v238
v_accvgpr_write_b32 a9, v239
v_accvgpr_write_b32 a10, v240
v_accvgpr_write_b32 a11, v241
R 238,12
R 239,13
)"
R"(R 240,14
R 241,15
v_mul_f32 v238, v238, v234
v_mul_f32 v239, v239, v234
v_mul_f32 v240, v240, v234
v_mul_f32 v241, v241, v234
v_accvgpr_write_b32 a12, v238
v_accvgpr_write_b32 a13, v239
v_accvgpr_write_b32 a14, v240
v_accvgpr_write_b32 a15, v241
R 238,16
R 239,17
R 240,18
R 241,19
v_mul_f32 v238, v238, v235
v_mul_f32 v239, v239, v235
)"
R"(v_mul_f32 v240, v240, v235
v_mul_f32 v241, v241, v235
v_accvgpr_write_b32 a16, v238
v_accvgpr_write_b32 a17, v239
v_accvgpr_write_b32 a18, v240
v_accvgpr_write_b32 a19, v241
R 238,20
R 239,21
R 240,22
R 241,23
v_mul_f32 v238, v238, v236
v_mul_f32 v239, v239, v236
v_mul_f32 v240, v240, v236
v_mul_f32 v241, v241, v236
v_accvgpr_write_b32 a20, v238
v_accvgpr_write_b32 a21, v239
)"
R"(v_accvgpr_write_b32 a22, v240
v_accvgpr_write_b32 a23, v241
R 238,24
R 239,25
R 240,26
R 241,27
v_mul_f32 v238, v238, v234
v_mul_f32 v239, v239, v234
v_mul_f32 v240, v240, v234
v_mul_f32 v241, v241, v234
v_accvgpr_write_b32 a24, v238
v_accvgpr_write_b32 a25, v239
v_accvgpr_write_b32 a26, v240
v_accvgpr_write_b32 a27, v241
R 238,28
R 239,29
)"
R"(R 240,30
R 241,31
v_mul_f32 v238, v238, v235
v_mul_f32 v239, v239, v235
v_mul_f32 v240, v240, v235
v_mul_f32 v241, v241, v235
v_accvgpr_write_b32 a28, v238
v_accvgpr_write_b32 a29, v239
v_accvgpr_write_b32 a30, v240
v_accvgpr_write_b32 a31, v241
R 238,32
R 239,33
R 240,34
R 241,35
v_mul_f32 v238, v238, v236
v_mul_f32 v239, v239, v236
)"
R"(v_mul_f32 v240, v240, v236
v_mul_f32 v241, v241, v236
v_accvgpr_write_b32 a32, v238
v_accvgpr_write_b32 a33, v239
v_accvgpr_write_b32 a34, v240
v_accvgpr_write_b32 a35, v241
R 238,36
R 239,37
R 240,38
R 241,39
v_mul_f32 v238, v238, v234
v_mul_f32 v239, v239, v234
v_mul_f32 v240, v240, v234
v_mul_f32 v241, v241, v234
v_accvgpr_write_b32 a36, v238
v_accvgpr_write_b32 a37, v239
)"
R"(v_accvgpr_write_b32 a38, v240
v_accvgpr_write_b32 a39, v241
R 238,40
R 239,41
R 240,42
R 241,43
v_mul_f32 v238, v238, v235
v_mul_f32 v239, v239, v235
v_mul_f32 v240, v240, v235
v_mul_f32 v241, v241, v235
v_accvgpr_write_b32 a40, v238
v_accvgpr_write_b32 a41, v239
v_accvgpr_write_b32 a42, v240
v_accvgpr_write_b32 a43, v241
R 238,44
R 239,45
)"
R"(R 240,46
R 241,47
v_mul_f32 v238, v238, v236
v_mul_f32 v239, v239, v236
v_mul_f32 v240, v240, v236
v_mul_f32 v241, v241, v236
v_accvgpr_write_b32 a44, v238
v_accvgpr_write_b32 a45, v239
v_accvgpr_write_b32 a46, v240
v_accvgpr_write_b32 a47, v241
R 238,48
R 239,49
R 240,50
R 241,51
v_mul_f32 v238, v238, v234
v_mul_f32 v239, v239, v234
)"
R"(v_mul_f32 v240, v240, v234
v_mul_f32 v241, v241, v234
v_accvgpr_write_b32 a48, v238
v_accvgpr_write_b32 a49, v239
v_accvgpr_write_b32 a50, v240
v_accvgpr_write_b32 a51, v241
R 238,52
R 239,53
R 240,54
R 241,55
v_mul_f32 v238, v238, v235
v_mul_f32 v239, v239, v235
v_mul_f32 v240, v240, v235
v_mul_f32 v241, v241, v235
v_accvgpr_write_b32 a52, v238
v_accvgpr_write_b32 a53, v239
)"
R"(v_accvgpr_write_b32 a54, v240
v_accvgpr_write_b32 a55, v241
R 238,56
R 239,57
R 240,58
R 241,59
v_mul_f32 v238, v238, v236
v_mul_f32 v239, v239, v236
v_mul_f32 v240, v240, v236
v_mul_f32 v241, v241, v236
v_accvgpr_write_b32 a56, v238
v_accvgpr_write_b32 a57, v239
v_accvgpr_write_b32 a58, v240
v_accvgpr_write_b32 a59, v241
R 238,60
R 239,61
)"
R"(R 240,62
R 241,63
v_mul_f32 v238, v238, v234
v_mul_f32 v239, v239, v234
v_mul_f32 v240, v240, v234
v_mul_f32 v241, v241, v234
v_accvgpr_write_b32 a60, v238
v_accvgpr_write_b32 a61, v239
v_accvgpr_write_b32 a62, v240
v_accvgpr_write_b32 a63, v241
R 238,64
R 239,65
R 240,66
R 241,67
v_mul_f32 v238, v238, v235
v_mul_f32 v239, v239, v235
)"
R"(v_mul_f32 v240, v240, v235
v_mul_f32 v241, v241, v235
v_accvgpr_write_b32 a64, v238
v_accvgpr_write_b32 a65, v239
v_accvgpr_write_b32 a66, v240
v_accvgpr_write_b32 a67, v241
R 238,68
R 239,69
R 240,70
R 241,71
v_mul_f32 v238, v238, v236
v_mul_f32 v239, v239, v236
v_mul_f32 v240, v240, v236
v_mul_f32 v241, v241, v236
v_accvgpr_write_b32 a68, v238
v_accvgpr_write_b32 a69, v239
)"
R"(v_accvgpr_write_b32 a70, v240
v_accvgpr_write_b32 a71, v241
R 238,72
R 239,73
R 240,74
R 241,75
v_mul_f32 v238, v238, v234
v_mul_f32 v239, v239, v234
v_mul_f32 v240, v240, v234
v_mul_f32 v241, v241, v234
v_accvgpr_write_b32 a72, v238
v_accvgpr_write_b32 a73, v239
v_accvgpr_write_b32 a74, v240
v_accvgpr_write_b32 a75, v241
R 238,76
R 239,77
)"
R"(R 240,78
R 241,79
v_mul_f32 v238, v238, v235
v_mul_f32 v239, v239, v235
v_mul_f32 v240, v240, v235
v_mul_f32 v241, v241, v235
v_accvgpr_write_b32 a76, v238
v_accvgpr_write_b32 a77, v239
v_accvgpr_write_b32 a78, v240
v_accvgpr_write_b32 a79, v241
R 238,80
R 239,81
R 240,82
R 241,83
v_mul_f32 v238, v238, v236
v_mul_f32 v239, v239, v236
)"
R"(v_mul_f32 v240, v240, v236
v_mul_f32 v241, v241, v236
v_accvgpr_write_b32 a80, v238
v_accvgpr_write_b32 a81, v239
v_accvgpr_write_b32 a82, v240
v_accvgpr_write_b32 a83, v241
R 238,84
R 239,85
R 240,86
R 241,87
v_mul_f32 v238, v238, v234
v_mul_f32 v239, v239, v234
v_mul_f32 v240, v240, v234
v_mul_f32 v241, v241, v234
v_accvgpr_write_b32 a84, v238
v_accvgpr_write_b32 a85, v239
)"
R"(v_accvgpr_write_b32 a86, v240
v_accvgpr_write_b32 a87, v241
R 238,88
R 239,89
R 240,90
R 241,91
v_mul_f32 v238, v238, v235
v_mul_f32 v239, v239, v235
v_mul_f32 v240, v240, v235
v_mul_f32 v241, v241, v235
v_accvgpr_write_b32 a88, v238
v_accvgpr_write_b32 a89, v239
v_accvgpr_write_b32 a90, v240
v_accvgpr_write_b32 a91, v241
R 238,92
R 239,93
)"
R"(R 240,94
R 241,95
v_mul_f32 v238, v238, v236
v_mul_f32 v239, v239, v236
v_mul_f32 v240, v240, v236
v_mul_f32 v241, v241, v236
v_accvgpr_write_b32 a92, v238
v_accvgpr_write_b32 a93, v239
v_accvgpr_write_b32 a94, v240
v_accvgpr_write_b32 a95, v241
R 238,96
R 239,97
R 240,98
R 241,99
v_mul_f32 v238, v238, v234
v_mul_f32 v239, v239, v234
)"
R"(v_mul_f32 v240, v240, v234
v_mul_f32 v241, v241, v234
v_accvgpr_write_b32 a96, v238
v_accvgpr_write_b32 a97, v239
v_accvgpr_write_b32 a98, v240
v_accvgpr_write_b32 a99, v241
R 238,100
R 239,101
R 240,102
R 241,103
v_mul_f32 v238, v238, v235
v_mul_f32 v239, v239, v235
v_mul_f32 v240, v240, v235
v_mul_f32 v241, v241, v235
v_accvgpr_write_b32 a100, v238
v_accvgpr_write_b32 a101, v239
)"
R"(v_accvgpr_write_b32 a102, v240
v_accvgpr_write_b32 a103, v241
R 238,104
R 239,105
R 240,106
R 241,107
v_mul_f32 v238, v238, v236
v_mul_f32 v239, v239, v236
v_mul_f32 v240, v240, v236
v_mul_f32 v241, v241, v236
v_accvgpr_write_b32 a104, v238
v_accvgpr_write_b32 a105, v239
v_accvgpr_write_b32 a106, v240
v_accvgpr_write_b32 a107, v241
R 238,108
R 239,109
)"
R"(R 240,110
R 241,111
v_mul_f32 v238, v238, v234
v_mul_f32 v239, v239, v234
v_mul_f32 v240, v240, v234
v_mul_f32 v241, v241, v234
v_accvgpr_write_b32 a108, v238
v_accvgpr_write_b32 a109, v239
v_accvgpr_write_b32 a110, v240
v_accvgpr_write_b32 a111, v241
R 238,112
R 239,113
R 240,114
R 241,115
v_mul_f32 v238, v238, v235
v_mul_f32 v239, v239, v235
)"
R"(v_mul_f32 v240, v240, v235
v_mul_f32 v241, v241, v235
v_accvgpr_write_b32 a112, v238
v_accvgpr_write_b32 a113, v239
v_accvgpr_write_b32 a114, v240
v_accvgpr_write_b32 a115, v241
R 238,116
R 239,117
R 240,118
R 241,119
v_mul_f32 v238, v238, v236
v_mul_f32 v239, v239, v236
v_mul_f32 v240, v240, v236
v_mul_f32 v241, v241, v236
v_accvgpr_write_b32 a116, v238
v_accvgpr_write_b32 a117, v239
)"
R"(v_accvgpr_write_b32 a118, v240
v_accvgpr_write_b32 a119, v241
R 238,120
R 239,121
R 240,122
R 241,123
v_mul_f32 v238, v238, v234
v_mul_f32 v239, v239, v234
v_mul_f32 v240, v240, v234
v_mul_f32 v241, v241, v234
v_accvgpr_write_b32 a120, v238
v_accvgpr_write_b32 a121, v239
v_accvgpr_write_b32 a122, v240
v_accvgpr_write_b32 a123, v241
R 238,124
R 239,125
)"
R"(R 240,126
R 241,127
v_mul_f32 v238, v238, v235
v_mul_f32 v239, v239, v235
v_mul_f32 v240, v240, v235
v_mul_f32 v241, v241, v235
v_accvgpr_write_b32 a124, v238
v_accvgpr_write_b32 a125, v239
v_accvgpr_write_b32 a126, v240
v_accvgpr_write_b32 a127, v241
R 238,128
R 239,129
R 240,130
R 241,131
v_mul_f32 v238, v238, v236
v_mul_f32 v239, v239, v236
)"
R"(v_mul_f32 v240, v240, v236
v_mul_f32 v241, v241, v236
v_accvgpr_write_b32 a128, v238
v_accvgpr_write_b32 a129, v239
v_accvgpr_write_b32 a130, v240
v_accvgpr_write_b32 a131, v241
R 238,132
R 239,133
R 240,134
R 241,135
v_mul_f32 v238, v238, v234
v_mul_f32 v239, v239, v234
v_mul_f32 v240, v240, v234
v_mul_f32 v241, v241, v234
v_accvgpr_write_b32 a132, v238
v_accvgpr_write_b32 a133, v239
)"
R"(v_accvgpr_write_b32 a134, v240
v_accvgpr_write_b32 a135, v241
R 238,136
R 239,137
R 240,138
R 241,139
v_mul_f32 v238, v238, v235
v_mul_f32 v239, v239, v235
v_mul_f32 v240, v240, v235
v_mul_f32 v241, v241, v235
v_accvgpr_write_b32 a136, v238
v_accvgpr_write_b32 a137, v239
v_accvgpr_write_b32 a138, v240
v_accvgpr_write_b32 a139, v241
R 238,140
R 239,141
)"
R"(R 240,142
R 241,143
v_mul_f32 v238, v238, v236
v_mul_f32 v239, v239, v236
v_mul_f32 v240, v240, v236
v_mul_f32 v241, v241, v236
v_accvgpr_write_b32 a140, v238
v_accvgpr_write_b32 a141, v239
v_accvgpr_write_b32 a142, v240
v_accvgpr_write_b32 a143, v241
R 238,144
R 239,145
R 240,146
R 241,147
v_mul_f32 v238, v238, v234
v_mul_f32 v239, v239, v234
)"
R"(v_mul_f32 v240, v240, v234
v_mul_f32 v241, v241, v234
v_accvgpr_write_b32 a144, v238
v_accvgpr_write_b32 a145, v239
v_accvgpr_write_b32 a146, v240
v_accvgpr_write_b32 a147, v241
R 238,148
R 239,149
R 240,150
R 241,151
v_mul_f32 v238, v238, v235
v_mul_f32 v239, v239, v235
v_mul_f32 v240, v240, v235
v_mul_f32 v241, v241, v235
v_accvgpr_write_b32 a148, v238
v_accvgpr_write_b32 a149, v239
)"
R"(v_accvgpr_write_b32 a150, v240
v_accvgpr_write_b32 a151, v241
R 238,152
R 239,153
R 240,154
R 241,155
v_mul_f32 v238, v238, v236
v_mul_f32 v239, v239, v236
v_mul_f32 v240, v240, v236
v_mul_f32 v241, v241, v236
v_accvgpr_write_b32 a152, v238
v_accvgpr_write_b32 a153, v239
v_accvgpr_write_b32 a154, v240
v_accvgpr_write_b32 a155, v241
R 238,156
R 239,157
)"
R"(R 240,158
R 241,159
v_mul_f32 v238, v238, v234
v_mul_f32 v239, v239, v234
v_mul_f32 v240, v240, v234
v_mul_f32 v241, v241, v234
v_accvgpr_write_b32 a156, v238
v_accvgpr_write_b32 a157, v239
v_accvgpr_write_b32 a158, v240
v_accvgpr_write_b32 a159, v241
R 238,160
R 239,161
R 240,162
R 241,163
v_mul_f32 v238, v238, v235
v_mul_f32 v239, v239, v235
)"
R"(v_mul_f32 v240, v240, v235
v_mul_f32 v241, v241, v235
v_accvgpr_write_b32 a160, v238
v_accvgpr_write_b32 a161, v239
v_accvgpr_write_b32 a162, v240
v_accvgpr_write_b32 a163, v241
R 238,164
R 239,165
R 240,166
R 241,167
v_mul_f32 v238, v238, v236
v_mul_f32 v239, v239, v236
v_mul_f32 v240, v240, v236
v_mul_f32 v241, v241, v236
v_accvgpr_write_b32 a164, v238
v_accvgpr_write_b32 a165, v239
)"
R"(v_accvgpr_write_b32 a166, v240
v_accvgpr_write_b32 a167, v241
R 238,168
R 239,169
R 240,170
R 241,171
v_mul_f32 v238, v238, v234
v_mul_f32 v239, v239, v234
v_mul_f32 v240, v240, v234
v_mul_f32 v241, v241, v234
v_accvgpr_write_b32 a168, v238
v_accvgpr_write_b32 a169, v239
v_accvgpr_write_b32 a170, v240
v_accvgpr_write_b32 a171, v241
R 238,172
R 239,173
)"
R"(R 240,174
R 241,175
v_mul_f32 v238, v238, v235
v_mul_f32 v239, v239, v235
v_mul_f32 v240, v240, v235
v_mul_f32 v241, v241, v235
v_accvgpr_write_b32 a172, v238
v_accvgpr_write_b32 a173, v239
v_accvgpr_write_b32 a174, v240
v_accvgpr_write_b32 a175, v241
R 238,176
R 239,177
R 240,178
R 241,179
v_mul_f32 v238, v238, v236
v_mul_f32 v239, v239, v236
)"
R"(v_mul_f32 v240, v240, v236
v_mul_f32 v241, v241, v236
v_accvgpr_write_b32 a176, v238
v_accvgpr_write_b32 a177, v239
v_accvgpr_write_b32 a178, v240
v_accvgpr_write_b32 a179, v241
R 238,180
R 239,181
R 240,182
R 241,183
v_mul_f32 v238, v238, v234
v_mul_f32 v239, v239, v234
v_mul_f32 v240, v240, v234
v_mul_f32 v241, v241, v234
v_accvgpr_write_b32 a180, v238
v_accvgpr_write_b32 a181, v239
)"
R"(v_accvgpr_write_b32 a182, v240
v_accvgpr_write_b32 a183, v241
R 238,184
R 239,185
R 240,186
R 241,187
v_mul_f32 v238, v238, v235
v_mul_f32 v239, v239, v235
v_mul_f32 v240, v240, v235
v_mul_f32 v241, v241, v235
v_accvgpr_write_b32 a184, v238
v_accvgpr_write_b32 a185, v239
v_accvgpr_write_b32 a186, v240
v_accvgpr_write_b32 a187, v241
R 238,188
R 239,189
)"
R"(R 240,190
R 241,191
v_mul_f32 v238, v238, v236
v_mul_f32 v239, v239, v236
v_mul_f32 v240, v240, v236
v_mul_f32 v241, v241, v236
v_accvgpr_write_b32 a188, v238
v_accvgpr_write_b32 a189, v239
v_accvgpr_write_b32 a190, v240
v_accvgpr_write_b32 a191, v241
R 238,192
R 239,193
R 240,194
R 241,195
v_mul_f32 v238, v238, v234
v_mul_f32 v239, v239, v234
)"
R"(v_mul_f32 v240, v240, v234
v_mul_f32 v241, v241, v234
v_accvgpr_write_b32 a192, v238
v_accvgpr_write_b32 a193, v239
v_accvgpr_write_b32 a194, v240
v_accvgpr_write_b32 a195, v241
R 238,196
R 239,197
R 240,198
R 241,199
v_mul_f32 v238, v238, v235
v_mul_f32 v239, v239, v235
v_mul_f32 v240, v240, v235
v_mul_f32 v241, v241, v235
v_accvgpr_write_b32 a196, v238
v_accvgpr_write_b32 a197, v239
)"
R"(v_accvgpr_write_b32 a198, v240
v_accvgpr_write_b32 a199, v241
R 238,200
R 239,201
R 240,202
R 241,203
v_mul_f32 v238, v238, v236
v_mul_f32 v239, v239, v236
v_mul_f32 v240, v240, v236
v_mul_f32 v241, v241, v236
v_accvgpr_write_b32 a200, v238
v_accvgpr_write_b32 a201, v239
v_accvgpr_write_b32 a202, v240
v_accvgpr_write_b32 a203, v241
s_nop 3
s_setpc_b64 s[66:67]
)"
R"(.Ldone:
.Lend:
)"
    :: "s"(__builtin_amdgcn_kernarg_segment_ptr()), "s"(blockIdx.x), "{v255}"(threadIdx.x), "s"((unsigned)(size_t)lds)
    : "memory", "vcc", "scc", "v0","v1","v2","v3","v4","v5","v6","v7","v8","v9","v10","v11","v12","v13","v14","v15","v16","v17","v18","v19","v20","v21","v22","v23","v24","v25","v26","v27","v28","v29","v30","v31",
      "v32","v33","v34","v35","v36","v37","v38","v39","v40","v41","v42","v43","v44","v45","v46","v47","v48","v49","v50","v51","v52","v53","v54","v55","v56","v57","v58","v59","v60","v61","v62","v63",
      "v64","v65","v66","v67","v68","v69","v70","v71","v72","v73","v74","v75","v76","v77","v78","v79","v80","v81","v82","v83","v84","v85","v86","v87","v88","v89","v90","v91","v92","v93","v94","v95",
      "v96","v97","v98","v99","v100","v101","v102","v103","v104","v105","v106","v107","v108","v109","v110","v111","v112","v113","v114","v115","v116","v117","v118","v119","v120","v121","v122","v123","v124","v125","v126","v127",
      "v128","v129","v130","v131","v132","v133","v134","v135","v136","v137","v138","v139","v140","v141","v142","v143","v144","v145","v146","v147","v148","v149","v150","v151","v152","v153","v154","v155","v156","v157","v158","v159",
      "v160","v161","v162","v163","v164","v165","v166","v167","v168","v169","v170","v171","v172","v173","v174","v175","v176","v177","v178","v179","v180","v181","v182","v183","v184","v185","v186","v187","v188","v189","v190","v191",
      "v192","v193","v194","v195","v196","v197","v198","v199","v200","v201","v202","v203","v204","v205","v206","v207","v208","v209","v210","v211","v212","v213","v214","v215","v216","v217","v218","v219","v220","v221","v222","v223",
      "v224","v225","v226","v227","v228","v229","v230","v231","v232","v233","v234","v235","v236","v237","v238","v239","v240","v241","v242","v243","v244","v245","v246","v247","v248","v249","v250","v251","v252","v253","v254","a0",
      "a1","a2","a3","a4","a5","a6","a7","a8","a9","a10","a11","a12","a13","a14","a15","a16","a17","a18","a19","a20","a21","a22","a23","a24","a25","a26","a27","a28","a29","a30","a31","a32",
      "a33","a34","a35","a36","a37","a38","a39","a40","a41","a42","a43","a44","a45","a46","a47","a48","a49","a50","a51","a52","a53","a54","a55","a56","a57","a58","a59","a60","a61","a62","a63","a64",
      "a65","a66","a67","a68","a69","a70","a71","a72","a73","a74","a75","a76","a77","a78","a79","a80","a81","a82","a83","a84","a85","a86","a87","a88","a89","a90","a91","a92","a93","a94","a95","a96",
      "a97","a98","a99","a100","a101","a102","a103","a104","a105","a106","a107","a108","a109","a110","a111","a112","a113","a114","a115","a116","a117","a118","a119","a120","a121","a122","a123","a124","a125","a126","a127","a128",
      "a129","a130","a131","a132","a133","a134","a135","a136","a137","a138","a139","a140","a141","a142","a143","a144","a145","a146","a147","a148","a149","a150","a151","a152","a153","a154","a155","a156","a157","a158","a159","a160",
      "a161","a162","a163","a164","a165","a166","a167","a168","a169","a170","a171","a172","a173","a174","a175","a176","a177","a178","a179","a180","a181","a182","a183","a184","a185","a186","a187","a188","a189","a190","a191","a192",
      "a193","a194","a195","a196","a197","a198","a199","a200","a201","a202","a203","a204","a205","a206","a207","a208","a209","a210","a211","a212","a213","a214","a215","a216","a217","a218","a219","a220","a221","a222","a223","a224",
      "a225","a226","a227","a228","a229","a230","a231","a232","a233","a234","a235","a236","a237","a238","a239","a240","a241","a242","a243","a244","a245","a246","a247","a248","a249","a250","a251","a252","a253","a254","a255","s4",
      "s5","s6","s7","s8","s9","s10","s11","s12","s13","s14","s15","s16","s17","s18","s19","s20","s21","s22","s23","s24","s25","s26","s27","s28","s29","s30","s31","s34","s35","s36","s37","s38",
      "s39","s40","s41","s42","s43","s44","s45","s46","s47","s48","s49","s50","s51","s52","s53","s54","s55","s56","s57","s58","s59","s60","s61","s62","s63","s64","s65","s66","s67","s68","s69","s70",
      "s71","s72","s73","s74","s75","s76","s77","s78","s79","s80","s81","s82","s83","s84","s85","s86","s87","s88","s89","s90","s91","s92","s93","s94");
}

extern "C" void kernel_launch(void* const* d_in, const int* in_sizes, int n_in, void* d_out, int out_size, void* d_ws, size_t ws_size,
                              hipStream_t stream) {
  mha_main<<<256, 256, 0, stream>>>(d_in[0], d_in[1], d_in[2], d_in[3], d_in[4], d_ws, d_out);
}
```
